```python
import torch

def _softmax(x, axis=-1):
    return torch.softmax(x, dim=axis)

def _gelu(x):
    return torch.nn.functional.gelu(x, approximate='tanh')

def _silu(x):
    return torch.nn.functional.silu(x)

def _sigmoid(x):
    return torch.sigmoid(x)

def _layernorm(x, axis=-1, eps=1e-05):
    m = x.mean(dim=axis, keepdim=True)
    v = x.var(dim=axis, keepdim=True, unbiased=False)
    return (x - m) / torch.sqrt(v + eps)

def _scatter_add(tgt, idx, src, axis=0):
    return tgt.clone().index_add_(axis, idx, src)

def _segment_sum(src, seg, n):
    out = torch.zeros((n,) + src.shape[1:], dtype=src.dtype)
    return out.index_add_(0, seg, src)

def _pad(x, pw):
    flat = tuple((v for pair in reversed(pw) for v in pair))
    return torch.nn.functional.pad(x, flat)
kernel_name = 'gated_mix__memory_aug_recurrent_mem_tr__moe_arch_mod__prompt'

def setup_inputs():

    _w = lambda a: (lambda t: t.to(torch.bfloat16) if t.is_floating_point() else t)(torch.as_tensor(a))

    def _A_setup():
        B, S, Dm = (1, 2048, 1024)
        H, Dh = (32, 128)
        M, seg = (8, 32)
        seg = min(seg, S)
        S = S // seg * seg
        return {'x': _w((lambda *s: torch.randn(*s, dtype=torch.bfloat16) * 0.05)(B, S, Dm)), 'mem0': _w((lambda *s: torch.randn(*s, dtype=torch.bfloat16) * 0.8)(B, M, Dm)), 'Wq': _w((lambda *s: torch.randn(*s, dtype=torch.bfloat16) * 3.2)(Dm, H, Dh) * Dm ** (-0.5)), 'Wk': _w((lambda *s: torch.randn(*s, dtype=torch.bfloat16) * 3.2)(Dm, H, Dh) * Dm ** (-0.5)), 'Wv': _w((lambda *s: torch.randn(*s, dtype=torch.bfloat16) * 0.8)(Dm, H, Dh) * Dm ** (-0.5)), 'Wo': _w((lambda *s: torch.randn(*s, dtype=torch.bfloat16) * 0.8)(H, Dh, Dm) * (H * Dh) ** (-0.5)), 'seg_len': seg}
    _A_in = _A_setup()

    def _B_setup():
        B, S, D, F, C = (1, 2048, 1024, 3584, 2)
        return {'x': _w((lambda *s: torch.randn(*s, dtype=torch.bfloat16) * 0.05)(B, S, D)), 'w_r': _w((lambda *s: torch.randn(*s, dtype=torch.bfloat16) * 1.6)(D)), 'w_gate': _w((lambda *s: torch.randn(*s, dtype=torch.bfloat16) * 0.2)(D, F)), 'w_up': _w((lambda *s: torch.randn(*s, dtype=torch.bfloat16) * 0.2)(D, F)), 'w_down': _w((lambda *s: torch.randn(*s, dtype=torch.bfloat16) * 0.2)(F, D))}
    _B_in = _B_setup()
    return {'A_x': _A_in['x'], 'A_mem0': _A_in['mem0'], 'A_Wq': _A_in['Wq'], 'A_Wk': _A_in['Wk'], 'A_Wv': _A_in['Wv'], 'A_Wo': _A_in['Wo'], 'A_seg_len': _A_in['seg_len'], 'B_x': _B_in['x'], 'B_w_r': _B_in['w_r'], 'B_w_gate': _B_in['w_gate'], 'B_w_up': _B_in['w_up'], 'B_w_down': _B_in['w_down'], 'gate_w': _w((lambda *s: torch.randn(*s, dtype=torch.bfloat16) * 0.05)(1024) * 16.0)}

def reference(A_x, A_mem0, A_Wq, A_Wk, A_Wv, A_Wo, A_seg_len, B_x, B_w_r, B_w_gate, B_w_up, B_w_down, gate_w):

    def _A_ref(x, mem0, Wq, Wk, Wv, Wo, seg_len):
        B, S, Dm = x.shape
        H, Dh = (Wq.shape[1], Wq.shape[2])
        M = mem0.shape[1]
        scale = Dh ** (-0.5)
        n_seg = S // seg_len
        L = M + seg_len
        idx = torch.arange(L)
        mask = (idx[:, None] < M) | (idx[:, None] >= idx[None, :])
        mem = mem0
        outs = []
        for s in range(n_seg):
            xs = x[:, s * seg_len:(s + 1) * seg_len, :].clone()
            h = torch.concatenate([mem, xs], axis=1)
            q = torch.einsum('btd,dhe->bhte', h, Wq)
            k = torch.einsum('btd,dhe->bhte', h, Wk)
            v = torch.einsum('btd,dhe->bhte', h, Wv)
            scores = torch.einsum('bhqe,bhke->bhqk', q, k) * scale
            scores = torch.where(mask[None, None], scores, -1000000000.0)
            p = _softmax(scores, axis=-1)
            o = torch.einsum('bhqk,bhke->bhqe', p, v)
            o = torch.einsum('bhte,hed->btd', o, Wo)
            mem = o[:, :M, :].clone()
            outs.append(o[:, M:, :])
        return torch.concatenate(outs, axis=1)

    def _B_ref(x, w_r, w_gate, w_up, w_down):
        B, S, D = x.shape
        C = 2
        r = x @ w_r
        idx = torch.argsort(-r, axis=-1)[:, :C]
        idx_e = (lambda x, n, axis: torch.repeat_interleave(x, n, dim=axis))(idx.reshape(B, C, 1), D, axis=2)
        x_sel = (lambda x, i, axis: torch.take_along_dim(x, i, dim=axis))(x, idx_e, axis=1)
        g = _silu(x_sel @ w_gate)
        u = x_sel @ w_up
        y = g * u @ w_down
        r_sel = _sigmoid((lambda x, i, axis: torch.take_along_dim(x, i, dim=axis))(r, idx, axis=1))
        y = y * r_sel.reshape(B, C, 1)
        b_off = (torch.arange(B) * S).reshape(B, 1)
        flat_idx = (idx + b_off).reshape(B * C)
        zero = (x * 0.0).reshape(B * S, D)
        upd = _scatter_add(zero, flat_idx, y.reshape(B * C, D), axis=0)
        return x + upd.reshape(B, S, D)
    x = A_x
    g = _sigmoid((x * gate_w).sum(-1, keepdims=True))
    a = _A_ref(x, A_mem0, A_Wq, A_Wk, A_Wv, A_Wo, A_seg_len)
    b = _B_ref(x, B_w_r, B_w_gate, B_w_up, B_w_down)
    return g * a + (1 - g) * b
```

```cpp
#include <hip/hip_runtime.h>
#include <hip/hip_bf16.h>
#include <stdint.h>

typedef unsigned short u16;
typedef unsigned v4u __attribute__((ext_vector_type(4)));
typedef unsigned v2u __attribute__((ext_vector_type(2)));
typedef unsigned v8u __attribute__((ext_vector_type(8)));
typedef float f32x4 __attribute__((ext_vector_type(4)));
typedef __bf16 bf16x2 __attribute__((ext_vector_type(2)));
typedef __bf16 bf16x8 __attribute__((ext_vector_type(8)));
typedef __bf16 bf16x16 __attribute__((ext_vector_type(16)));
typedef short s16x4 __attribute__((ext_vector_type(4)));
#define GAS __attribute__((address_space(1)))
#define LAS __attribute__((address_space(3)))
#define RLX_AGENT __ATOMIC_RELAXED, __HIP_MEMORY_SCOPE_AGENT
#define DEVI __device__ __forceinline__
#define BAR() asm volatile("s_waitcnt lgkmcnt(0)\n\ts_barrier" ::: "memory")

constexpr int S_ = 2048, D_ = 1024, H_ = 32, DH = 128, NSEG = 64, F_ = 3584;
constexpr float SCALE = 0.08838834764831845f, LOG2E = 1.4426950408889634f;
constexpr unsigned SENT = 0xFFFFFFFFu;
constexpr unsigned PAGE = 2u << 20;
constexpr unsigned O_CTL = 0, O_R = 65536, O_G = O_R + 8192, O_FH = 131072, O_FY = O_FH + 32768;
constexpr unsigned P_HAND = 1, P_PART = 17;
constexpr unsigned O_KX = 21 * PAGE, O_VX = 29 * PAGE, O_OX = 37 * PAGE, O_DBG = 45 * PAGE;
constexpr unsigned L_QKV = 16384, L_LEAD = 16384 + 32 * 6144;
constexpr int C_ABORT = 0, C_ARRIVE = 64, C_T0 = 128, C_GRANK = 192, C_XR = 256  , C_SLOT = 1024  , C_SIDE = 2048  ;

struct Params { const u16 *x, *mem0, *wq, *wk, *wv, *wo, *wr, *wg, *wu, *wd, *gw; u16* out; unsigned char* ws; };
struct Tim { int ta, tv, tb, tc, dmin, dbig, back, t0; };

DEVI float bf2f(u16 b) { return __uint_as_float((unsigned)b << 16); }
DEVI unsigned pk2(float lo, float hi) { bf16x2 v = {(__bf16)lo, (__bf16)hi}; return __builtin_bit_cast(unsigned, v); }
DEVI float rnd(float f) { return __uint_as_float(pk2(f, 0.f) << 16); }
DEVI float lo16(unsigned u) { return __uint_as_float(u << 16); }
DEVI float hi16(unsigned u) { return __uint_as_float(u & 0xffff0000u); }
DEVI unsigned xcc_id() { return (unsigned)__builtin_amdgcn_s_getreg(20 | (3 << 11)) & 0xfu; }
DEVI unsigned rt() { return (unsigned)__builtin_amdgcn_s_memrealtime(); }
DEVI unsigned amap(unsigned lo) { return ((lo >> 13) << 14) + (lo & 8191); }
DEVI v4u ld16(__amdgpu_buffer_rsrc_t r, unsigned vo, unsigned so) { return __builtin_amdgcn_raw_buffer_load_b128(r, vo, so, 16); }
DEVI void st16(v4u v, __amdgpu_buffer_rsrc_t r, unsigned vo, unsigned so) { __builtin_amdgcn_raw_buffer_store_b128(v, r, vo, so, 16); }
DEVI void st8(v2u v, __amdgpu_buffer_rsrc_t r, unsigned vo, unsigned so) { __builtin_amdgcn_raw_buffer_store_b64(v, r, vo, so, 16); }
DEVI void st4(unsigned v, __amdgpu_buffer_rsrc_t r, unsigned vo, unsigned so) { __builtin_amdgcn_raw_buffer_store_b32(v, r, vo, so, 16); }
DEVI unsigned umax3(unsigned a, unsigned b, unsigned c) { unsigned r; asm("v_max3_u32 %0, %1, %2, %3" : "=v"(r) : "v"(a), "v"(b), "v"(c)); return r; }
DEVI unsigned mx4(v4u v, unsigned m) { return umax3(umax3(v.x, v.y, m), v.z, v.w); }
DEVI bool any_sent(unsigned m) { return __builtin_amdgcn_ballot_w64(m == SENT) != 0; }
DEVI unsigned nosent(unsigned u) { return u < 0xFFFFFFFEu ? u : 0xFFFFFFFEu; }
DEVI f32x4 mfma32(v4u a, v4u b, f32x4 c) { return __builtin_amdgcn_mfma_f32_16x16x32_bf16(__builtin_bit_cast(bf16x8, a), __builtin_bit_cast(bf16x8, b), c, 0, 0, 0); }
DEVI f32x4 mfma16(v2u a, v2u b, f32x4 c) { return __builtin_amdgcn_mfma_f32_16x16x16bf16_1k(__builtin_bit_cast(s16x4, a), __builtin_bit_cast(s16x4, b), c, 0, 0, 0); }
DEVI f32x4 smf(v4u a, v8u b, f32x4 c, int idx) { return __builtin_amdgcn_smfmac_f32_16x16x64_bf16(__builtin_bit_cast(bf16x8, a), __builtin_bit_cast(bf16x16, b), c, idx, 0, 0); }
DEVI v2u trrd(LAS unsigned char* p) { return __builtin_bit_cast(v2u, __builtin_amdgcn_ds_read_tr16_b64_v4i16((LAS s16x4*)p)); }
DEVI float ror8(float v) { return __int_as_float(__builtin_amdgcn_update_dpp(0, __float_as_int(v), 0x128, 0xf, 0xf, false)); }
DEVI float xor1(float v) { return __int_as_float(__builtin_amdgcn_update_dpp(0, __float_as_int(v), 0xB1, 0xf, 0xf, false)); }
DEVI float sw32sum(float a, float b) { auto r = __builtin_amdgcn_permlane32_swap(__float_as_uint(a), __float_as_uint(b), false, false); return __uint_as_float(r[0]) + __uint_as_float(r[1]); }
DEVI float sw16sum(float a, float b) { auto r = __builtin_amdgcn_permlane16_swap(__float_as_uint(a), __float_as_uint(b), false, false); return __uint_as_float(r[0]) + __uint_as_float(r[1]); }
DEVI float sw32max(float a) { auto r = __builtin_amdgcn_permlane32_swap(__float_as_uint(a), __float_as_uint(a), false, false); return fmaxf(__uint_as_float(r[0]), __uint_as_float(r[1])); }
DEVI float sw16max(float a) { auto r = __builtin_amdgcn_permlane16_swap(__float_as_uint(a), __float_as_uint(a), false, false); return fmaxf(__uint_as_float(r[0]), __uint_as_float(r[1])); }
DEVI f32x4 fold(f32x4 a, f32x4 b) { f32x4 r; r.x = sw32sum(a.x, b.x); r.y = sw32sum(a.y, b.y); r.z = sw32sum(a.z, b.z); r.w = sw32sum(a.w, b.w); return r; }
DEVI void sleep_cyc(int n) { for (; n >= 64; n -= 80) __builtin_amdgcn_s_sleep(1); }
DEVI void wait_tick(unsigned now, unsigned t) {
    const int rem = (int)(t - now);
    if (rem <= 0) return;
    sleep_cyc(rem * 23 - 50);
    while ((int)(rt() - t) < 0) { }
}
constexpr unsigned SPINCAP = 60000;
DEVI bool poll_fail(unsigned& spins, GAS unsigned* ctl, int back) {
    sleep_cyc(back);
    ++spins;
    if ((spins & 63u) == 0u) {
        if (spins > SPINCAP) { __hip_atomic_store(ctl + C_ABORT, 1u, RLX_AGENT); return true; }
        if (__builtin_amdgcn_readfirstlane(__hip_atomic_load(ctl + C_ABORT, RLX_AGENT))) return true;
    }
    return false;
}

__global__ void __launch_bounds__(256) k_prep(Params p) {
    const int tid = threadIdx.x, lane = tid & 63, wave = tid >> 6, b = blockIdx.x;
    unsigned* ctl = (unsigned*)(p.ws + O_CTL);
    if (b == 255) { for (int k = tid; k < C_SIDE; k += 256) ctl[k] = 0u; }
    if (b < 160 && tid == 192) {
        const unsigned page = 1 + (b >> 3);
        __amdgpu_buffer_rsrc_t rs = __builtin_amdgcn_make_buffer_rsrc(p.ws, 0, 0xFFFFFF00u, 0x00020000);
        unsigned t[2] = {0, 0};
        for (int rep = 0; rep < 3; ++rep)
            for (int side = 0; side < 2; ++side) {
                const unsigned long long c0 = __builtin_amdgcn_s_memtime();
                asm volatile("s_waitcnt lgkmcnt(0)" ::: "memory");
                st4(SENT, rs, page * PAGE + 480 * 1024 + side * 8192 + (b & 7) * 512 + rep * 128, 0);
                asm volatile("s_waitcnt vmcnt(0)" ::: "memory");
                const unsigned long long c1 = __builtin_amdgcn_s_memtime();
                if (rep) t[side] += (unsigned)(c1 - c0);
            }
        ctl[C_SIDE + page] = (t[1] < t[0] ? 1u : 0u) ^ (xcc_id() >= 4 ? 1u : 0u);
    }
    float* R = (float*)(p.ws + O_R); float* G = (float*)(p.ws + O_G);
    for (int rr = 0; rr < 2; ++rr) {
        const int row = b * 8 + wave * 2 + rr;
        float a = 0.f, g = 0.f;
#pragma unroll
        for (int q = 0; q < 2; ++q) {
            const int k = q * 512 + lane * 8;
            const v4u xv = *(const v4u*)(p.x + row * D_ + k), wr = *(const v4u*)(p.wr + k), gw = *(const v4u*)(p.gw + k);
#pragma unroll
            for (int e = 0; e < 4; ++e) {
                a = fmaf(lo16(xv[e]), lo16(wr[e]), a); a = fmaf(hi16(xv[e]), hi16(wr[e]), a);
                const unsigned pr = pk2(lo16(xv[e]) * lo16(gw[e]), hi16(xv[e]) * hi16(gw[e]));
                g += lo16(pr); g += hi16(pr);
            }
        }
#pragma unroll
        for (int m = 32; m >= 1; m >>= 1) { a += __shfl_xor(a, m); g += __shfl_xor(g, m); }
        if (lane == 0) { R[row] = rnd(a); g = rnd(g); G[row] = rnd(1.f / (1.f + __expf(-g))); }
    }
}

constexpr unsigned LC_PA = 0, LC_QK = 24576, LC_V = LC_QK + 4096, LC_O = LC_V + 2048, LC_MISC = LC_O + 2560, LC_KV = 33792  , LC_END = LC_KV + 32768;

DEVI void chain_body(const Params& p, const Tim& tm, LAS unsigned char* lds, const int g, const int i) {
    const int tid = threadIdx.x, lane = tid & 63, wave = __builtin_amdgcn_readfirstlane(tid >> 6);
    const int h = g * 8 + (i >> 2), j = i & 3, cid = h * 4 + j;
    GAS unsigned* ctl = (GAS unsigned*)(p.ws + O_CTL);
    const __amdgpu_buffer_rsrc_t rs = __builtin_amdgcn_make_buffer_rsrc(p.ws, 0, 0xFFFFFF00u, 0x00020000);
    unsigned sidemask = 0;
    { const unsigned sv = lane < 21 ? (ctl[C_SIDE + lane] & 1u) : 0u; sidemask = (unsigned)__builtin_amdgcn_ballot_w64(sv != 0); }
    v8u WA[6][2], WO[2][2];
    {
        v4u sa[2][12], so[8];
        const u16* wm[3] = {p.wq, p.wk, p.wv};
#pragma unroll
        for (int ks = 0; ks < 2; ++ks)
#pragma unroll
            for (int m = 0; m < 3; ++m)
#pragma unroll
                for (int q = 0; q < 4; ++q)
                    sa[ks][m * 4 + q] = *(const v4u*)(wm[m] + (size_t)(128 * wave + 64 * ks + 16 * q + (lane >> 2)) * 4096 + h * 128 + 32 * j + (lane & 3) * 8);
        LAS unsigned char* img = lds + wave * 12288;
        const int gB = lane >> 4, q4 = (lane & 15) >> 2, p4 = lane & 3;
        const unsigned rbase = (16 * (gB >> 1) + 4 * (gB & 1) + (q4 & 1) + 8 * (q4 >> 1)) * 64 + p4 * 8;
#pragma unroll
        for (int ks = 0; ks < 2; ++ks) {
#pragma unroll
            for (int q = 0; q < 12; ++q) *(LAS v4u*)(img + q * 1024 + lane * 16) = sa[ks][q];
            if (ks == 0) {
#pragma unroll
        for (int q = 0; q < 8; ++q) so[q] = *(const v4u*)(p.wo + (size_t)(h * 128 + 16 * q + (lane >> 2)) * 1024 + 256 * j + 32 * wave + (lane & 3) * 8);
            }
            asm volatile("s_waitcnt lgkmcnt(0)" ::: "memory");
#pragma unroll
            for (int t = 0; t < 6; ++t)
#pragma unroll
                for (int m = 0; m < 4; ++m) {
                    const v2u r = trrd(img + (t >> 1) * 4096 + (t & 1) * 32 + (32 * (m >> 1) + 2 * (m & 1)) * 64 + rbase);
                    WA[t][ks][2 * m] = r.x; WA[t][ks][2 * m + 1] = r.y;
                }
            asm volatile("s_waitcnt lgkmcnt(0)" ::: "memory");
        }
#pragma unroll
        for (int q = 0; q < 8; ++q) *(LAS v4u*)(img + q * 1024 + lane * 16) = so[q];
        asm volatile("s_waitcnt lgkmcnt(0)" ::: "memory");
#pragma unroll
        for (int t = 0; t < 2; ++t)
#pragma unroll
            for (int ks = 0; ks < 2; ++ks)
#pragma unroll
                for (int m = 0; m < 4; ++m) {
                    const v2u r = trrd(img + t * 32 + (64 * ks + 32 * (m >> 1) + 2 * (m & 1)) * 64 + rbase);
                    WO[t][ks][2 * m] = r.x; WO[t][ks][2 * m + 1] = r.y;
                }
    }
    const int tau = lane >> 5, rh = (lane >> 4) & 1, n16 = lane & 15, c8 = (lane >> 3) & 1, g4 = lane >> 4;
    const unsigned o_gm = amap(wave * 2048 + lane * 16);
    const unsigned o_lead = amap(L_LEAD + (lane & 3) * 128);
    const unsigned qkvb = L_QKV + h * 6144;
    const unsigned o_pub = wave < 2 ? amap(qkvb + j * 1024 + (16 * wave + n16) * 32 + rh * 16 + tau * 8) : amap(qkvb + 4096 + j * 512 + (lane & 31) * 16 + tau * 8);
    const unsigned l_red = LC_PA + (tau * 96 + (wave < 2 ? rh * 32 + 16 * wave + n16 : 64 + (lane & 31))) * 16;
    const unsigned o_gq = amap(qkvb + (wave < 4 ? wave * 1024 : 4096 + (wave & 1) * 1024) + lane * 16);
    const int pcol = 256 * j + 32 * wave + 16 * tau + n16;
    const unsigned o_pp = amap((pcol >> 3) * 8192 + h * 256 + (rh * 8 + (pcol & 7)) * 16);
    const unsigned o_pp2 = o_pp - 16 * 16384;
    const unsigned o_gp = amap(cid * 8192 + lane * 16);
    const int crow = 4 * ((lane >> 3) & 1) + (((g4 & 1) << 1) | (g4 >> 1));
    const unsigned o_pm = amap(cid * 128 + crow * 16 + ((lane & 7) >> 1) * 4);
    const int sidx = c8 ? 0xEEEE : 0x4444;
    {
        const v4u zz = {SENT, SENT, SENT, SENT};
        for (int s = wave; s < NSEG; s += 8) {
            const unsigned pg = P_HAND + (s >> 2), sb = pg * PAGE + (s & 3) * 524288u, sx = ((sidemask >> pg) & 1u) << 13;
            st16(zz, rs, amap(qkvb + j * 1024 + lane * 16) ^ sx, sb);
            if (lane < 32) st16(zz, rs, amap(qkvb + 4096 + j * 512 + lane * 16) ^ sx, sb);
            else if (lane < 40) st16(zz, rs, amap(cid * 128 + (lane - 32) * 16) ^ sx, sb);
            else if (lane == 40 && i == 0) st16(zz, rs, amap(L_LEAD + g * 128) ^ sx, sb);
        }
#pragma unroll
        for (int sl = 0; sl < 4; ++sl) st16(zz, rs, o_pp ^ (((sidemask >> (P_PART + sl)) & 1u) << 13), (P_PART + sl) * PAGE);
    }
    const unsigned o_kv = (wave & 2 ? O_VX : O_KX) + h * 8192 + (wave & 1) * 4096 + lane * 16;
    v4u kx[8];
#define pf kx
    if (wave >= 4) {
#pragma unroll
        for (int q = 0; q < 4; ++q) pf[q] = ld16(rs, o_kv + q * 1024, 0);
    }
    asm volatile("s_waitcnt vmcnt(0)" ::: "memory");
    BAR();
    if (wave >= 4) {
#pragma unroll
        for (int q = 0; q < 4; ++q) *(LAS v4u*)(lds + LC_KV + (wave - 4) * 4096 + q * 1024 + lane * 16) = pf[q];
    }
    unsigned anchor = 0, plan = 0;
    bool dead = false;
    if (wave == 0) {
        unsigned old = 0;
        if (lane == 0) old = __hip_atomic_fetch_add(ctl + C_ARRIVE, 1u, RLX_AGENT);
        old = __builtin_amdgcn_readfirstlane(old);
        if (old == 127u) { plan = (rt() + tm.t0) | 1u; if (lane == 0) __hip_atomic_store(ctl + C_T0, plan, RLX_AGENT); }
        else { unsigned spins = 0; for (;;) { plan = __builtin_amdgcn_readfirstlane(__hip_atomic_load(ctl + C_T0, RLX_AGENT)); if (plan) break; if (poll_fail(spins, ctl, 128)) { dead = true; break; } } }
        if (lane == 0) *(LAS unsigned*)(lds + LC_MISC) = plan;
        wait_tick(rt(), plan);
    }
#define STAMP(k)
    for (int s = 0; s < NSEG; ++s) {
        const unsigned pg = P_HAND + (s >> 2), sb = pg * PAGE + (s & 3) * 524288u, sx = ((sidemask >> pg) & 1u) << 13;
        BAR();
        STAMP(0);
        v4u a0, a1; unsigned lead = 0;
        if (s == 0) {
            const u16* mp = p.mem0 + (lane & 7) * 1024 + 128 * wave + 16 * g4 + 8 * c8;
            a0 = *(const v4u*)mp; a1 = *(const v4u*)(mp + 64);
        } else {
            unsigned spins = 0;
            for (;;) {
                a0 = ld16(rs, o_gm ^ sx, sb); a1 = ld16(rs, (o_gm + 1024) ^ sx, sb);
                if (wave == 7) lead = __builtin_amdgcn_raw_buffer_load_b32(rs, o_lead ^ sx, sb, 16);
                if (!any_sent(mx4(a1, mx4(a0, lead))) || dead) break;
                if (poll_fail(spins, ctl, tm.back)) { dead = true; break; }
                asm volatile("" ::: "memory");
            }
        }
        STAMP(1);
        {
            f32x4 c0 = {0, 0, 0, 0}, c1 = c0, c2 = c0, c3 = c0;
            c0 = smf(a0, WA[0][0], c0, sidx); c1 = smf(a0, WA[1][0], c1, sidx); c2 = smf(a0, WA[2][0], c2, sidx); c3 = smf(a0, WA[3][0], c3, sidx);
            c0 = smf(a1, WA[0][1], c0, sidx); c1 = smf(a1, WA[1][1], c1, sidx); c2 = smf(a1, WA[2][1], c2, sidx); c3 = smf(a1, WA[3][1], c3, sidx);
            LAS unsigned char* pw = lds + LC_PA + ((wave * 2 + rh) * 96 + 16 * tau + n16) * 16;
            *(LAS f32x4*)pw = fold(c0, c1);
            *(LAS f32x4*)(pw + 512) = fold(c2, c3);
        }
        if (wave == 7 && s > 0) {
            const unsigned now = rt();
            int rel = (int)((lead - now) << 1) >> 1;
            rel = max(rel, __builtin_amdgcn_update_dpp(rel, rel, 0xB1, 0xf, 0xf, false));
            rel = max(rel, __builtin_amdgcn_update_dpp(rel, rel, 0x4E, 0xf, 0xf, false));
            if (lane == 0) *(LAS unsigned*)(lds + LC_MISC) = now + rel;
        }
        BAR();
        STAMP(2);
        if (wave < 2) {
            f32x4 r[8];
#pragma unroll
            for (int w = 0; w < 8; ++w) r[w] = *(LAS f32x4*)(lds + l_red + w * 3072);
            const f32x4 t = ((r[0] + r[1]) + (r[2] + r[3])) + ((r[4] + r[5]) + (r[6] + r[7]));
            st8((v2u){nosent(pk2(t.x, t.y)), nosent(pk2(t.z, t.w))}, rs, o_pub ^ sx, sb);
        }
        STAMP(3);
        anchor = __builtin_amdgcn_readfirstlane(*(LAS unsigned*)(lds + LC_MISC));
        {
            f32x4 c4 = {0, 0, 0, 0}, c5 = c4;
            c4 = smf(a0, WA[4][0], c4, sidx); c5 = smf(a0, WA[5][0], c5, sidx);
            c4 = smf(a1, WA[4][1], c4, sidx); c5 = smf(a1, WA[5][1], c5, sidx);
            *(LAS f32x4*)(lds + LC_PA + ((wave * 2 + rh) * 96 + 64 + 16 * tau + n16) * 16) = fold(c4, c5);
        }
        BAR();
        if (wave == 2) {
            f32x4 r[8];
#pragma unroll
            for (int w = 0; w < 8; ++w) r[w] = *(LAS f32x4*)(lds + l_red + w * 3072);
            const f32x4 t = ((r[0] + r[1]) + (r[2] + r[3])) + ((r[4] + r[5]) + (r[6] + r[7]));
            st8((v2u){nosent(pk2(t.x, t.y)), nosent(pk2(t.z, t.w))}, rs, o_pub ^ sx, sb);
        }
        if (s == NSEG - 1) break;
        LAS unsigned char* kvb = lds + LC_KV + (s & 1) * 16384;
        v4u vx[2], gq;
        if (wave < 4) {
#pragma unroll
            for (int q = 0; q < 8; ++q) kx[q] = *(LAS v4u*)(kvb + q * 1024 + lane * 16);
            vx[0] = *(LAS v4u*)(kvb + 8192 + wave * 2048 + lane * 16); vx[1] = *(LAS v4u*)(kvb + 8192 + wave * 2048 + 1024 + lane * 16);
            const unsigned now = rt(), sch = anchor + tm.ta;
            wait_tick(now, (int)(now + tm.dmin - sch) > 0 ? now + tm.dbig : sch);
            STAMP(4);
            unsigned spins = 0;
            for (;;) {
                gq = ld16(rs, o_gq ^ sx, sb);
                if (!any_sent(mx4(gq, 0)) || dead) break;
                if (poll_fail(spins, ctl, tm.back)) { dead = true; break; }
                asm volatile("" ::: "memory");
            }
            *(LAS v4u*)(lds + LC_QK + wave * 1024 + lane * 16) = gq;
            STAMP(5);
        } else if (wave < 6) {
            const unsigned now = rt(), sch = anchor + tm.tv;
            wait_tick(now, (int)(now + tm.dmin - sch) > 0 ? now + tm.dbig : sch);
            gq = ld16(rs, o_gq ^ sx, sb);
        }
        BAR();
        v4u b1; unsigned b2 = 0; float rinv = 0.f;
        if (wave < 4) {
            f32x4 s0 = {0, 0, 0, 0}, s1 = s0, sm = s0;
            LAS unsigned char* qb = lds + LC_QK + (4 * g4 + ((lane & 15) >> 2)) * 32 + (lane & 1) * 8;
#pragma unroll
            for (int c = 0; c < 4; ++c) {
                const v2u qa = trrd(qb + c * 1024), qc = trrd(qb + c * 1024 + 512), ka = trrd(qb + c * 1024 + 16), kc = trrd(qb + c * 1024 + 528);
                const v4u qf = {qa.x, qa.y, qc.x, qc.y}, kf = {ka.x, ka.y, kc.x, kc.y};
                s0 = mfma32(kx[c], qf, s0); s1 = mfma32(kx[4 + c], qf, s1); sm = mfma32(kf, qf, sm);
            }
            float x0 = c8 ? s1.x : s0.x, x1 = c8 ? s1.y : s0.y, x2 = c8 ? s1.z : s0.z, x3 = c8 ? s1.w : s0.w;
            float xm = tau ? (c8 ? sm.w : sm.z) : (c8 ? sm.y : sm.x);
            { unsigned u = pk2(x0, x1); u = pk2(lo16(u) * SCALE, hi16(u) * SCALE); x0 = lo16(u); x1 = hi16(u);
              u = pk2(x2, x3); u = pk2(lo16(u) * SCALE, hi16(u) * SCALE); x2 = lo16(u); x3 = hi16(u);
              xm = rnd(rnd(xm) * SCALE); }
            float m = fmaxf(fmaxf(fmaxf(x0, x1), fmaxf(x2, x3)), xm);
            m = fmaxf(m, ror8(m)); m = sw16max(m); m = sw32max(m);
            const float ml = -m * LOG2E;
            x0 = __builtin_amdgcn_exp2f(fmaf(x0, LOG2E, ml)); x1 = __builtin_amdgcn_exp2f(fmaf(x1, LOG2E, ml));
            x2 = __builtin_amdgcn_exp2f(fmaf(x2, LOG2E, ml)); x3 = __builtin_amdgcn_exp2f(fmaf(x3, LOG2E, ml));
            xm = __builtin_amdgcn_exp2f(fmaf(xm, LOG2E, ml));
            const unsigned e01 = pk2(x0, x1), e23 = pk2(x2, x3), em = pk2(xm, xm);
            b1 = (v4u){c8 ? 0u : e01, c8 ? 0u : e23, c8 ? e01 : 0u, c8 ? e23 : 0u};
            b2 = em & (c8 ? 0xffff0000u : 0x0000ffffu);
            float l = ((x0 + x1) + (x2 + x3)) + xm;
            l += ror8(l); l = sw16sum(l, l); l = sw32sum(l, l);
            rinv = __builtin_amdgcn_rcpf(l);
            STAMP(6);
        } else {
            if (wave < 6) {
                unsigned spins = 0;
                while (any_sent(mx4(gq, 0)) && !dead) {
                    if (poll_fail(spins, ctl, tm.back)) { dead = true; break; }
                    asm volatile("" ::: "memory");
                    gq = ld16(rs, o_gq ^ sx, sb);
                }
                *(LAS v4u*)(lds + LC_V + (wave & 1) * 1024 + lane * 16) = gq;
            }
#pragma unroll
            for (int q = 0; q < 4; ++q) pf[q] = ld16(rs, o_kv + q * 1024, (s + 1) * 262144u);
            const unsigned rp = P_PART + ((s + 2) & 3), rx = ((sidemask >> rp) & 1u) << 13;
            const v4u zz = {SENT, SENT, SENT, SENT};
            st16(zz, rs, o_pp ^ rx, rp * PAGE); st16(zz, rs, o_pp2 ^ rx, rp * PAGE);
        }
        BAR();
        if (wave < 4) {
            LAS unsigned char* vb = lds + LC_V + (32 * wave + n16) * 16 + (4 * (g4 & 1) + 2 * (g4 >> 1)) * 2;
            const unsigned va = *(LAS unsigned*)vb, vc = *(LAS unsigned*)(vb + 256);
            f32x4 o0 = {0, 0, 0, 0}, o1 = o0;
            o0 = mfma32(vx[0], b1, o0); o1 = mfma32(vx[1], b1, o1);
            o0 = mfma16((v2u){va, 0u}, (v2u){b2, 0u}, o0); o1 = mfma16((v2u){vc, 0u}, (v2u){b2, 0u}, o1);
            float y[4];
#pragma unroll
            for (int e = 0; e < 4; ++e) y[e] = ((c8 ? o1[e] : o0[e]) + ror8(c8 ? o0[e] : o1[e])) * rinv;
            *(LAS v2u*)(lds + LC_O + (lane & 7) * 320 + (32 * wave + 16 * c8 + 4 * g4) * 2) = (v2u){pk2(y[0], y[1]), pk2(y[2], y[3])};
        }
        BAR();
        const unsigned pp = P_PART + (s & 3), px = ((sidemask >> pp) & 1u) << 13;
        {
            LAS unsigned char* ob = lds + LC_O + (lane & 7) * 320 + c8 * 16 + g4 * 32;
            const v4u o0 = *(LAS v4u*)ob, o1 = *(LAS v4u*)(ob + 128);
            f32x4 d0 = {0, 0, 0, 0}, d1 = d0;
            d0 = smf(o0, WO[0][0], d0, sidx); d1 = smf(o0, WO[1][0], d1, sidx);
            d0 = smf(o1, WO[0][1], d0, sidx); d1 = smf(o1, WO[1][1], d1, sidx);
            const f32x4 f = fold(d0, d1);
            st16((v4u){nosent(__float_as_uint(f.x)), nosent(__float_as_uint(f.y)), nosent(__float_as_uint(f.z)), nosent(__float_as_uint(f.w))}, rs, o_pp ^ px, pp * PAGE);
        }
        STAMP(7);
        if (wave == 0) {
            { const unsigned now = rt(), sch = anchor + tm.tb; wait_tick(now, (int)(now + tm.dmin - sch) > 0 ? now + tm.dbig : sch); }
            STAMP(8);
            v4u r[8]; unsigned spins = 0;
            for (;;) {
#pragma unroll
                for (int q = 0; q < 8; ++q) r[q] = ld16(rs, (o_gp + q * 1024) ^ px, pp * PAGE);
                unsigned m = 0;
#pragma unroll
                for (int q = 0; q < 8; ++q) m = mx4(r[q], m);
                if (!any_sent(m) || dead) break;
                if (poll_fail(spins, ctl, tm.back)) { dead = true; break; }
                asm volatile("" ::: "memory");
            }
            STAMP(9);
            f32x4 t[8];
#pragma unroll
            for (int q = 0; q < 8; ++q) t[q] = __builtin_bit_cast(f32x4, r[q]);
            const f32x4 u = ((t[0] + t[1]) + (t[2] + t[3])) + ((t[4] + t[5]) + (t[6] + t[7]));
            const float z = sw16sum(sw32sum(u.x, u.y), sw32sum(u.z, u.w));
            const unsigned pg1 = P_HAND + ((s + 1) >> 2), sb1 = pg1 * PAGE + ((s + 1) & 3) * 524288u, sx1 = ((sidemask >> pg1) & 1u) << 13;
            const unsigned w2 = nosent(pk2(z, xor1(z)));
            if ((lane & 1) == 0) st4(w2, rs, o_pm ^ sx1, sb1);
            const unsigned now = rt(), sch = anchor + tm.tc;
            plan = (int)(now + tm.dmin - sch) > 0 ? now + tm.dbig : sch;
            if (i == 0 && lane == 0) st4(plan & 0x7fffffffu, rs, amap(L_LEAD + g * 128) ^ sx1, sb1);
            wait_tick(now, plan);
        } else if (wave >= 4) {
#pragma unroll
            for (int q = 0; q < 4; ++q) *(LAS v4u*)(lds + LC_KV + ((s + 1) & 1) * 16384 + (wave - 4) * 4096 + q * 1024 + lane * 16) = pf[q];
        }
    }
}

#undef pf
DEVI int claim_role(GAS unsigned* ctl) {
    const unsigned x = xcc_id();
    if (x < 4) {
        const unsigned r = __hip_atomic_fetch_add(ctl + C_XR + 64 * x, 1u, RLX_AGENT);
        if (r < 32 && __hip_atomic_exchange(ctl + C_SLOT + x * 32 + r, 1u, RLX_AGENT) == 0u) return (int)(x * 32 + r);
    }
    const unsigned g = __hip_atomic_fetch_add(ctl + C_GRANK, 1u, RLX_AGENT);
    if (g < 128) return (int)(128 + g);
    for (int k = 127;; k = (k + 127) & 127) if (__hip_atomic_exchange(ctl + C_SLOT + k, 1u, RLX_AGENT) == 0u) return k;
}

__global__ void __launch_bounds__(512, 2) k_chain(Params p, Tim tm) {
    extern __shared__ __attribute__((aligned(16))) unsigned char smem[];
    LAS unsigned char* lds = (LAS unsigned char*)smem;
    if (threadIdx.x == 0) *(LAS int*)(lds + 98304) = claim_role((GAS unsigned*)(p.ws + O_CTL));
    __syncthreads();
    const int role = __builtin_amdgcn_readfirstlane(*(LAS int*)(lds + 98304));
    if (role < 128) chain_body(p, tm, lds, role >> 5, role & 31);
}

template <int BM, int WM, bool TR, bool PERMK>
struct Gemm {
    static constexpr int WN = 8 / WM, MT = BM / WM / 16, NT = 128 / WN / 16, NA = BM / 64, STAGE = BM * 128 + 16384;
    f32x4 acc[MT][NT];
    DEVI void run(LAS unsigned char* lds, const u16* A, int lda, const u16* B, int ldb, int nk) {
        const int tid = threadIdx.x, lane = tid & 63, wave = __builtin_amdgcn_readfirstlane(tid >> 6);
        const int wm = wave / WN, wn = wave % WN;
        const u16* ga = A + (size_t)(wave * NA * 8 + (lane >> 3)) * lda + (((lane & 7) ^ ((lane >> 3) & 7)) * 8);
        const int brow = wave * 8 + (lane >> 4);
        int src0, src1;
        { const int k0 = brow, k1 = brow + 4;
          auto pr = [](int k) { return PERMK ? ((k & 32) + 16 * ((k >> 2) & 1) + 4 * ((k >> 3) & 3) + (k & 3)) : k; };
          auto cf = [&](int k) { const int f = (k & 3) | (((k >> 3) & 1) << 2); return ((((lane & 15) >> 1) ^ f) * 16 + (lane & 1) * 8); };
          src0 = pr(k0) * ldb + cf(k0); src1 = pr(k1) * ldb + cf(k1); }
        auto stage = [&](int kt, int buf) {
            LAS unsigned char* sa = lds + buf * STAGE + wave * NA * 1024;
#pragma unroll
            for (int q = 0; q < NA; ++q) __builtin_amdgcn_global_load_lds((const GAS unsigned*)(ga + (size_t)q * 8 * lda + kt * 64), (LAS unsigned*)(sa + q * 1024), 16, 0, 0);
            LAS unsigned char* sbp = lds + buf * STAGE + BM * 128 + wave * 2048;
            const u16* gb = B + (size_t)kt * 64 * ldb;
            __builtin_amdgcn_global_load_lds((const GAS unsigned*)(gb + src0), (LAS unsigned*)sbp, 16, 0, 0);
            __builtin_amdgcn_global_load_lds((const GAS unsigned*)(gb + src1), (LAS unsigned*)(sbp + 1024), 16, 0, 0);
        };
        const int fr = lane & 15, fg = lane >> 4;
        const unsigned ra = (wm * (BM / WM) + fr) * 128;
        const unsigned ra0 = ra + ((fg ^ (fr & 7)) * 16), ra1 = ra + (((4 + fg) ^ (fr & 7)) * 16);
        const int q4 = fr >> 2, p4 = fr & 3;
        const unsigned rb = BM * 128 + (8 * fg + q4) * 256 + (p4 >> 1) * 16 + (p4 & 1) * 8;
        const int fsw = q4 | ((fg & 1) << 2);
#pragma unroll
        for (int m = 0; m < MT; ++m)
#pragma unroll
            for (int n = 0; n < NT; ++n) acc[m][n] = (f32x4){0, 0, 0, 0};
        stage(0, 0); stage(1, 1);
        int buf = 0;
        for (int kt = 0; kt < nk; ++kt) {
            if (kt + 1 < nk) asm volatile("s_waitcnt vmcnt(%0)" :: "n"(NA + 2) : "memory"); else asm volatile("s_waitcnt vmcnt(0)" ::: "memory");
            asm volatile("s_barrier" ::: "memory");
            if (kt + 2 < nk) stage(kt + 2, buf == 0 ? 2 : buf - 1);
            LAS unsigned char* sp = lds + buf * STAGE;
#pragma unroll
            for (int ks = 0; ks < 2; ++ks) {
                v4u af[MT], bf[NT];
#pragma unroll
                for (int m = 0; m < MT; ++m) af[m] = *(LAS v4u*)(sp + (ks ? ra1 : ra0) + m * 2048);
#pragma unroll
                for (int n = 0; n < NT; ++n) {
                    LAS unsigned char* bp = sp + rb + ks * 8192 + (((wn * NT + n) ^ fsw) * 32);
                    const v2u x = trrd(bp), y = trrd(bp + 1024);
                    bf[n] = (v4u){x.x, x.y, y.x, y.y};
                }
#pragma unroll
                for (int m = 0; m < MT; ++m)
#pragma unroll
                    for (int n = 0; n < NT; ++n) acc[m][n] = TR ? mfma32(bf[n], af[m], acc[m][n]) : mfma32(af[m], bf[n], acc[m][n]);
            }
            buf = buf == 2 ? 0 : buf + 1;
        }
        asm volatile("s_waitcnt lgkmcnt(0)\n\ts_barrier" ::: "memory");
    }
};
DEVI v4u pk8(f32x4 a, f32x4 b) { return (v4u){pk2(a.x, a.y), pk2(a.z, a.w), pk2(b.x, b.y), pk2(b.z, b.w)}; }

template <int BM>
DEVI void kv_unit(const Params& p, LAS unsigned char* lds, int r0, int hh, int mat) {
    const int lane = threadIdx.x & 63, wave = __builtin_amdgcn_readfirstlane(threadIdx.x >> 6), wm = wave >> 1, wn = wave & 1;
    constexpr int MT = BM / 64;
    const __amdgpu_buffer_rsrc_t rs = __builtin_amdgcn_make_buffer_rsrc(p.ws, 0, 0xFFFFFF00u, 0x00020000);
    const int row0 = r0 + wm * (BM / 4);
    if (mat == 0) {
        Gemm<BM, 4, true, false> g; g.run(lds, p.x + (size_t)r0 * D_, D_, p.wk + hh * 128, 4096, 16);
#pragma unroll
        for (int m = 0; m < MT; ++m)
#pragma unroll
            for (int c = 0; c < 2; ++c) {
                const int row = row0 + m * 16;
                st16(pk8(g.acc[m][2 * c], g.acc[m][2 * c + 1]), rs, lane * 16, O_KX + ((row >> 5) * 32 + hh) * 8192 + (((row >> 4) & 1) * 4 + wn * 2 + c) * 1024);
            }
    } else {
        Gemm<BM, 4, false, false> g; g.run(lds, p.x + (size_t)r0 * D_, D_, p.wv + hh * 128, 4096, 16);
#pragma unroll
        for (int m = 0; m < MT; m += 2)
#pragma unroll
            for (int n = 0; n < 4; ++n) {
                const int row = row0 + m * 16;
                st16(pk8(g.acc[m][n], g.acc[m + 1][n]), rs, lane * 16, O_VX + ((row >> 5) * 32 + hh) * 8192 + (wn * 4 + n) * 1024);
            }
    }
}
__global__ void __launch_bounds__(512, 2) k_kv(Params p) {
    extern __shared__ __attribute__((aligned(16))) unsigned char smem[];
    const int u = blockIdx.x;
    kv_unit<256>(p, (LAS unsigned char*)smem, (u >> 6) * 256, (u >> 1) & 31, u & 1);
}

DEVI void qa_unit(const Params& p, LAS unsigned char* lds, int panel, int hh, unsigned sidemask) {
    const int lane = threadIdx.x & 63, wave = __builtin_amdgcn_readfirstlane(threadIdx.x >> 6);
    const int g4 = lane >> 4, n16 = lane & 15;
    const __amdgpu_buffer_rsrc_t rs = __builtin_amdgcn_make_buffer_rsrc(p.ws, 0, 0xFFFFFF00u, 0x00020000);
    v4u qf[2][4];
    {
        Gemm<256, 8, true, false> g; g.run(lds, p.x + (size_t)panel * 256 * D_, D_, p.wq + hh * 128, 4096, 16);
#pragma unroll
        for (int m = 0; m < 2; ++m)
#pragma unroll
            for (int c = 0; c < 4; ++c) qf[m][c] = pk8(g.acc[m][2 * c], g.acc[m][2 * c + 1]);
    }
    const int seg = panel * 8 + wave;
    const unsigned kvo = (seg * 32 + hh) * 8192;
    v4u kx[8], vx[8];
#pragma unroll
    for (int q = 0; q < 8; ++q) kx[q] = ld16(rs, lane * 16 + q * 1024, O_KX + kvo);
#pragma unroll
    for (int q = 0; q < 8; ++q) vx[q] = ld16(rs, lane * 16 + q * 1024, O_VX + kvo);
    LAS unsigned char* img = lds + wave * 6144;
    {
        const unsigned pg = P_HAND + (seg >> 2), sb = pg * PAGE + (seg & 3) * 524288u, sx = ((sidemask >> pg) & 1u) << 13;
        v4u t[6];
#pragma unroll
        for (int q = 0; q < 6; ++q) t[q] = ld16(rs, amap(L_QKV + hh * 6144 + q * 1024 + lane * 16) ^ sx, sb);
#pragma unroll
        for (int q = 0; q < 6; ++q) *(LAS v4u*)(img + q * 1024 + lane * 16) = t[q];
        asm volatile("s_waitcnt lgkmcnt(0)" ::: "memory");
    }
    f32x4 z = {0, 0, 0, 0};
    f32x4 s00 = z, s10 = z, s11 = z, sm0 = z, sm1 = z;
    LAS unsigned char* kb = img + (4 * g4 + (n16 >> 2)) * 32 + 16 + (lane & 1) * 8;
#pragma unroll
    for (int c = 0; c < 4; ++c) {
        const v2u ka = trrd(kb + c * 1024), kc = trrd(kb + c * 1024 + 512);
        const v4u kf = {ka.x, ka.y, kc.x, kc.y};
        s00 = mfma32(kx[c], qf[0][c], s00); s10 = mfma32(kx[c], qf[1][c], s10); s11 = mfma32(kx[4 + c], qf[1][c], s11);
        sm0 = mfma32(kf, qf[0][c], sm0); sm1 = mfma32(kf, qf[1][c], sm1);
    }
    v4u b1[2]; v2u b2[2];
#pragma unroll
    for (int m = 0; m < 2; ++m) {
        float v[12];
#pragma unroll
        for (int e = 0; e < 4; ++e) {
            const bool diag = 4 * g4 + e <= n16;
            v[e] = rnd(rnd(m ? s10[e] : s00[e]) * SCALE); if (m == 0 && !diag) v[e] = -INFINITY;
            v[4 + e] = (m && diag) ? rnd(rnd(s11[e]) * SCALE) : -INFINITY;
            v[8 + e] = g4 < 2 ? rnd(rnd(m ? sm1[e] : sm0[e]) * SCALE) : -INFINITY;
        }
        float mx = v[0];
#pragma unroll
        for (int e = 1; e < 12; ++e) mx = fmaxf(mx, v[e]);
        mx = sw16max(mx); mx = sw32max(mx);
        const float ml = -mx * LOG2E; float l = 0.f;
#pragma unroll
        for (int e = 0; e < 12; ++e) { v[e] = __builtin_amdgcn_exp2f(fmaf(v[e], LOG2E, ml)); l += v[e]; }
        l = sw16sum(l, l); l = sw32sum(l, l);
        const float ri = 1.f / l;
        b1[m] = (v4u){pk2(v[0] * ri, v[1] * ri), pk2(v[2] * ri, v[3] * ri), pk2(v[4] * ri, v[5] * ri), pk2(v[6] * ri, v[7] * ri)};
        b2[m] = (v2u){pk2(v[8] * ri, v[9] * ri), pk2(v[10] * ri, v[11] * ri)};
    }
    u16* ox = (u16*)(p.ws + O_OX);
#pragma unroll
    for (int c = 0; c < 4; ++c) {
        f32x4 o[2][2];
#pragma unroll
        for (int t = 0; t < 2; ++t) {
            const v2u va = *(LAS v2u*)(img + 4096 + ((2 * c + t) * 16 + n16) * 16 + (g4 & 1) * 8);
#pragma unroll
            for (int m = 0; m < 2; ++m) { o[m][t] = mfma32(vx[2 * c + t], b1[m], z); o[m][t] = mfma16(va, b2[m], o[m][t]); }
        }
#pragma unroll
        for (int m = 0; m < 2; ++m) st16(pk8(o[m][0], o[m][1]), rs, (n16 * 4096 + 8 * g4) * 2, O_OX + ((seg * 32 + m * 16) * 4096 + hh * 128 + c * 32) * 2);
    }
    (void)ox;
}
__global__ void __launch_bounds__(512, 2) k_qa(Params p) {
    extern __shared__ __attribute__((aligned(16))) unsigned char smem[];
    const int lane = threadIdx.x & 63;
    const unsigned* ctl = (const unsigned*)(p.ws + O_CTL);
    const unsigned sv = lane < 21 ? (ctl[C_SIDE + lane] & 1u) : 0u;
    const unsigned sidemask = (unsigned)__builtin_amdgcn_ballot_w64(sv != 0);
    qa_unit(p, (LAS unsigned char*)smem, blockIdx.x >> 5, blockIdx.x & 31, sidemask);
}

DEVI void top2(const float* R, int& i0, int& i1) {
    const int lane = threadIdx.x & 63;
    float b0 = -INFINITY, b1 = -INFINITY; int j0 = 0x7fffffff, j1 = 0x7fffffff;
    for (int k = lane; k < S_; k += 64) {
        const float v = R[k];
        if (v > b0 || j0 == 0x7fffffff) { b1 = b0; j1 = j0; b0 = v; j0 = k; } else if (v > b1 || j1 == 0x7fffffff) { b1 = v; j1 = k; }
    }
    for (int m = 1; m < 64; m <<= 1) {
        const float c0 = __shfl_xor(b0, m), c1 = __shfl_xor(b1, m); const int k0 = __shfl_xor(j0, m), k1 = __shfl_xor(j1, m);
        auto gt = [](float a, int ja, float b, int jb) { return a > b || (!(b > a) && ja < jb); };
        if (gt(c0, k0, b0, j0)) { if (gt(b0, j0, c1, k1)) { b1 = b0; j1 = j0; } else { b1 = c1; j1 = k1; } b0 = c0; j0 = k0; }
        else if (gt(c0, k0, b1, j1)) { b1 = c0; j1 = k0; }
    }
    i0 = j0; i1 = j1;
}
__global__ void __launch_bounds__(256) k_ffn1(Params p) {
    __shared__ float red[32][4][64];
    int i0, i1; top2((const float*)(p.ws + O_R), i0, i1);
    const int tid = threadIdx.x, co = tid & 7, ksl = tid >> 3, col = blockIdx.x * 64 + co * 8;
    float a[4][8] = {};
    for (int k = ksl; k < D_; k += 32) {
        const v4u wg = *(const v4u*)(p.wg + (size_t)k * F_ + col), wu = *(const v4u*)(p.wu + (size_t)k * F_ + col);
        const float x0 = bf2f(p.x[i0 * D_ + k]), x1 = bf2f(p.x[i1 * D_ + k]);
#pragma unroll
        for (int e = 0; e < 4; ++e) {
            a[0][2 * e] += x0 * lo16(wg[e]); a[0][2 * e + 1] += x0 * hi16(wg[e]); a[1][2 * e] += x1 * lo16(wg[e]); a[1][2 * e + 1] += x1 * hi16(wg[e]);
            a[2][2 * e] += x0 * lo16(wu[e]); a[2][2 * e + 1] += x0 * hi16(wu[e]); a[3][2 * e] += x1 * lo16(wu[e]); a[3][2 * e + 1] += x1 * hi16(wu[e]);
        }
    }
#pragma unroll
    for (int q = 0; q < 4; ++q)
#pragma unroll
        for (int e = 0; e < 8; ++e) red[ksl][q][co * 8 + e] = a[q][e];
    __syncthreads();
    if (tid < 128) {
        const int t = tid >> 6, c = tid & 63; float g = 0.f, u = 0.f;
        for (int k = 0; k < 32; ++k) { g += red[k][t][c]; u += red[k][2 + t][c]; }
        g = rnd(g); u = rnd(u); g = rnd(g / (1.f + __expf(-g)));
        ((float*)(p.ws + O_FH))[t * F_ + blockIdx.x * 64 + c] = rnd(g * u);
    }
}
__global__ void __launch_bounds__(256) k_ffn2(Params p) {
    __shared__ float red[32][2][64];
    const int tid = threadIdx.x, co = tid & 7, ksl = tid >> 3, col = blockIdx.x * 64 + co * 8;
    const float* hb = (const float*)(p.ws + O_FH);
    float a[2][8] = {};
    for (int k = ksl; k < F_; k += 32) {
        const v4u w = *(const v4u*)(p.wd + (size_t)k * D_ + col);
        const float x0 = hb[k], x1 = hb[F_ + k];
#pragma unroll
        for (int e = 0; e < 4; ++e) { a[0][2 * e] += x0 * lo16(w[e]); a[0][2 * e + 1] += x0 * hi16(w[e]); a[1][2 * e] += x1 * lo16(w[e]); a[1][2 * e + 1] += x1 * hi16(w[e]); }
    }
#pragma unroll
    for (int q = 0; q < 2; ++q)
#pragma unroll
        for (int e = 0; e < 8; ++e) red[ksl][q][co * 8 + e] = a[q][e];
    __syncthreads();
    if (tid < 128) {
        const int t = tid >> 6, c = tid & 63; float y = 0.f;
        for (int k = 0; k < 32; ++k) y += red[k][t][c];
        ((float*)(p.ws + O_FY))[t * D_ + blockIdx.x * 64 + c] = rnd(y);
    }
}

__global__ void __launch_bounds__(512, 2) k_wo(Params p) {
    extern __shared__ __attribute__((aligned(16))) unsigned char smem[];
    const int lane = threadIdx.x & 63, wave = __builtin_amdgcn_readfirstlane(threadIdx.x >> 6), wm = wave >> 1, wn = wave & 1;
    const int r0 = (blockIdx.x >> 3) * 128, c0 = (blockIdx.x & 7) * 128;
    Gemm<128, 4, true, true> g; g.run((LAS unsigned char*)smem, (const u16*)(p.ws + O_OX) + (size_t)r0 * 4096, 4096, p.wo + c0, 1024, 64);
    const float* R = (const float*)(p.ws + O_R); const float* G = (const float*)(p.ws + O_G); const float* Y = (const float*)(p.ws + O_FY);
    int i0, i1; top2(R, i0, i1);
#pragma unroll
    for (int m = 0; m < 2; ++m) {
        const int row = r0 + wm * 32 + m * 16 + (lane & 15);
        const float gv = G[row], og = rnd(1.f - gv);
        const bool sel = row == i0 || row == i1;
        const float rs_ = sel ? rnd(1.f / (1.f + __expf(-R[row]))) : 0.f;
#pragma unroll
        for (int n = 0; n < 4; ++n) {
            const int col = c0 + wn * 64 + n * 16 + (lane >> 4) * 4;
            const v2u xv = *(const v2u*)(p.x + row * D_ + col);
            float b[4] = {lo16(xv.x), hi16(xv.x), lo16(xv.y), hi16(xv.y)}, o[4];
#pragma unroll
            for (int e = 0; e < 4; ++e) {
                if (sel) b[e] = rnd(b[e] + rnd(Y[(row == i0 ? 0 : D_) + col + e] * rs_));
                o[e] = rnd(gv * rnd(g.acc[m][n][e])) + rnd(og * b[e]);
            }
            *(v2u*)(p.out + row * D_ + col) = (v2u){pk2(o[0], o[1]), pk2(o[2], o[3])};
        }
    }
}

#define TIM_INIT {70, 82, 166, 215, 4, 35, 200, 150}
extern "C" void kernel_launch(void* const* d_in, const int* in_sizes, int n_in, void* d_out, int out_size, void* d_ws, size_t ws_size, hipStream_t stream) {
    Params p;
    p.x = (const u16*)d_in[0]; p.mem0 = (const u16*)d_in[1]; p.wq = (const u16*)d_in[2]; p.wk = (const u16*)d_in[3]; p.wv = (const u16*)d_in[4]; p.wo = (const u16*)d_in[5];
    p.wr = (const u16*)d_in[8]; p.wg = (const u16*)d_in[9]; p.wu = (const u16*)d_in[10]; p.wd = (const u16*)d_in[11]; p.gw = (const u16*)d_in[12];
    p.out = (u16*)d_out;
    p.ws = (unsigned char*)(((uintptr_t)d_ws + PAGE - 1) & ~(uintptr_t)(PAGE - 1));
    Tim tm = TIM_INIT;
    constexpr int LDS_G = 3 * (256 * 128 + 16384), LDS_W = 3 * (128 * 128 + 16384), LDS_C = 98304 + 64;
    (void)hipFuncSetAttribute((const void*)k_kv, hipFuncAttributeMaxDynamicSharedMemorySize, LDS_G);
    (void)hipFuncSetAttribute((const void*)k_qa, hipFuncAttributeMaxDynamicSharedMemorySize, LDS_G);
    (void)hipFuncSetAttribute((const void*)k_wo, hipFuncAttributeMaxDynamicSharedMemorySize, LDS_W);
    (void)hipFuncSetAttribute((const void*)k_chain, hipFuncAttributeMaxDynamicSharedMemorySize, LDS_C);
    hipLaunchKernelGGL(k_prep, dim3(256), dim3(256), 0, stream, p);
    hipLaunchKernelGGL(k_kv, dim3(512), dim3(512), LDS_G, stream, p);
    hipLaunchKernelGGL(k_chain, dim3(256), dim3(512), LDS_C, stream, p, tm);
    hipLaunchKernelGGL(k_qa, dim3(256), dim3(512), LDS_G, stream, p);
    hipLaunchKernelGGL(k_ffn1, dim3(56), dim3(256), 0, stream, p);
    hipLaunchKernelGGL(k_ffn2, dim3(16), dim3(256), 0, stream, p);
    hipLaunchKernelGGL(k_wo, dim3(128), dim3(512), LDS_W, stream, p);
}
```

```cpp
#include <hip/hip_runtime.h>
#include <hip/hip_bf16.h>
#include <stdint.h>

typedef unsigned short u16;
typedef unsigned v4u __attribute__((ext_vector_type(4)));
typedef unsigned v2u __attribute__((ext_vector_type(2)));
typedef unsigned v8u __attribute__((ext_vector_type(8)));
typedef float f32x4 __attribute__((ext_vector_type(4)));
typedef __bf16 bf16x2 __attribute__((ext_vector_type(2)));
typedef __bf16 bf16x8 __attribute__((ext_vector_type(8)));
typedef __bf16 bf16x16 __attribute__((ext_vector_type(16)));
typedef short s16x4 __attribute__((ext_vector_type(4)));
#define GAS __attribute__((address_space(1)))
#define LAS __attribute__((address_space(3)))
#define RLX_AGENT __ATOMIC_RELAXED, __HIP_MEMORY_SCOPE_AGENT
#define DEVI __device__ __forceinline__
#define BAR() asm volatile("s_waitcnt lgkmcnt(0)\n\ts_barrier" ::: "memory")

constexpr int S_ = 2048, D_ = 1024, H_ = 32, DH = 128, NSEG = 64, F_ = 3584;
constexpr float SCALE = 0.08838834764831845f, LOG2E = 1.4426950408889634f;
constexpr unsigned SENT = 0xFFFFFFFFu;
constexpr unsigned PAGE = 2u << 20;
constexpr unsigned O_CTL = 0, O_R = 65536, O_G = O_R + 8192, O_FH = 131072, O_FY = O_FH + 32768;
constexpr unsigned P_HAND = 1, P_PART = 17;
constexpr unsigned O_KX = 21 * PAGE, O_VX = 29 * PAGE, O_OX = 37 * PAGE, O_SLAB = 45 * PAGE  , O_DBG = 61 * PAGE;
constexpr unsigned L_QKV = 16384, L_LEAD = 16384 + 32 * 6144;
constexpr int C_ABORT = 0, C_ARRIVE = 64, C_T0 = 128, C_QUEUE = 192, C_XR = 256  , C_F1 = 768, C_F2 = 800, C_SLOT = 1024  ,
              C_KV = 2048  , C_PROG = 6144  , C_OC = 7168  , C_TK = 8192  , C_ZEND = 10240,
              C_SIDE = 10240  ;

struct Params { const u16 *x, *mem0, *wq, *wk, *wv, *wo, *wr, *wg, *wu, *wd, *gw; u16* out; unsigned char* ws; };
struct Tim { int ta, tv, tb, tc, dmin, dbig, back, t0, tb2, dmin3, dbig1, tol; };

DEVI float bf2f(u16 b) { return __uint_as_float((unsigned)b << 16); }
DEVI unsigned pk2(float lo, float hi) { bf16x2 v = {(__bf16)lo, (__bf16)hi}; return __builtin_bit_cast(unsigned, v); }
DEVI float rnd(float f) { return __uint_as_float(pk2(f, 0.f) << 16); }
DEVI float lo16(unsigned u) { return __uint_as_float(u << 16); }
DEVI float hi16(unsigned u) { return __uint_as_float(u & 0xffff0000u); }
DEVI int otid() { int t = threadIdx.x; asm volatile("" : "+v"(t)); return t; }
DEVI unsigned xcc_id() { return (unsigned)__builtin_amdgcn_s_getreg(20 | (3 << 11)) & 0xfu; }
DEVI unsigned rt() { return (unsigned)__builtin_amdgcn_s_memrealtime(); }
DEVI unsigned amap(unsigned lo) { return ((lo >> 13) << 14) + (lo & 8191); }
DEVI v4u ld16(__amdgpu_buffer_rsrc_t r, unsigned vo, unsigned so) { return __builtin_amdgcn_raw_buffer_load_b128(r, vo, so, 16); }
DEVI void st16(v4u v, __amdgpu_buffer_rsrc_t r, unsigned vo, unsigned so) { __builtin_amdgcn_raw_buffer_store_b128(v, r, vo, so, 16); }
DEVI void st8(v2u v, __amdgpu_buffer_rsrc_t r, unsigned vo, unsigned so) { __builtin_amdgcn_raw_buffer_store_b64(v, r, vo, so, 16); }
DEVI void st4(unsigned v, __amdgpu_buffer_rsrc_t r, unsigned vo, unsigned so) { __builtin_amdgcn_raw_buffer_store_b32(v, r, vo, so, 16); }
DEVI unsigned umax3(unsigned a, unsigned b, unsigned c) { unsigned r; asm("v_max3_u32 %0, %1, %2, %3" : "=v"(r) : "v"(a), "v"(b), "v"(c)); return r; }
DEVI unsigned mx4(v4u v, unsigned m) { return umax3(umax3(v.x, v.y, m), v.z, v.w); }
DEVI bool any_sent(unsigned m) { return __builtin_amdgcn_ballot_w64(m == SENT) != 0; }
DEVI unsigned nosent(unsigned u) { return u < 0xFFFFFFFEu ? u : 0xFFFFFFFEu; }
DEVI f32x4 mfma32(v4u a, v4u b, f32x4 c) { return __builtin_amdgcn_mfma_f32_16x16x32_bf16(__builtin_bit_cast(bf16x8, a), __builtin_bit_cast(bf16x8, b), c, 0, 0, 0); }
DEVI f32x4 mfma16(v2u a, v2u b, f32x4 c) { return __builtin_amdgcn_mfma_f32_16x16x16bf16_1k(__builtin_bit_cast(s16x4, a), __builtin_bit_cast(s16x4, b), c, 0, 0, 0); }
DEVI f32x4 smf(v4u a, v8u b, f32x4 c, int idx) { return __builtin_amdgcn_smfmac_f32_16x16x64_bf16(__builtin_bit_cast(bf16x8, a), __builtin_bit_cast(bf16x16, b), c, idx, 0, 0); }
DEVI v2u trrd(LAS unsigned char* p) { return __builtin_bit_cast(v2u, __builtin_amdgcn_ds_read_tr16_b64_v4i16((LAS s16x4*)p)); }
DEVI float ror8(float v) { return __int_as_float(__builtin_amdgcn_update_dpp(0, __float_as_int(v), 0x128, 0xf, 0xf, false)); }
DEVI float xor1(float v) { return __int_as_float(__builtin_amdgcn_update_dpp(0, __float_as_int(v), 0xB1, 0xf, 0xf, false)); }
DEVI float sw32sum(float a, float b) { auto r = __builtin_amdgcn_permlane32_swap(__float_as_uint(a), __float_as_uint(b), false, false); return __uint_as_float(r[0]) + __uint_as_float(r[1]); }
DEVI float sw16sum(float a, float b) { auto r = __builtin_amdgcn_permlane16_swap(__float_as_uint(a), __float_as_uint(b), false, false); return __uint_as_float(r[0]) + __uint_as_float(r[1]); }
DEVI float sw32max(float a) { auto r = __builtin_amdgcn_permlane32_swap(__float_as_uint(a), __float_as_uint(a), false, false); return fmaxf(__uint_as_float(r[0]), __uint_as_float(r[1])); }
DEVI float sw16max(float a) { auto r = __builtin_amdgcn_permlane16_swap(__float_as_uint(a), __float_as_uint(a), false, false); return fmaxf(__uint_as_float(r[0]), __uint_as_float(r[1])); }
DEVI f32x4 fold(f32x4 a, f32x4 b) { f32x4 r; r.x = sw32sum(a.x, b.x); r.y = sw32sum(a.y, b.y); r.z = sw32sum(a.z, b.z); r.w = sw32sum(a.w, b.w); return r; }
DEVI float dot2(unsigned a, unsigned b, float c) { return __builtin_amdgcn_fdot2_f32_bf16(__builtin_bit_cast(bf16x2, a), __builtin_bit_cast(bf16x2, b), c, false); }
DEVI unsigned cvtpk(float lo, float hi) { unsigned r; asm("v_cvt_pk_bf16_f32 %0, %1, %2" : "=v"(r) : "v"(lo), "v"(hi)); return r; }
DEVI float max5x8(float a, float b, float c, float d, float e) {
    float m, t;
    asm("v_max3_f32 %0, %2, %3, %4\n\tv_max3_f32 %0, %0, %5, %6\n\ts_nop 1\n\t"
        "v_max_f32_dpp %0, %0, %0 row_ror:8 row_mask:0xf bank_mask:0xf\n\tv_mov_b32 %1, %0\n\ts_nop 1\n\t"
        "v_permlane16_swap_b32 %0, %1\n\tv_max_f32 %0, %0, %1\n\tv_mov_b32 %1, %0\n\ts_nop 1\n\t"
        "v_permlane32_swap_b32 %0, %1\n\tv_max_f32 %0, %0, %1"
        : "=&v"(m), "=&v"(t) : "v"(a), "v"(b), "v"(c), "v"(d), "v"(e));
    return m;
}
#define ALD16(D, VO, SO, IMM) asm volatile("buffer_load_dwordx4 %0, %1, %2, %3 offen offset:" #IMM " sc1" : "=v"(D) : "v"(VO), "s"(rd), "s"(SO) : "memory")
#define ALD4(D, VO, SO, IMM) asm volatile("buffer_load_dword %0, %1, %2, %3 offen offset:" #IMM " sc1" : "=v"(D) : "v"(VO), "s"(rd), "s"(SO) : "memory")
#define WAITV1(N, A) asm volatile("s_waitcnt vmcnt(" #N ")" : "+v"(A) :: "memory")
#define WAITV2(N, A, B) asm volatile("s_waitcnt vmcnt(" #N ")" : "+v"(A), "+v"(B) :: "memory")
#define SB() __builtin_amdgcn_sched_barrier(0)
#define PIN2(a, b) asm volatile("" : "+v"(a), "+v"(b))
DEVI void sleep_units(int n) {
    if (n & 32) __builtin_amdgcn_s_sleep(32);
    if (n & 16) __builtin_amdgcn_s_sleep(16);
    if (n & 8) __builtin_amdgcn_s_sleep(8);
    if (n & 4) __builtin_amdgcn_s_sleep(4);
    if (n & 2) __builtin_amdgcn_s_sleep(2);
    if (n & 1) __builtin_amdgcn_s_sleep(1);
}
DEVI void sleep_cyc(int n) { sleep_units(min(n >> 6, 63)); }
DEVI unsigned mt() { return (unsigned)__builtin_amdgcn_s_memtime(); }
DEVI void wait_l(unsigned t) {
    unsigned d;
    asm volatile("1:\n\ts_memrealtime vcc\n\ts_waitcnt lgkmcnt(0)\n\ts_sub_i32 %0, vcc_lo, %1\n\ts_cmp_lt_i32 %0, 0\n\ts_cbranch_scc1 1b"
                 : "=&s"(d) : "s"(__builtin_amdgcn_readfirstlane(t)) : "memory", "scc", "vcc");
}
constexpr unsigned SPINCAP = 40000;
DEVI bool poll_fail(unsigned& spins, GAS unsigned* ctl, int back) {
    sleep_cyc(back);
    ++spins;
    if ((spins & 63u) == 0u) {
        if (spins > SPINCAP) { __hip_atomic_store(ctl + C_ABORT, 1u, RLX_AGENT); return true; }
        if (__builtin_amdgcn_readfirstlane(__hip_atomic_load(ctl + C_ABORT, RLX_AGENT))) return true;
    }
    return false;
}

DEVI void wait_ge(GAS unsigned* w, unsigned v, GAS unsigned* ctl, bool& dead) {
    unsigned spins = 0;
    while (!dead && (unsigned)__builtin_amdgcn_readfirstlane(__hip_atomic_load(w, RLX_AGENT)) < v) { if (poll_fail(spins, ctl, 256)) dead = true; }
    asm volatile("" ::: "memory");
}

__global__ void __launch_bounds__(256) k_prep(Params p) {
    const int tid = threadIdx.x, lane = tid & 63, wave = tid >> 6, b = blockIdx.x;
    unsigned* ctl = (unsigned*)(p.ws + O_CTL);
    if (b >= 216) ctl[(b - 216) * 256 + tid] = 0u;
    if (b < 160 && tid == 192) {
        const unsigned page = 1 + (b >> 3);
        __amdgpu_buffer_rsrc_t rs = __builtin_amdgcn_make_buffer_rsrc(p.ws, 0, 0xFFFFFF00u, 0x00020000);
        unsigned t[2] = {0xffffffffu, 0xffffffffu};
        for (int rep = 0; rep < 6; ++rep)
            for (int side = 0; side < 2; ++side) {
                const unsigned long long c0 = __builtin_amdgcn_s_memtime();
                asm volatile("s_waitcnt lgkmcnt(0)" ::: "memory");
                st4(SENT, rs, page * PAGE + 480 * 1024 + side * 8192 + (b & 7) * 512, 0);
                asm volatile("s_waitcnt vmcnt(0)" ::: "memory");
                const unsigned long long c1 = __builtin_amdgcn_s_memtime();
                t[side] = min(t[side], (unsigned)(c1 - c0));
            }
        ctl[C_SIDE + page] = (t[1] < t[0] ? 1u : 0u) ^ (xcc_id() >= 4 ? 1u : 0u);
    }
    float* R = (float*)(p.ws + O_R); float* G = (float*)(p.ws + O_G);
    for (int rr = 0; rr < 2; ++rr) {
        const int row = b * 8 + wave * 2 + rr;
        float a = 0.f, g = 0.f;
#pragma unroll
        for (int q = 0; q < 2; ++q) {
            const int k = q * 512 + lane * 8;
            const v4u xv = *(const v4u*)(p.x + row * D_ + k), wr = *(const v4u*)(p.wr + k), gw = *(const v4u*)(p.gw + k);
#pragma unroll
            for (int e = 0; e < 4; ++e) {
                a = fmaf(lo16(xv[e]), lo16(wr[e]), a); a = fmaf(hi16(xv[e]), hi16(wr[e]), a);
                const unsigned pr = pk2(lo16(xv[e]) * lo16(gw[e]), hi16(xv[e]) * hi16(gw[e]));
                g += lo16(pr); g += hi16(pr);
            }
        }
#pragma unroll
        for (int m = 32; m >= 1; m >>= 1) { a += __shfl_xor(a, m); g += __shfl_xor(g, m); }
        if (lane == 0) { R[row] = rnd(a); g = rnd(g); G[row] = rnd(1.f / (1.f + __expf(-g))); }
    }
}

constexpr unsigned LC_PA = 0, LC_QK = 24576, LC_V = LC_QK + 4096, LC_O = LC_V + 2048, LC_MISC = 98304 + 16, LC_KV = 33792  , LC_END = LC_KV + 32768;

DEVI void chain_body(const Params& p, const Tim& tm, LAS unsigned char* lds, const int g, const int i) {
    const int tid = threadIdx.x, lane = tid & 63, wave = __builtin_amdgcn_readfirstlane(tid >> 6);
    const int h = g * 8 + (i >> 2), j = i & 3, cid = h * 4 + j;
    const int kw = (wave + i) & 7;
    GAS unsigned* ctl = (GAS unsigned*)(p.ws + O_CTL);
    const __amdgpu_buffer_rsrc_t rs = __builtin_amdgcn_make_buffer_rsrc(p.ws, 0, 0xFFFFFF00u, 0x00020000);
    unsigned sidemask = 0;
    { const unsigned sv = lane < 21 ? (ctl[C_SIDE + lane] & 1u) : 0u; sidemask = (unsigned)__builtin_amdgcn_ballot_w64(sv != 0); }
    v8u WA[6][2], WO[2][2];
    {
        v4u sa[2][12], so[8];
        const u16* wm[3] = {p.wq, p.wk, p.wv};
#pragma unroll
        for (int ks = 0; ks < 2; ++ks)
#pragma unroll
            for (int m = 0; m < 3; ++m)
#pragma unroll
                for (int q = 0; q < 4; ++q)
                    sa[ks][m * 4 + q] = *(const v4u*)(wm[m] + (size_t)(128 * kw + 64 * ks + 16 * q + (lane >> 2)) * 4096 + h * 128 + 32 * j + (lane & 3) * 8);
        LAS unsigned char* img = lds + wave * 12288;
        const int gB = lane >> 4, q4 = (lane & 15) >> 2, p4 = lane & 3;
        const unsigned rbase = (16 * (gB >> 1) + 4 * (gB & 1) + (q4 & 1) + 8 * (q4 >> 1)) * 64 + p4 * 8;
#pragma unroll
        for (int ks = 0; ks < 2; ++ks) {
#pragma unroll
            for (int q = 0; q < 12; ++q) *(LAS v4u*)(img + q * 1024 + lane * 16) = sa[ks][q];
            if (ks == 0) {
#pragma unroll
        for (int q = 0; q < 8; ++q) so[q] = *(const v4u*)(p.wo + (size_t)(h * 128 + 16 * q + (lane >> 2)) * 1024 + 256 * j + 32 * wave + (lane & 3) * 8);
            }
            asm volatile("s_waitcnt lgkmcnt(0)" ::: "memory");
#pragma unroll
            for (int t = 0; t < 6; ++t)
#pragma unroll
                for (int m = 0; m < 4; ++m) {
                    const v2u r = trrd(img + (t >> 1) * 4096 + (t & 1) * 32 + (32 * (m >> 1) + 2 * (m & 1)) * 64 + rbase);
                    WA[t][ks][2 * m] = r.x; WA[t][ks][2 * m + 1] = r.y;
                }
            asm volatile("s_waitcnt lgkmcnt(0)" ::: "memory");
        }
#pragma unroll
        for (int q = 0; q < 8; ++q) *(LAS v4u*)(img + q * 1024 + lane * 16) = so[q];
        asm volatile("s_waitcnt lgkmcnt(0)" ::: "memory");
#pragma unroll
        for (int t = 0; t < 2; ++t)
#pragma unroll
            for (int ks = 0; ks < 2; ++ks)
#pragma unroll
                for (int m = 0; m < 4; ++m) {
                    const v2u r = trrd(img + t * 32 + (64 * ks + 32 * (m >> 1) + 2 * (m & 1)) * 64 + rbase);
                    WO[t][ks][2 * m] = r.x; WO[t][ks][2 * m + 1] = r.y;
                }
    }
    const int tau = lane >> 5, rh = (lane >> 4) & 1, n16 = lane & 15, c8 = (lane >> 3) & 1, g4 = lane >> 4;
    const unsigned o_gm = amap(kw * 2048 + lane * 16);
    const unsigned qkvb = L_QKV + h * 6144;
    const int pcol = 256 * j + 32 * wave + 16 * tau + n16;
    const unsigned o_pp = amap((pcol >> 3) * 4096 + h * 128 + (rh * 8 + (pcol & 7)) * 8);
    const int sidx = c8 ? 0xEEEE : 0x4444;
    LAS unsigned char* const pw = lds + LC_PA + ((wave * 2 + rh) * 96 + 16 * tau + n16) * 16;
    LAS unsigned char* const ob = lds + LC_O + (lane & 7) * 320 + c8 * 16 + g4 * 32;
    const v4u rd = {(unsigned)(uintptr_t)p.ws, (unsigned)((uintptr_t)p.ws >> 32) & 0xffffu, 0xFFFFFF00u, 0x00020000u};
    {
        const v4u zz = {SENT, SENT, SENT, SENT};
        for (int s = wave; s < NSEG; s += 8) {
            const unsigned pg = P_HAND + (s >> 2), sb = pg * PAGE + (s & 3) * 524288u, sx = ((sidemask >> pg) & 1u) << 13;
            st16(zz, rs, amap(qkvb + j * 1024 + lane * 16) ^ sx, sb);
            unsigned o2 = amap(qkvb + 4096 + j * 512 + (lane & 31) * 16);
            if (lane >= 32) o2 = amap(cid * 128 + (lane & 7) * 16);
            if (lane >= 40 && (cid & 7) == 0) o2 = amap(L_LEAD + (cid >> 3) * 128);
            v4u iv = zz;
            if (s == 0 && lane >= 32) {
                iv = *(const v4u*)(p.mem0 + (lane & 7) * 1024 + cid * 8);
                iv = (v4u){nosent(iv.x), nosent(iv.y), nosent(iv.z), nosent(iv.w)};
            }
            st16(iv, rs, o2 ^ sx, sb);
        }
#pragma unroll
        for (int sl = 0; sl < 4; ++sl) st8((v2u){SENT, SENT}, rs, o_pp ^ (((sidemask >> (P_PART + sl)) & 1u) << 13), (P_PART + sl) * PAGE);
    }
    bool dead = false;
#define STAMP(k)
#define STAMPN(k)
#define ROLL(n)
#define NSP(k, v)
#define DUMP()
#define LATE(k, c)
#define XREC()
#define HAND_ADDR(s) do { const unsigned pg = P_HAND + ((s) >> 2); sb = pg * PAGE + ((s) & 3) * 524288u; sx = ((sidemask >> pg) & 1u) << 13; } while (0)
#define RETRY(K, RELOAD, BAD) do { unsigned spins = 0;                                                                    \
                while (!dead) { if (poll_fail(spins, ctl, tm.back)) { dead = true; break; } RELOAD; if (!(BAD)) break; }  \
                NSP(K, spins); } while (0)
#define ZERO_A() f32x4 c0 = {0, 0, 0, 0}, c1 = c0, c2 = c0, c3 = c0, c4 = c0, c5 = c0; PIN2(c0, c1); PIN2(c2, c3); PIN2(c4, c5)
#define GATHER_MEM(EXTRA, NX) v4u a0, a1;                                                                                 \
            ALD16(a0, o_gmx, sb, 0); ALD16(a1, o_gmx, sb, 1024); EXTRA; WAITV2(NX, a0, a1);                               \
            if (__builtin_expect(any_sent(mx4(a1, mx4(a0, 0u))), 0))                                                      \
                RETRY(0, ALD16(a0, o_gmx, sb, 0); ALD16(a1, o_gmx, sb, 1024); WAITV2(0, a0, a1), any_sent(mx4(a1, mx4(a0, 0u))))
#define QK_MFMA() do {                                                                                                    \
            c0 = smf(a0, WA[0][0], c0, sidx); c1 = smf(a0, WA[1][0], c1, sidx); c0 = smf(a1, WA[0][1], c0, sidx); c1 = smf(a1, WA[1][1], c1, sidx); \
            SB();                                                                                                         \
            c2 = smf(a0, WA[2][0], c2, sidx); c3 = smf(a0, WA[3][0], c3, sidx); c2 = smf(a1, WA[2][1], c2, sidx); c3 = smf(a1, WA[3][1], c3, sidx); \
            SB(); *(LAS f32x4*)pw = fold(c0, c1); SB(); *(LAS f32x4*)(pw + 512) = fold(c2, c3); SB(); } while (0)
#define V_PART() do { c4 = smf(a0, WA[4][0], c4, sidx); c5 = smf(a0, WA[5][0], c5, sidx); c4 = smf(a1, WA[4][1], c4, sidx); c5 = smf(a1, WA[5][1], c5, sidx); \
            *(LAS f32x4*)(pw + 1024) = fold(c4, c5); } while (0)
#define ZERO_D() f32x4 d0 = {0, 0, 0, 0}, d1 = d0; const unsigned pp = P_PART + (s & 3), px = ((sidemask >> pp) & 1u) << 13; unsigned o_ppx = o_pp ^ px; PIN2(d0, d1); asm volatile("" : "+v"(o_ppx))
#define WO_STAGE() do {                                                                                                   \
            const v4u o0 = *(LAS v4u*)ob, o1 = *(LAS v4u*)(ob + 128); SB();                                               \
            d0 = smf(o0, WO[0][0], d0, sidx); d1 = smf(o0, WO[1][0], d1, sidx); d0 = smf(o1, WO[0][1], d0, sidx); d1 = smf(o1, WO[1][1], d1, sidx); \
            const f32x4 f = fold(d0, d1);                                                                                 \
            st8((v2u){nosent(pk2(f.x, f.y)), nosent(pk2(f.z, f.w))}, rs, o_ppx, pp * PAGE); } while (0)
#define REDUCE4(LP, OFF) do {                                                                                             \
            const f32x4 r0 = *(LAS f32x4*)(LP), r1 = *(LAS f32x4*)((LP) + 3072), r2 = *(LAS f32x4*)((LP) + 6144), r3 = *(LAS f32x4*)((LP) + 9216); \
            const f32x4 t = (r0 + r1) + (r2 + r3);                                                                        \
            st4(nosent(pk2(sw32sum(t.x, t.z), sw32sum(t.y, t.w))), rs, (OFF), sb); } while (0)
    unsigned sb, sx;
    if (wave < 4) {
        const unsigned o_lead = amap(L_LEAD + n16 * 128);
        const unsigned o_gq = amap(qkvb + wave * 1024 + lane * 16);
        const unsigned o_gp = amap(cid * 4096 + lane * 16);
        const int crow = 4 * ((lane >> 2) & 1) + g4;
        v4u sel = {0u, 0u, 0u, 0u};
        sel[(lane & 7) >> 1] = (lane & 1) ? 0x3f800000u : 0x00003f80u;
        const unsigned o_pm = amap(cid * 128 + crow * 16 + (lane & 3) * 4);
        const unsigned o_gv = amap(qkvb + 4096 + wave * 512 + n16 * 16 + (4 * (g4 & 1) + 2 * (g4 >> 1)) * 2);
        LAS unsigned char* const qb = lds + LC_QK + (4 * g4 + ((lane & 15) >> 2)) * 32 + (lane & 1) * 8;
        LAS unsigned char* const ow = lds + LC_O + (lane & 7) * 320 + (32 * wave + 16 * c8 + 4 * g4) * 2;
        asm volatile("s_waitcnt vmcnt(0)" ::: "memory");
        BAR();
        if (wave == 0) {
            unsigned old = 0, plan;
            if (lane == 0) old = __hip_atomic_fetch_add(ctl + C_ARRIVE, 1u, RLX_AGENT);
            old = __builtin_amdgcn_readfirstlane(old);
            if (old == 127u) { plan = ((rt() + tm.t0) & ~3u) | 0x80000000u; if (lane == 0) __hip_atomic_store(ctl + C_T0, plan, RLX_AGENT); }
            else { unsigned spins = 0; for (;;) { plan = __builtin_amdgcn_readfirstlane(__hip_atomic_load(ctl + C_T0, RLX_AGENT)); if (plan) break; if (poll_fail(spins, ctl, 128)) { dead = true; break; } } }
            { const unsigned now = rt(); plan = now + ((int)((plan - now) << 1) >> 1); }
            if (lane == 0) *(LAS unsigned*)(lds + LC_MISC) = plan;
            asm volatile("s_waitcnt vmcnt(0)" ::: "memory");
            wait_l(plan);
        }
#define A_STAGE_A(FIRST)                                                                                                  \
            BAR();                                                                                  \
            STAMPN(0);                                                                                                    \
            GATHER_MEM(, 0);                                                                                              \
            STAMPN(1);                                                                                                    \
            unsigned lead = 0;                                                                                            \
            if (!(FIRST) && wave == 1) ALD4(lead, o_leadx, sb, 0);                                                        \
            QK_MFMA();                                                                                                    \
            BAR();                                                                                                 \
            STAMPN(2);                                                                                                    \
            V_PART();                                                                                                     \
            if (!(FIRST) && wave == 1) {                                                                                  \
                const unsigned aox = *(LAS unsigned*)(lds + LC_MISC);                                                     \
                WAITV1(0, lead);                                                                                          \
                if (__builtin_expect(any_sent(lead), 0)) RETRY(1, ALD4(lead, o_leadx, sb, 0); WAITV1(0, lead), any_sent(lead)); \
                int rel = max((int)((lead - aox) << 1) >> 1, 0);                                                          \
                rel = max(rel, __builtin_amdgcn_update_dpp(rel, rel, 0xB1, 0xf, 0xf, false));                             \
                rel = max(rel, __builtin_amdgcn_update_dpp(rel, rel, 0x4E, 0xf, 0xf, false));                             \
                rel = max(rel, __builtin_amdgcn_update_dpp(rel, rel, 0x124, 0xf, 0xf, false));                            \
                rel = max(rel, __builtin_amdgcn_update_dpp(rel, rel, 0x128, 0xf, 0xf, false));                            \
                if (lane == 0) *(LAS unsigned*)(lds + LC_MISC) = aox + rel;                                               \
            }                                                                                                             \
            BAR()
        {
            HAND_ADDR(0);
            ZERO_A();
            const unsigned o_gmx = o_gm ^ sx, o_leadx = 0;
            A_STAGE_A(true);
        }
        for (int s = 0; s < NSEG - 1; ++s) {
            __builtin_amdgcn_s_setprio(3);
            ROLL(3); STAMP(3);
            LAS unsigned char* kvb = lds + LC_KV + (s & 1) * 16384 + lane * 16;
            v4u kx[8], vx[2], gq;
            const unsigned gan = __builtin_amdgcn_readfirstlane(*(LAS unsigned*)(lds + LC_MISC));
            const unsigned now0 = rt(); const bool late0 = (int)(now0 - gan) > tm.tb2 + tm.tol; XREC(); LATE(0, late0);
            SB();
#pragma unroll
            for (int q = 0; q < 8; ++q) kx[q] = *(LAS v4u*)(kvb + q * 1024);
            vx[0] = *(LAS v4u*)(kvb + 8192 + wave * 2048); vx[1] = *(LAS v4u*)(kvb + 8192 + wave * 2048 + 1024);
            ZERO_D();
            unsigned o_gqx = o_gq ^ sx, o_gvx = o_gv ^ sx; PIN2(o_gqx, o_gvx);
            wait_l(late0 ? now0 + tm.dbig - 4 : gan + tm.ta);
            STAMP(4);
            ALD16(gq, o_gqx, sb, 0); WAITV1(0, gq);
            if (__builtin_expect(any_sent(mx4(gq, 0u)), 0)) RETRY(1, ALD16(gq, o_gqx, sb, 0); WAITV1(0, gq), any_sent(mx4(gq, 0u)));
            *(LAS v4u*)(lds + LC_QK + wave * 1024 + lane * 16) = gq;
            STAMP(5);
            BAR();
            {
                unsigned va, vc;
                ALD4(va, o_gvx, sb, 0); ALD4(vc, o_gvx, sb, 256);
                v4u qf[4], kf[4];
#pragma unroll
                for (int c = 0; c < 4; ++c) { const v2u qa = trrd(qb + c * 1024), qc = trrd(qb + c * 1024 + 512); qf[c] = (v4u){qa.x, qa.y, qc.x, qc.y}; }
#pragma unroll
                for (int c = 0; c < 4; ++c) { const v2u ka = trrd(qb + c * 1024 + 16), kc = trrd(qb + c * 1024 + 528); kf[c] = (v4u){ka.x, ka.y, kc.x, kc.y}; }
                SB();
                f32x4 s0 = {0, 0, 0, 0}, s1 = s0, sm = s0;
#pragma unroll
                for (int c = 0; c < 4; ++c) { s0 = mfma32(kx[c], qf[c], s0); s1 = mfma32(kx[4 + c], qf[c], s1); sm = mfma32(kf[c], qf[c], sm); }
                SB();
                float x0 = c8 ? s1.x : s0.x, x1 = c8 ? s1.y : s0.y, x2 = c8 ? s1.z : s0.z, x3 = c8 ? s1.w : s0.w;
                float xm = tau ? (c8 ? sm.w : sm.z) : (c8 ? sm.y : sm.x);
                constexpr float CS = SCALE * LOG2E;
                const float ml = -CS * max5x8(x0, x1, x2, x3, xm);
                STAMP(6);
                x0 = __builtin_amdgcn_exp2f(fmaf(x0, CS, ml)); x1 = __builtin_amdgcn_exp2f(fmaf(x1, CS, ml));
                x2 = __builtin_amdgcn_exp2f(fmaf(x2, CS, ml)); x3 = __builtin_amdgcn_exp2f(fmaf(x3, CS, ml));
                xm = __builtin_amdgcn_exp2f(fmaf(xm, CS, ml));
                const unsigned e01 = pk2(x0, x1), e23 = pk2(x2, x3), em = pk2(xm, xm);
                const v4u b1 = {c8 ? 0u : e01, c8 ? 0u : e23, c8 ? e01 : 0u, c8 ? e23 : 0u};
                unsigned b2 = em & (c8 ? 0xffff0000u : 0x0000ffffu);
                asm volatile("" : "+v"(b2));
                f32x4 o0 = {0, 0, 0, 0}, o1 = o0;
                o0 = mfma32(vx[0], b1, o0); o1 = mfma32(vx[1], b1, o1);
                SB();
                WAITV2(0, va, vc);
                if (__builtin_expect(any_sent(max(va, vc)), 0)) RETRY(1, ALD4(va, o_gvx, sb, 0); ALD4(vc, o_gvx, sb, 256); WAITV2(0, va, vc), any_sent(max(va, vc)));
                o0 = mfma16((v2u){va, 0u}, (v2u){b2, 0u}, o0); o1 = mfma16((v2u){vc, 0u}, (v2u){b2, 0u}, o1);
                SB();
                float l = ((x0 + x1) + (x2 + x3)) + xm;
                l += ror8(l); l = sw16sum(l, l); l = sw32sum(l, l);
                const float rinv = __builtin_amdgcn_rcpf(l);
                SB();
                float y[4];
#pragma unroll
                for (int e = 0; e < 4; ++e) y[e] = ((c8 ? o1[e] : o0[e]) + ror8(c8 ? o0[e] : o1[e])) * rinv;
                *(LAS v2u*)ow = (v2u){pk2(y[0], y[1]), pk2(y[2], y[3])};
            }
            BAR();
            WO_STAGE();
            STAMP(7);
            __builtin_amdgcn_s_setprio(0);
            HAND_ADDR(s + 1);
            ZERO_A();
            unsigned o_gmx = o_gm ^ sx, o_leadx = o_lead ^ sx; PIN2(o_gmx, o_leadx);
            if (__builtin_expect(wave == 0, 1)) {
                unsigned o_pmx = o_pm ^ sx, o_gpx = o_gp ^ px; PIN2(o_pmx, o_gpx);
                { const unsigned now = rt(), sch = gan + tm.tb; const int lt = (int)(now + tm.dmin3 - sch); LATE(1, lt > 0); wait_l(lt > tm.tol ? now + tm.dbig : (lt > 0 ? now + tm.dmin3 : sch)); }
                STAMP(8);
                v4u r0, r1, r2, r3; f32x4 u; unsigned m;
#define C_LOAD() ALD16(r0, o_gpx, pp * PAGE, 0); ALD16(r1, o_gpx, pp * PAGE, 1024); ALD16(r2, o_gpx, pp * PAGE, 2048); ALD16(r3, o_gpx, pp * PAGE, 3072);  \
                WAITV1(3, r0); u = mfma32(sel, r0, (f32x4){0, 0, 0, 0}); m = mx4(r0, 0u); SB(); WAITV1(2, r1); u = mfma32(sel, r1, u); m = mx4(r1, m); SB(); \
                WAITV1(1, r2); u = mfma32(sel, r2, u); m = mx4(r2, m); SB(); WAITV1(0, r3); u = mfma32(sel, r3, u); m = mx4(r3, m)
                C_LOAD();
                if (__builtin_expect(any_sent(m), 0)) RETRY(2, C_LOAD(), any_sent(m));
                float z0 = tau ? u.z : u.x, z1 = tau ? u.w : u.y;
                z0 += ror8(z0); z1 += ror8(z1);
                { auto sw = __builtin_amdgcn_permlane16_swap(__float_as_uint(z0), __float_as_uint(z1), false, false); z0 = __uint_as_float(sw[0]); z1 = __uint_as_float(sw[1]); }
                const unsigned w2 = nosent(pk2(z0, z1));
                if (c8 == 0) st4(w2, rs, o_pmx, sb);
                STAMP(9);
                const unsigned now = rt(), sch = gan + tm.tc;
                const int lt = (int)(now + tm.dmin - sch); LATE(2, lt > 0);
                const unsigned plan = lt > tm.tol ? now + tm.dbig1 : (lt > 0 ? now + tm.dmin : sch);
                if ((cid & 7) == 0 && lane == 0) st4(plan & 0x7fffffffu, rs, amap(L_LEAD + (cid >> 3) * 128) ^ sx, sb);
                if (lane == 0) *(LAS unsigned*)(lds + LC_MISC) = plan;
                DUMP();
                wait_l(plan);
            } else { DUMP(); }
            A_STAGE_A(false);
        }
    } else {
        const int hw = wave - 4;
        const int sl_d = lane & 7, sl_m = (lane >> 3) & 1, sl_r = (lane >> 4) & 1;
        LAS unsigned char* const l_qk = lds + LC_PA + tau * 12288 + (sl_r * 96 + sl_m * 32 + 8 * hw + sl_d) * 16;
        const unsigned o_pqk = amap(qkvb + j * 1024 + (8 * hw + sl_d) * 32 + sl_m * 16 + sl_r * 8 + tau * 4);
        LAS unsigned char* const l_v = lds + LC_PA + tau * 12288 + (rh * 96 + 64 + 16 * (hw & 1) + n16) * 16;
        const unsigned o_pv = amap(qkvb + 4096 + j * 512 + (16 * (hw & 1) + n16) * 16 + rh * 8 + tau * 4);
        const unsigned o_kv = (hw & 2 ? O_VX : O_KX) + h * 8192 + (hw & 1) * 4096 + lane * 16;
        LAS unsigned char* const l_kv = lds + LC_KV + hw * 4096 + lane * 16;
        v4u p0, p1, p2, p3;
        wait_ge(ctl + C_KV + 16 * h, 4u, ctl, dead);
        ALD16(p0, o_kv, 0, 0); ALD16(p1, o_kv, 0, 1024); ALD16(p2, o_kv, 0, 2048); ALD16(p3, o_kv, 0, 3072);
        WAITV2(0, p0, p1); WAITV2(0, p2, p3);
        BAR();
        *(LAS v4u*)l_kv = p0; *(LAS v4u*)(l_kv + 1024) = p1; *(LAS v4u*)(l_kv + 2048) = p2; *(LAS v4u*)(l_kv + 3072) = p3;
        __builtin_amdgcn_s_setprio(2);
        for (int s = -1; s < NSEG - 1; ++s) {
            if (s >= 0) {
                ROLL(7);
                ZERO_D();
                BAR();
                __builtin_amdgcn_s_sleep(3);
                if (((s + 1) & 7) == 0) wait_ge(ctl + C_KV + 16 * (((s + 1) >> 3) * 32 + h), 2u, ctl, dead);
                ALD16(p0, o_kv, (s + 1) * 262144u, 0); ALD16(p1, o_kv, (s + 1) * 262144u, 1024); ALD16(p2, o_kv, (s + 1) * 262144u, 2048); ALD16(p3, o_kv, (s + 1) * 262144u, 3072);
                {
                    const unsigned rp = P_PART + ((s + 2) & 3), rx = ((sidemask >> rp) & 1u) << 13;
                    st8((v2u){SENT, SENT}, rs, o_pp ^ rx, rp * PAGE); st8((v2u){SENT, SENT}, rs, (o_pp - 8 * 16384) ^ rx, rp * PAGE);
                }
                STAMP(7); STAMP(8);
                BAR();
                STAMP(9);
                WO_STAGE();
                STAMP(10);
                DUMP();
                if (j == 0 && tid == 256) __hip_atomic_store(ctl + C_PROG + 32 * h, (unsigned)(s + 1), RLX_AGENT);
                WAITV2(0, p0, p1); WAITV2(0, p2, p3);
                LAS unsigned char* kw = l_kv + ((s + 1) & 1) * 16384;
                *(LAS v4u*)kw = p0; *(LAS v4u*)(kw + 1024) = p1; *(LAS v4u*)(kw + 2048) = p2; *(LAS v4u*)(kw + 3072) = p3;
            }
            HAND_ADDR(s + 1);
            ZERO_A();
            unsigned o_pqkx = o_pqk ^ sx, o_gmx = o_gm ^ sx; PIN2(o_pqkx, o_gmx);
            BAR();
            STAMPN(0);
            GATHER_MEM(, 0);
            STAMPN(1);
            QK_MFMA();
            STAMPN(2);
            BAR();
            STAMPN(3);
            REDUCE4(l_qk, o_pqkx);
            SB();
            STAMPN(4);
            V_PART();
            STAMPN(5);
            BAR();
            STAMPN(6);
            if (hw < 2) REDUCE4(l_v, o_pv ^ sx);
        }
        if (j == 0 && tid == 256) __hip_atomic_store(ctl + C_PROG + 32 * h, (unsigned)NSEG, RLX_AGENT);
    }
}

DEVI int claim_slot(GAS unsigned* ctl) {
    const unsigned x = xcc_id() & 7u;
    const unsigned r = __hip_atomic_fetch_add(ctl + C_XR + 32 * x, 1u, RLX_AGENT);
    if (r < 32 && __hip_atomic_exchange(ctl + C_SLOT + x * 32 + r, 1u, RLX_AGENT) == 0u) return (int)(x * 32 + r);
    for (int k = 255;; k = (k + 255) & 255) if (__hip_atomic_exchange(ctl + C_SLOT + k, 1u, RLX_AGENT) == 0u) return k;
}

DEVI void glds(unsigned voff, const void* sbase, unsigned ldsaddr) {
    unsigned keep;
    asm volatile("s_mov_b32 %0, m0\n\ts_mov_b32 m0, %3\n\ts_nop 0\n\tglobal_load_lds_dwordx4 %1, %2\n\ts_mov_b32 m0, %0" : "=&s"(keep) : "v"(voff), "s"(sbase), "s"(ldsaddr) : "memory");
}
template <int BM, int WM, bool TR, bool PERMK>
struct Gemm {
    static constexpr int WN = 8 / WM, MT = BM / WM / 16, NT = 128 / WN / 16, NA = BM / 64, STAGE = BM * 128 + 16384;
    f32x4 acc[MT][NT];
    DEVI void run(LAS unsigned char* lds, const u16* A, int lda, const u16* B, int ldb, int nk) {
        const int tid = otid(), lane = tid & 63, wave = __builtin_amdgcn_readfirstlane(tid >> 6);
        const int wm = wave / WN, wn = wave % WN;
        unsigned va[NA], vb[2];
#pragma unroll
        for (int q = 0; q < NA; ++q) va[q] = ((wave * NA * 8 + q * 8 + (lane >> 3)) * lda + (((lane & 7) ^ ((lane >> 3) & 7)) * 8)) * 2;
#pragma unroll
        for (int q = 0; q < 2; ++q) {
            const int k = wave * 8 + (lane >> 4) + 4 * q;
            const int pk = PERMK ? ((k & 32) + 16 * ((k >> 2) & 1) + 4 * ((k >> 3) & 3) + (k & 3)) : k;
            const int f = (k & 3) | (((k >> 3) & 1) << 2);
            vb[q] = (pk * ldb + ((((lane & 15) >> 1) ^ f) * 16 + (lane & 1) * 8)) * 2;
        }
        const unsigned lbase = (unsigned)(uintptr_t)lds;
        auto stage = [&](int kt, int buf) {
            const unsigned la = lbase + buf * STAGE + wave * NA * 1024, lb = lbase + buf * STAGE + BM * 128 + wave * 2048;
            const u16* ga = A + kt * 64; const u16* gb = B + (size_t)kt * 64 * ldb;
#pragma unroll
            for (int q = 0; q < NA; ++q) glds(va[q], ga, la + q * 1024);
            glds(vb[0], gb, lb); glds(vb[1], gb, lb + 1024);
        };
        const int fr = lane & 15, fg = lane >> 4;
        const unsigned ra = (wm * (BM / WM) + fr) * 128;
        const unsigned ra0 = ra + ((fg ^ (fr & 7)) * 16), ra1 = ra + (((4 + fg) ^ (fr & 7)) * 16);
        const int q4 = fr >> 2, p4 = fr & 3, fsw = q4 | ((fg & 1) << 2);
        unsigned rb[NT];
#pragma unroll
        for (int n = 0; n < NT; ++n) rb[n] = BM * 128 + (8 * fg + q4) * 256 + p4 * 8 + (((wn * NT + n) ^ fsw) * 32);
        v4u a0[MT], b0[NT], a1[MT], b1[NT];
        auto frags = [&](LAS unsigned char* sp, int ks, v4u* af, v4u* bf) {
#pragma unroll
            for (int n = 0; n < NT; ++n) { const v2u x = trrd(sp + rb[n] + ks * 8192), y = trrd(sp + rb[n] + ks * 8192 + 1024); bf[n] = (v4u){x.x, x.y, y.x, y.y}; }
#pragma unroll
            for (int m = 0; m < MT; ++m) af[m] = *(LAS v4u*)(sp + (ks ? ra1 : ra0) + m * 2048);
        };
        auto mma = [&](const v4u* af, const v4u* bf) {
#pragma unroll
            for (int m = 0; m < MT; ++m)
#pragma unroll
                for (int n = 0; n < NT; ++n) acc[m][n] = TR ? mfma32(bf[n], af[m], acc[m][n]) : mfma32(af[m], bf[n], acc[m][n]);
        };
#pragma unroll
        for (int m = 0; m < MT; ++m)
#pragma unroll
            for (int n = 0; n < NT; ++n) acc[m][n] = (f32x4){0, 0, 0, 0};
        stage(0, 0); stage(1, 1); stage(2, 2);
        asm volatile("s_waitcnt vmcnt(%0)\n\ts_barrier" :: "n"(2 * (NA + 2)) : "memory");
        frags(lds, 0, a0, b0);
        int buf = 0;
        for (int kt = 0; kt < nk; ++kt) {
            LAS unsigned char* sp = lds + buf * STAGE;
            const int nb = buf == 2 ? 0 : buf + 1;
            frags(sp, 1, a1, b1);
            SB();
            mma(a0, b0);
            SB();
            if (kt + 2 < nk) asm volatile("s_waitcnt vmcnt(%0) lgkmcnt(0)\n\ts_barrier" :: "n"(NA + 2) : "memory");
            else asm volatile("s_waitcnt vmcnt(0) lgkmcnt(0)\n\ts_barrier" ::: "memory");
            if (kt + 3 < nk) stage(kt + 3, buf);
            if (kt + 1 < nk) frags(lds + nb * STAGE, 0, a0, b0);
            SB();
            mma(a1, b1);
            SB();
            buf = nb;
        }
    }
};
DEVI v4u pk8(f32x4 a, f32x4 b) { return (v4u){pk2(a.x, a.y), pk2(a.z, a.w), pk2(b.x, b.y), pk2(b.z, b.w)}; }
DEVI void announce(GAS unsigned* w) {
    asm volatile("s_waitcnt vmcnt(0) lgkmcnt(0)\n\ts_barrier" ::: "memory");
    if (otid() == 0) __hip_atomic_fetch_add(w, 1u, RLX_AGENT);
}

template <int BM>
DEVI void kv_unit(const Params& p, LAS unsigned char* lds, int r0, int hh, int mat) {
    const int lane = otid() & 63, wave = __builtin_amdgcn_readfirstlane(otid() >> 6), wm = wave >> 1, wn = wave & 1;
    constexpr int MT = BM / 64;
    const __amdgpu_buffer_rsrc_t rs = __builtin_amdgcn_make_buffer_rsrc(p.ws, 0, 0xFFFFFF00u, 0x00020000);
    const int row0 = r0 + wm * (BM / 4);
    if (mat == 0) {
        Gemm<BM, 4, true, false> g; g.run(lds, p.x + (size_t)r0 * D_, D_, p.wk + hh * 128, 4096, 16);
#pragma unroll
        for (int m = 0; m < MT; ++m)
#pragma unroll
            for (int c = 0; c < 2; ++c) {
                const int row = row0 + m * 16;
                st16(pk8(g.acc[m][2 * c], g.acc[m][2 * c + 1]), rs, lane * 16, O_KX + ((row >> 5) * 32 + hh) * 8192 + (((row >> 4) & 1) * 4 + wn * 2 + c) * 1024);
            }
    } else {
        Gemm<BM, 4, false, false> g; g.run(lds, p.x + (size_t)r0 * D_, D_, p.wv + hh * 128, 4096, 16);
#pragma unroll
        for (int m = 0; m < MT; m += 2)
#pragma unroll
            for (int n = 0; n < 4; ++n) {
                const int row = row0 + m * 16;
                st16(pk8(g.acc[m][n], g.acc[m + 1][n]), rs, lane * 16, O_VX + ((row >> 5) * 32 + hh) * 8192 + (wn * 4 + n) * 1024);
            }
    }
    announce((GAS unsigned*)(p.ws + O_CTL) + C_KV + 16 * ((r0 >> 8) * 32 + hh));
}

DEVI void qa_unit(const Params& p, LAS unsigned char* lds, int panel, int hh, unsigned sidemask, bool& dead) {
    const int lane = otid() & 63, wave = __builtin_amdgcn_readfirstlane(otid() >> 6);
    const int g4 = lane >> 4, n16 = lane & 15;
    const __amdgpu_buffer_rsrc_t rs = __builtin_amdgcn_make_buffer_rsrc(p.ws, 0, 0xFFFFFF00u, 0x00020000);
    GAS unsigned* ctl = (GAS unsigned*)(p.ws + O_CTL);
    v4u qf[2][4];
    {
        Gemm<256, 8, true, false> g; g.run(lds, p.x + (size_t)panel * 256 * D_, D_, p.wq + hh * 128, 4096, 16);
#pragma unroll
        for (int m = 0; m < 2; ++m)
#pragma unroll
            for (int c = 0; c < 4; ++c) qf[m][c] = pk8(g.acc[m][2 * c], g.acc[m][2 * c + 1]);
    }
    const int seg = panel * 8 + wave;
    const unsigned kvo = (seg * 32 + hh) * 8192;
    wait_ge(ctl + C_KV + 16 * (panel * 32 + hh), panel ? 2u : 4u, ctl, dead);
    v4u kx[8], vx[8];
#pragma unroll
    for (int q = 0; q < 8; ++q) kx[q] = ld16(rs, lane * 16 + q * 1024, O_KX + kvo);
#pragma unroll
    for (int q = 0; q < 8; ++q) vx[q] = ld16(rs, lane * 16 + q * 1024, O_VX + kvo);
    f32x4 z = {0, 0, 0, 0};
    f32x4 s00 = z, s10 = z, s11 = z, sm0 = z, sm1 = z;
#pragma unroll
    for (int c = 0; c < 4; ++c) { s00 = mfma32(kx[c], qf[0][c], s00); s10 = mfma32(kx[c], qf[1][c], s10); s11 = mfma32(kx[4 + c], qf[1][c], s11); }
    LAS unsigned char* img = lds + wave * 6144;
    wait_ge(ctl + C_PROG + 32 * hh, (unsigned)(seg + 1), ctl, dead);
    {
        const unsigned pg = P_HAND + (seg >> 2), sb = pg * PAGE + (seg & 3) * 524288u, sx = ((sidemask >> pg) & 1u) << 13;
        v4u t[6];
        for (unsigned spins = 0;;) {
            unsigned m = 0;
#pragma unroll
            for (int q = 0; q < 6; ++q) t[q] = ld16(rs, amap(L_QKV + hh * 6144 + q * 1024 + lane * 16) ^ sx, sb);
#pragma unroll
            for (int q = 0; q < 6; ++q) m = mx4(t[q], m);
            if (!any_sent(m) || dead) break;
            if (poll_fail(spins, ctl, 128)) dead = true;
            asm volatile("" ::: "memory");
        }
#pragma unroll
        for (int q = 0; q < 6; ++q) *(LAS v4u*)(img + q * 1024 + lane * 16) = t[q];
        asm volatile("s_waitcnt lgkmcnt(0)" ::: "memory");
    }
    LAS unsigned char* kb = img + (4 * g4 + (n16 >> 2)) * 32 + 16 + (lane & 1) * 8;
#pragma unroll
    for (int c = 0; c < 4; ++c) {
        const v2u ka = trrd(kb + c * 1024), kc = trrd(kb + c * 1024 + 512);
        const v4u kf = {ka.x, ka.y, kc.x, kc.y};
        sm0 = mfma32(kf, qf[0][c], sm0); sm1 = mfma32(kf, qf[1][c], sm1);
    }
    v4u b1[2]; v2u b2[2];
#pragma unroll
    for (int m = 0; m < 2; ++m) {
        float v[12];
#pragma unroll
        for (int e = 0; e < 4; ++e) {
            const bool diag = 4 * g4 + e <= n16;
            v[e] = rnd(rnd(m ? s10[e] : s00[e]) * SCALE); if (m == 0 && !diag) v[e] = -INFINITY;
            v[4 + e] = (m && diag) ? rnd(rnd(s11[e]) * SCALE) : -INFINITY;
            v[8 + e] = g4 < 2 ? rnd(rnd(m ? sm1[e] : sm0[e]) * SCALE) : -INFINITY;
        }
        float mx = v[0];
#pragma unroll
        for (int e = 1; e < 12; ++e) mx = fmaxf(mx, v[e]);
        mx = sw16max(mx); mx = sw32max(mx);
        const float ml = -mx * LOG2E; float l = 0.f;
#pragma unroll
        for (int e = 0; e < 12; ++e) { v[e] = __builtin_amdgcn_exp2f(fmaf(v[e], LOG2E, ml)); l += v[e]; }
        l = sw16sum(l, l); l = sw32sum(l, l);
        const float ri = 1.f / l;
        b1[m] = (v4u){pk2(v[0] * ri, v[1] * ri), pk2(v[2] * ri, v[3] * ri), pk2(v[4] * ri, v[5] * ri), pk2(v[6] * ri, v[7] * ri)};
        b2[m] = (v2u){pk2(v[8] * ri, v[9] * ri), pk2(v[10] * ri, v[11] * ri)};
    }
#pragma unroll
    for (int c = 0; c < 4; ++c) {
        f32x4 o[2][2];
#pragma unroll
        for (int t = 0; t < 2; ++t) {
            const v2u va = *(LAS v2u*)(img + 4096 + ((2 * c + t) * 16 + n16) * 16 + (g4 & 1) * 8);
#pragma unroll
            for (int m = 0; m < 2; ++m) { o[m][t] = mfma32(vx[2 * c + t], b1[m], z); o[m][t] = mfma16(va, b2[m], o[m][t]); }
        }
#pragma unroll
        for (int m = 0; m < 2; ++m) st16(pk8(o[m][0], o[m][1]), rs, (n16 * 4096 + 8 * g4) * 2, O_OX + ((seg * 32 + m * 16) * 4096 + hh * 128 + c * 32) * 2);
    }
    asm volatile("s_waitcnt vmcnt(0)" ::: "memory");
    if (lane == 0) __hip_atomic_fetch_add(ctl + C_OC + 16 * ((seg >> 2) * 4 + (hh >> 3)), 1u, RLX_AGENT);
}

DEVI void top2(const float* R, int& i0, int& i1) {
    const int lane = otid() & 63;
    float b0 = -INFINITY, b1 = -INFINITY; int j0 = 0x7fffffff, j1 = 0x7fffffff;
#pragma unroll 8
    for (int k = lane; k < S_; k += 64) {
        const float v = R[k];
        if (v > b0 || j0 == 0x7fffffff) { b1 = b0; j1 = j0; b0 = v; j0 = k; } else if (v > b1 || j1 == 0x7fffffff) { b1 = v; j1 = k; }
    }
    for (int m = 1; m < 64; m <<= 1) {
        const float c0 = __shfl_xor(b0, m), c1 = __shfl_xor(b1, m); const int k0 = __shfl_xor(j0, m), k1 = __shfl_xor(j1, m);
        auto gt = [](float a, int ja, float b, int jb) { return a > b || (!(b > a) && ja < jb); };
        if (gt(c0, k0, b0, j0)) { if (gt(b0, j0, c1, k1)) { b1 = b0; j1 = j0; } else { b1 = c1; j1 = k1; } b0 = c0; j0 = k0; }
        else if (gt(c0, k0, b1, j1)) { b1 = c0; j1 = k0; }
    }
    i0 = __builtin_amdgcn_readfirstlane(j0); i1 = __builtin_amdgcn_readfirstlane(j1);
}
DEVI void ffn1_unit(const Params& p, LAS unsigned char* lds, int u, int i0, int i1) {
    const int tid = otid(), co = tid & 7, ksl = tid >> 3, col = u * 64 + co * 8;
    LAS float* red = (LAS float*)lds;
    float a[4][8] = {};
#pragma unroll 4
    for (int k = ksl; k < D_; k += 64) {
        const v4u wg = *(const v4u*)(p.wg + (size_t)k * F_ + col), wu = *(const v4u*)(p.wu + (size_t)k * F_ + col);
        const float x0 = bf2f(p.x[i0 * D_ + k]), x1 = bf2f(p.x[i1 * D_ + k]);
#pragma unroll
        for (int e = 0; e < 4; ++e) {
            a[0][2 * e] += x0 * lo16(wg[e]); a[0][2 * e + 1] += x0 * hi16(wg[e]); a[1][2 * e] += x1 * lo16(wg[e]); a[1][2 * e + 1] += x1 * hi16(wg[e]);
            a[2][2 * e] += x0 * lo16(wu[e]); a[2][2 * e + 1] += x0 * hi16(wu[e]); a[3][2 * e] += x1 * lo16(wu[e]); a[3][2 * e + 1] += x1 * hi16(wu[e]);
        }
    }
#pragma unroll
    for (int q = 0; q < 4; ++q)
#pragma unroll
        for (int e = 0; e < 8; ++e) red[(ksl * 4 + q) * 64 + co * 8 + e] = a[q][e];
    BAR();
    if (tid < 128) {
        const int t = tid >> 6, c = tid & 63; float g = 0.f, uu = 0.f;
        for (int k = 0; k < 64; ++k) { g += red[(k * 4 + t) * 64 + c]; uu += red[(k * 4 + 2 + t) * 64 + c]; }
        g = rnd(g); uu = rnd(uu); g = rnd(g / (1.f + __expf(-g)));
        __hip_atomic_store((GAS float*)(p.ws + O_FH) + t * F_ + u * 64 + c, rnd(g * uu), RLX_AGENT);
    }
    announce((GAS unsigned*)(p.ws + O_CTL) + C_F1);
}
DEVI void ffn2_unit(const Params& p, LAS unsigned char* lds, int u, bool& dead) {
    const int tid = otid(), co = tid & 3, ksl = tid >> 2, col = u * 32 + co * 8;
    GAS unsigned* ctl = (GAS unsigned*)(p.ws + O_CTL);
    LAS float* red = (LAS float*)lds;
    v4u w[28];
#pragma unroll
    for (int q = 0; q < 28; ++q) w[q] = *(const v4u*)(p.wd + (size_t)(ksl + q * 128) * D_ + col);
    wait_ge(ctl + C_F1, 56u, ctl, dead);
    GAS float* hb = (GAS float*)(p.ws + O_FH);
    float a[2][8] = {};
#pragma unroll
    for (int q = 0; q < 28; ++q) {
        const float x0 = __hip_atomic_load(hb + ksl + q * 128, RLX_AGENT), x1 = __hip_atomic_load(hb + F_ + ksl + q * 128, RLX_AGENT);
#pragma unroll
        for (int e = 0; e < 4; ++e) { a[0][2 * e] += x0 * lo16(w[q][e]); a[0][2 * e + 1] += x0 * hi16(w[q][e]); a[1][2 * e] += x1 * lo16(w[q][e]); a[1][2 * e + 1] += x1 * hi16(w[q][e]); }
    }
#pragma unroll
    for (int q = 0; q < 2; ++q)
#pragma unroll
        for (int e = 0; e < 8; ++e) red[(ksl * 2 + q) * 32 + co * 8 + e] = a[q][e];
    BAR();
    if (tid < 64) {
        const int t = tid >> 5, c = tid & 31; float y = 0.f;
        for (int k = 0; k < 128; ++k) y += red[(k * 2 + t) * 32 + c];
        __hip_atomic_store((GAS float*)(p.ws + O_FY) + t * D_ + u * 32 + c, rnd(y), RLX_AGENT);
    }
    announce(ctl + C_F2);
}

DEVI void wo_unit(const Params& p, LAS unsigned char* lds, int rbk, int ct, int kq, int i0, int i1, bool& dead) {
    const int tid = otid(), lane = tid & 63, wave = __builtin_amdgcn_readfirstlane(tid >> 6), wm = wave >> 1, wn = wave & 1;
    GAS unsigned* ctl = (GAS unsigned*)(p.ws + O_CTL);
    const __amdgpu_buffer_rsrc_t rs = __builtin_amdgcn_make_buffer_rsrc(p.ws, 0, 0xFFFFFF00u, 0x00020000);
    const int r0 = rbk * 128, c0 = ct * 128, tile = rbk * 8 + ct;
    if (wave == 0) {
        wait_ge(ctl + C_OC + 16 * (rbk * 4 + kq), 32u, ctl, dead);
        __builtin_amdgcn_fence(__ATOMIC_ACQUIRE, "agent");
        asm volatile("s_waitcnt vmcnt(0)" ::: "memory");
    }
    BAR();
    Gemm<128, 4, true, true> g; g.run(lds, (const u16*)(p.ws + O_OX) + (size_t)r0 * 4096 + kq * 1024, 4096, p.wo + (size_t)kq * 1024 * D_ + c0, 1024, 16);
    const unsigned slab = O_SLAB + tile * 262144u;
#pragma unroll
    for (int m = 0; m < 2; ++m)
#pragma unroll
        for (int n = 0; n < 4; ++n) st16(__builtin_bit_cast(v4u, g.acc[m][n]), rs, tid * 16 + (m * 4 + n) * 8192, slab + kq * 65536u);
    asm volatile("s_waitcnt vmcnt(0) lgkmcnt(0)\n\ts_barrier" ::: "memory");
    if (tid == 0) *(LAS unsigned*)lds = __hip_atomic_fetch_add(ctl + C_TK + 16 * tile, 1u, RLX_AGENT);
    BAR();
    const unsigned tk = __builtin_amdgcn_readfirstlane(*(LAS unsigned*)lds);
    if (tk != 3u) return;
    const float* R = (const float*)(p.ws + O_R); const float* G = (const float*)(p.ws + O_G); GAS float* Y = (GAS float*)(p.ws + O_FY);
    if ((i0 >> 7) == rbk || (i1 >> 7) == rbk) wait_ge(ctl + C_F2, 32u, ctl, dead);
#pragma unroll
    for (int m = 0; m < 2; ++m) {
        f32x4 s[4][4];
#pragma unroll
        for (int q = 0; q < 4; ++q)
#pragma unroll
            for (int n = 0; n < 4; ++n) s[q][n] = __builtin_bit_cast(f32x4, ld16(rs, tid * 16 + (m * 4 + n) * 8192, slab + q * 65536u));
        const int row = r0 + wm * 32 + m * 16 + (lane & 15);
        const float gv = G[row], og = rnd(1.f - gv);
        const bool sel = row == i0 || row == i1;
        const float rs_ = sel ? rnd(1.f / (1.f + __expf(-R[row]))) : 0.f;
#pragma unroll
        for (int n = 0; n < 4; ++n) {
            const f32x4 a = (s[0][n] + s[1][n]) + (s[2][n] + s[3][n]);
            const int col = c0 + wn * 64 + n * 16 + (lane >> 4) * 4;
            const v2u xv = *(const v2u*)(p.x + row * D_ + col);
            float b[4] = {lo16(xv.x), hi16(xv.x), lo16(xv.y), hi16(xv.y)}, o[4];
#pragma unroll
            for (int e = 0; e < 4; ++e) {
                if (sel) b[e] = rnd(b[e] + rnd(__hip_atomic_load(Y + (row == i0 ? 0 : D_) + col + e, RLX_AGENT) * rs_));
                o[e] = rnd(gv * rnd(a[e])) + rnd(og * b[e]);
            }
            *(v2u*)(p.out + row * D_ + col) = (v2u){pk2(o[0], o[1]), pk2(o[2], o[3])};
        }
    }
}

constexpr int Q_F1 = 64, Q_F2 = Q_F1 + 56, Q_QA = Q_F2 + 32, Q_END = Q_QA + 8 * 32 + 8 * 64;
DEVI void worker_body(const Params& p, LAS unsigned char* lds, int w) {
    const int tid = otid(), lane = tid & 63;
    GAS unsigned* ctl = (GAS unsigned*)(p.ws + O_CTL);
    const unsigned sv = lane < 21 ? (ctl[C_SIDE + lane] & 1u) : 0u;
    const unsigned sidemask = (unsigned)__builtin_amdgcn_ballot_w64(sv != 0);
    bool dead = false;
    const int hm = (w >> 5) * 16 + ((w & 31) >> 1);
    kv_unit<128>(p, lds, (w & 1) * 128, hm >> 1, hm & 1);
    for (int r = 0; r < 3; ++r) kv_unit<256>(p, lds, (1 + 2 * r + (w & 1)) * 256, hm >> 1, hm & 1);
    int i0, i1; top2((const float*)(p.ws + O_R), i0, i1);
    for (;;) {
        BAR();
        if (tid == 0) *(LAS unsigned*)(lds + 147456) = __hip_atomic_fetch_add(ctl + C_QUEUE, 1u, RLX_AGENT);
        BAR();
        int t = __builtin_amdgcn_readfirstlane(*(LAS unsigned*)(lds + 147456));
        if (t >= Q_END) break;
        if (t < Q_F1) kv_unit<256>(p, lds, 7 * 256, t >> 1, t & 1);
        else if (t < Q_F2) ffn1_unit(p, lds, t - Q_F1, i0, i1);
        else if (t < Q_QA) ffn2_unit(p, lds, t - Q_F2, dead);
        else {
            t -= Q_QA;
            int qp = -1;
            if (t < 64) qp = t >> 5;
            else { t -= 64; const int pp = min(t / 96, 6), r = t - pp * 96; if (pp < 6 && r >= 64) { qp = pp + 2; t = r - 64; } else t = pp * 64 + r; }
            if (qp >= 0) qa_unit(p, lds, qp, t & 31, sidemask, dead);
            else wo_unit(p, lds, t >> 5, (t >> 2) & 7, t & 3, i0, i1, dead);
        }
    }
}

__global__ void __launch_bounds__(512, 2) k_main(Params p, Tim tm) {
    extern __shared__ __attribute__((aligned(16))) unsigned char smem[];
    LAS unsigned char* lds = (LAS unsigned char*)smem;
    if (threadIdx.x == 0) *(LAS int*)(lds + 147456) = claim_slot((GAS unsigned*)(p.ws + O_CTL));
    __syncthreads();
    const int slot = __builtin_amdgcn_readfirstlane(*(LAS int*)(lds + 147456));
    if (slot < 128) chain_body(p, tm, lds, slot >> 5, slot & 31);
    else worker_body(p, lds, slot - 128);
}

#define TIM_INIT {10, 12, 20, 30, 8, 24, 200, 152, -2000, 8, 40, -1000}
extern "C" void kernel_launch(void* const* d_in, const int* in_sizes, int n_in, void* d_out, int out_size, void* d_ws, size_t ws_size, hipStream_t stream) {
    Params p;
    p.x = (const u16*)d_in[0]; p.mem0 = (const u16*)d_in[1]; p.wq = (const u16*)d_in[2]; p.wk = (const u16*)d_in[3]; p.wv = (const u16*)d_in[4]; p.wo = (const u16*)d_in[5];
    p.wr = (const u16*)d_in[8]; p.wg = (const u16*)d_in[9]; p.wu = (const u16*)d_in[10]; p.wd = (const u16*)d_in[11]; p.gw = (const u16*)d_in[12];
    p.out = (u16*)d_out;
    p.ws = (unsigned char*)(((uintptr_t)d_ws + PAGE - 1) & ~(uintptr_t)(PAGE - 1));
    Tim tm = TIM_INIT;
    constexpr int LDS_M = 147456 + 64;
    (void)hipFuncSetAttribute((const void*)k_main, hipFuncAttributeMaxDynamicSharedMemorySize, LDS_M);
    hipLaunchKernelGGL(k_prep, dim3(256), dim3(256), 0, stream, p);
    hipLaunchKernelGGL(k_main, dim3(256), dim3(512), LDS_M, stream, p, tm);
}
```

```cpp
#include <hip/hip_runtime.h>
#include <hip/hip_bf16.h>
#include <stdint.h>

typedef unsigned short u16;
typedef unsigned v4u __attribute__((ext_vector_type(4)));
typedef unsigned v2u __attribute__((ext_vector_type(2)));
typedef unsigned v8u __attribute__((ext_vector_type(8)));
typedef float f32x4 __attribute__((ext_vector_type(4)));
typedef __bf16 bf16x2 __attribute__((ext_vector_type(2)));
typedef __bf16 bf16x8 __attribute__((ext_vector_type(8)));
typedef __bf16 bf16x16 __attribute__((ext_vector_type(16)));
typedef short s16x4 __attribute__((ext_vector_type(4)));
#define GAS __attribute__((address_space(1)))
#define LAS __attribute__((address_space(3)))
#define RLX_AGENT __ATOMIC_RELAXED, __HIP_MEMORY_SCOPE_AGENT
#define DEVI __device__ __forceinline__
#define BAR() asm volatile("s_waitcnt lgkmcnt(0)\n\ts_barrier" ::: "memory")

constexpr int S_ = 2048, D_ = 1024, H_ = 32, DH = 128, NSEG = 64, F_ = 3584;
constexpr float SCALE = 0.08838834764831845f, LOG2E = 1.4426950408889634f;
constexpr unsigned SENT = 0xFFFFFFFFu;
constexpr unsigned PAGE = 2u << 20;
constexpr unsigned O_CTL = 0, O_R = 65536, O_G = O_R + 8192, O_FH = 131072, O_FY = O_FH + 32768;
constexpr unsigned P_HAND = 1, P_PART = 17;
constexpr unsigned O_KX = 21 * PAGE, O_VX = 29 * PAGE, O_OX = 37 * PAGE, O_SLAB = 45 * PAGE  , O_S2 = 61 * PAGE  , O_DBG = 73 * PAGE;
constexpr unsigned L_QKV = 65536;
constexpr int C_ABORT = 0, C_ARRIVE = 64, C_T0 = 128, C_QUEUE = 192, C_XR = 256  , C_F1 = 768, C_F2 = 800, C_INIT = 832, C_SLOT = 1024  ,
              C_KV = 2048  , C_PROG = 6144  , C_OC = 7168  , C_TK = 8192  , C_OF = 10240  , C_ZEND = 13312,
              C_SIDE = 13312  ;
constexpr int SF0 = 40;

struct Params { const u16 *x, *mem0, *wq, *wk, *wv, *wo, *wr, *wg, *wu, *wd, *gw; u16* out; unsigned char* ws; };
struct Tim { int ta, tb, tc, m1, mq, m3, back, t0, qs, mr; };

DEVI float bf2f(u16 b) { return __uint_as_float((unsigned)b << 16); }
DEVI unsigned pk2(float lo, float hi) { bf16x2 v = {(__bf16)lo, (__bf16)hi}; return __builtin_bit_cast(unsigned, v); }
DEVI float rnd(float f) { return __uint_as_float(pk2(f, 0.f) << 16); }
DEVI float lo16(unsigned u) { return __uint_as_float(u << 16); }
DEVI float hi16(unsigned u) { return __uint_as_float(u & 0xffff0000u); }
DEVI int otid() { int t = threadIdx.x; asm volatile("" : "+v"(t)); return t; }
DEVI unsigned xcc_id() { return (unsigned)__builtin_amdgcn_s_getreg(20 | (3 << 11)) & 0xfu; }
DEVI unsigned rt() { return (unsigned)__builtin_amdgcn_s_memrealtime(); }
DEVI unsigned amap(unsigned lo) { return ((lo >> 13) << 14) + (lo & 8191); }
DEVI v4u ld16(__amdgpu_buffer_rsrc_t r, unsigned vo, unsigned so) { return __builtin_amdgcn_raw_buffer_load_b128(r, vo, so, 16); }
DEVI void st16(v4u v, __amdgpu_buffer_rsrc_t r, unsigned vo, unsigned so) {
    __builtin_amdgcn_raw_buffer_store_b128(v, r, vo, so, 16);
    asm volatile("s_nop 1" :: "v"(v));
}
DEVI void st8(v2u v, __amdgpu_buffer_rsrc_t r, unsigned vo, unsigned so) { __builtin_amdgcn_raw_buffer_store_b64(v, r, vo, so, 16); }
DEVI void st4(unsigned v, __amdgpu_buffer_rsrc_t r, unsigned vo, unsigned so) { __builtin_amdgcn_raw_buffer_store_b32(v, r, vo, so, 16); }
DEVI unsigned umax3(unsigned a, unsigned b, unsigned c) { unsigned r; asm("v_max3_u32 %0, %1, %2, %3" : "=v"(r) : "v"(a), "v"(b), "v"(c)); return r; }
DEVI unsigned mx4(v4u v, unsigned m) { return umax3(umax3(v.x, v.y, m), v.z, v.w); }
DEVI bool any_sent(unsigned m) { return __builtin_amdgcn_ballot_w64(m == SENT) != 0; }
DEVI unsigned nosent(unsigned u) { return u < 0xFFFFFFFEu ? u : 0xFFFFFFFEu; }
DEVI v4u nosent4(v4u v) { return (v4u){nosent(v.x), nosent(v.y), nosent(v.z), nosent(v.w)}; }
DEVI f32x4 mfma32(v4u a, v4u b, f32x4 c) { return __builtin_amdgcn_mfma_f32_16x16x32_bf16(__builtin_bit_cast(bf16x8, a), __builtin_bit_cast(bf16x8, b), c, 0, 0, 0); }
DEVI f32x4 mfma16(v2u a, v2u b, f32x4 c) { return __builtin_amdgcn_mfma_f32_16x16x16bf16_1k(__builtin_bit_cast(s16x4, a), __builtin_bit_cast(s16x4, b), c, 0, 0, 0); }
DEVI f32x4 smf(v4u a, v8u b, f32x4 c, int idx) { return __builtin_amdgcn_smfmac_f32_16x16x64_bf16(__builtin_bit_cast(bf16x8, a), __builtin_bit_cast(bf16x16, b), c, idx, 0, 0); }
DEVI v2u trrd(LAS unsigned char* p) { return __builtin_bit_cast(v2u, __builtin_amdgcn_ds_read_tr16_b64_v4i16((LAS s16x4*)p)); }
DEVI float ror8(float v) { return __int_as_float(__builtin_amdgcn_update_dpp(0, __float_as_int(v), 0x128, 0xf, 0xf, false)); }
DEVI float xor1(float v) { return __int_as_float(__builtin_amdgcn_update_dpp(0, __float_as_int(v), 0xB1, 0xf, 0xf, false)); }
DEVI float sw32sum(float a, float b) { auto r = __builtin_amdgcn_permlane32_swap(__float_as_uint(a), __float_as_uint(b), false, false); return __uint_as_float(r[0]) + __uint_as_float(r[1]); }
DEVI float sw16sum(float a, float b) { auto r = __builtin_amdgcn_permlane16_swap(__float_as_uint(a), __float_as_uint(b), false, false); return __uint_as_float(r[0]) + __uint_as_float(r[1]); }
DEVI float sw32max(float a) { auto r = __builtin_amdgcn_permlane32_swap(__float_as_uint(a), __float_as_uint(a), false, false); return fmaxf(__uint_as_float(r[0]), __uint_as_float(r[1])); }
DEVI float sw16max(float a) { auto r = __builtin_amdgcn_permlane16_swap(__float_as_uint(a), __float_as_uint(a), false, false); return fmaxf(__uint_as_float(r[0]), __uint_as_float(r[1])); }
DEVI f32x4 fold(f32x4 a, f32x4 b) { f32x4 r; r.x = sw32sum(a.x, b.x); r.y = sw32sum(a.y, b.y); r.z = sw32sum(a.z, b.z); r.w = sw32sum(a.w, b.w); return r; }
DEVI float dot2(unsigned a, unsigned b, float c) { return __builtin_amdgcn_fdot2_f32_bf16(__builtin_bit_cast(bf16x2, a), __builtin_bit_cast(bf16x2, b), c, false); }
DEVI unsigned cvtpk(float lo, float hi) { unsigned r; asm("v_cvt_pk_bf16_f32 %0, %1, %2" : "=v"(r) : "v"(lo), "v"(hi)); return r; }
DEVI float max5x8(float a, float b, float c, float d, float e) {
    float m, t;
    asm("v_max3_f32 %0, %2, %3, %4\n\tv_max3_f32 %0, %0, %5, %6\n\ts_nop 1\n\t"
        "v_max_f32_dpp %0, %0, %0 row_ror:8 row_mask:0xf bank_mask:0xf\n\tv_mov_b32 %1, %0\n\ts_nop 1\n\t"
        "v_permlane16_swap_b32 %0, %1\n\tv_max_f32 %0, %0, %1\n\tv_mov_b32 %1, %0\n\ts_nop 1\n\t"
        "v_permlane32_swap_b32 %0, %1\n\tv_max_f32 %0, %0, %1"
        : "=&v"(m), "=&v"(t) : "v"(a), "v"(b), "v"(c), "v"(d), "v"(e));
    return m;
}
#define ALD16(D, VO, SO, IMM) asm volatile("buffer_load_dwordx4 %0, %1, %2, %3 offen offset:" #IMM " sc1" : "=v"(D) : "v"(VO), "s"(rd), "s"(SO) : "memory")
#define ALD4(D, VO, SO, IMM) asm volatile("buffer_load_dword %0, %1, %2, %3 offen offset:" #IMM " sc1" : "=v"(D) : "v"(VO), "s"(rd), "s"(SO) : "memory")
#define WAITV1(N, A) asm volatile("s_waitcnt vmcnt(" #N ")" : "+v"(A) :: "memory")
#define WAITV2(N, A, B) asm volatile("s_waitcnt vmcnt(" #N ")" : "+v"(A), "+v"(B) :: "memory")
#define SB() __builtin_amdgcn_sched_barrier(0)
#define PIN2(a, b) asm volatile("" : "+v"(a), "+v"(b))
DEVI void sleep_units(int n) {
    if (n & 32) __builtin_amdgcn_s_sleep(32);
    if (n & 16) __builtin_amdgcn_s_sleep(16);
    if (n & 8) __builtin_amdgcn_s_sleep(8);
    if (n & 4) __builtin_amdgcn_s_sleep(4);
    if (n & 2) __builtin_amdgcn_s_sleep(3);
    if (n & 1) __builtin_amdgcn_s_sleep(1);
}
DEVI void sleep_cyc(int n) { sleep_units(min(n >> 6, 63)); }
DEVI unsigned mt() { return (unsigned)__builtin_amdgcn_s_memtime(); }
DEVI void wait_l(unsigned t) {
    unsigned d;
    asm volatile("1:\n\ts_memrealtime vcc\n\ts_waitcnt lgkmcnt(0)\n\ts_sub_i32 %0, vcc_lo, %1\n\ts_cmp_lt_i32 %0, 0\n\ts_cbranch_scc1 1b"
                 : "=&s"(d) : "s"(__builtin_amdgcn_readfirstlane(t)) : "memory", "scc", "vcc");
}
constexpr unsigned SPINCAP = 40000;
DEVI bool poll_fail(unsigned& spins, GAS unsigned* ctl, int back) {
    sleep_cyc(back);
    ++spins;
    if ((spins & 63u) == 0u) {
        if (spins > SPINCAP) { __hip_atomic_store(ctl + C_ABORT, 1u, RLX_AGENT); return true; }
        if (__builtin_amdgcn_readfirstlane(__hip_atomic_load(ctl + C_ABORT, RLX_AGENT))) return true;
    }
    return false;
}

#define EV(code)
#define EVT 0
DEVI void wait_ge(GAS unsigned* w, unsigned v, GAS unsigned* ctl, bool& dead) {
    unsigned spins = 0;
    while (!dead && (unsigned)__builtin_amdgcn_readfirstlane(__hip_atomic_load(w, RLX_AGENT)) < v) { if (poll_fail(spins, ctl, 64)) dead = true; }
    asm volatile("" ::: "memory");
}

__global__ void __launch_bounds__(256) k_prep(Params p) {
    const int tid = threadIdx.x, lane = tid & 63, wave = tid >> 6, b = blockIdx.x;
    unsigned* ctl = (unsigned*)(p.ws + O_CTL);
    if (b >= 204) ctl[(b - 204) * 256 + tid] = 0u;
    if (b < 160 && tid == 192) {
        const unsigned page = 1 + (b >> 3);
        __amdgpu_buffer_rsrc_t rs = __builtin_amdgcn_make_buffer_rsrc(p.ws, 0, 0xFFFFFF00u, 0x00020000);
        unsigned t[2] = {0xffffffffu, 0xffffffffu};
        for (int rep = 0; rep < 6; ++rep)
            for (int side = 0; side < 2; ++side) {
                const unsigned long long c0 = __builtin_amdgcn_s_memtime();
                asm volatile("s_waitcnt lgkmcnt(0)" ::: "memory");
                st4(SENT, rs, page * PAGE + 480 * 1024 + side * 8192 + (b & 7) * 512, 0);
                asm volatile("s_waitcnt vmcnt(0)" ::: "memory");
                const unsigned long long c1 = __builtin_amdgcn_s_memtime();
                t[side] = min(t[side], (unsigned)(c1 - c0));
            }
        ctl[C_SIDE + page] = (t[1] < t[0] ? 1u : 0u) ^ (xcc_id() >= 4 ? 1u : 0u);
    }
    float* R = (float*)(p.ws + O_R); float* G = (float*)(p.ws + O_G);
    for (int rr = 0; rr < 2; ++rr) {
        const int row = b * 8 + wave * 2 + rr;
        float a = 0.f, g = 0.f;
#pragma unroll
        for (int q = 0; q < 2; ++q) {
            const int k = q * 512 + lane * 8;
            const v4u xv = *(const v4u*)(p.x + row * D_ + k), wr = *(const v4u*)(p.wr + k), gw = *(const v4u*)(p.gw + k);
#pragma unroll
            for (int e = 0; e < 4; ++e) {
                a = fmaf(lo16(xv[e]), lo16(wr[e]), a); a = fmaf(hi16(xv[e]), hi16(wr[e]), a);
                const unsigned pr = pk2(lo16(xv[e]) * lo16(gw[e]), hi16(xv[e]) * hi16(gw[e]));
                g += lo16(pr); g += hi16(pr);
            }
        }
#pragma unroll
        for (int m = 32; m >= 1; m >>= 1) { a += __shfl_xor(a, m); g += __shfl_xor(g, m); }
        if (lane == 0) { R[row] = rnd(a); g = rnd(g); G[row] = rnd(1.f / (1.f + __expf(-g))); }
    }
}

constexpr unsigned LC_PA = 0, LC_QK = 24576, LC_V = LC_QK + 4096, LC_O = LC_V + 2048, LC_MISC = 98304 + 16, LC_KV = 33792  , LC_END = LC_KV + 32768;

DEVI void chain_body(const Params& p, const Tim& tm, LAS unsigned char* lds, const int g, const int i, const unsigned tbase) {
    const int tid = threadIdx.x, lane = tid & 63, wave = __builtin_amdgcn_readfirstlane(tid >> 6);
    const int h = g * 8 + (i >> 2), j = i & 3, cid = h * 4 + j;
    const int kw = (wave + i) & 7;
    GAS unsigned* ctl = (GAS unsigned*)(p.ws + O_CTL);
    const __amdgpu_buffer_rsrc_t rs = __builtin_amdgcn_make_buffer_rsrc(p.ws, 0, 0xFFFFFF00u, 0x00020000);
    unsigned sidemask = 0;
    { const unsigned sv = lane < 21 ? (ctl[C_SIDE + lane] & 1u) : 0u; sidemask = (unsigned)__builtin_amdgcn_ballot_w64(sv != 0); }
    v8u WA[6][2], WO[2][2];
    v4u sa[2][12], so[8];
    {
        const u16* wm[3] = {p.wq, p.wk, p.wv};
#pragma unroll
        for (int ks = 0; ks < 2; ++ks)
#pragma unroll
            for (int m = 0; m < 3; ++m)
#pragma unroll
                for (int q = 0; q < 4; ++q)
                    sa[ks][m * 4 + q] = *(const v4u*)(wm[m] + (size_t)(128 * kw + 64 * ks + 16 * q + (lane >> 2)) * 4096 + h * 128 + 32 * j + (lane & 3) * 8);
#pragma unroll
        for (int q = 0; q < 8; ++q) so[q] = *(const v4u*)(p.wo + (size_t)(h * 128 + 16 * q + (lane >> 2)) * 1024 + 256 * j + 32 * wave + (lane & 3) * 8);
    }
    SB();
    const int tau = lane >> 5, rh = (lane >> 4) & 1, n16 = lane & 15, c8 = (lane >> 3) & 1, g4 = lane >> 4;
    const unsigned o_gm = amap(g * 16384 + kw * 2048 + lane * 16);
    const unsigned qkvb = L_QKV + h * 6144;
    const int pcol = 256 * j + 32 * wave + 16 * tau + n16;
    const unsigned o_pp = amap((pcol >> 3) * 4096 + h * 128 + (rh * 8 + (pcol & 7)) * 8);
    const int sidx = c8 ? 0xEEEE : 0x4444;
    LAS unsigned char* const pw = lds + LC_PA + ((wave * 2 + rh) * 96 + 16 * tau + n16) * 16;
    LAS unsigned char* const ob = lds + LC_O + (lane & 7) * 320 + c8 * 16 + g4 * 32;
    const v4u rd = {(unsigned)(uintptr_t)p.ws, (unsigned)((uintptr_t)p.ws >> 32) & 0xffffu, 0xFFFFFF00u, 0x00020000u};
    const unsigned o_i1 = amap(qkvb + j * 1024 + lane * 16);
    const unsigned o_i2 = lane < 32 ? amap(qkvb + 4096 + j * 512 + lane * 16) : amap(((lane >> 3) & 3) * 16384 + cid * 128 + (lane & 7) * 16);
    {
        const v4u zz = {SENT, SENT, SENT, SENT};
        if (wave < 2) {
            const unsigned sb = P_HAND * PAGE + wave * 524288u, sx = ((sidemask >> P_HAND) & 1u) << 13;
            st16(zz, rs, o_i1 ^ sx, sb);
            v4u iv = zz;
            if (wave == 0 && lane >= 32) {
                iv = *(const v4u*)(p.mem0 + (lane & 7) * 1024 + cid * 8);
                iv = (v4u){nosent(iv.x), nosent(iv.y), nosent(iv.z), nosent(iv.w)};
            }
            st16(iv, rs, o_i2 ^ sx, sb);
        }
#pragma unroll
        for (int sl = 0; sl < 2; ++sl) st8((v2u){SENT, SENT}, rs, o_pp ^ (((sidemask >> (P_PART + sl)) & 1u) << 13), (P_PART + sl) * PAGE);
    }
    bool dead = false;
    const unsigned o_kv = (wave & 2 ? O_VX : O_KX) + h * 8192 + (wave & 1) * 4096 + lane * 16;
    v4u p0, p1, p2, p3;
    if (wave >= 4) wait_ge(ctl + C_KV + 8 * h, 4u, ctl, dead);
    asm volatile("s_waitcnt vmcnt(0)" ::: "memory");
    if (wave >= 4) { ALD16(p0, o_kv, 0, 0); ALD16(p1, o_kv, 0, 1024); ALD16(p2, o_kv, 0, 2048); ALD16(p3, o_kv, 0, 3072); }
    BAR();
    EV(2);
    if (tid == 0) {
        if (__hip_atomic_fetch_add(ctl + C_ARRIVE, 1u, RLX_AGENT) == 127u) __hip_atomic_store(ctl + C_T0, ((rt() + tm.t0) & ~3u) | 0x80000000u, RLX_AGENT);
    }
    SB();
    {
        LAS unsigned char* img = lds + wave * 12288;
        const int gB = lane >> 4, q4 = (lane & 15) >> 2, p4 = lane & 3;
        const unsigned rbase = (16 * (gB >> 1) + 4 * (gB & 1) + (q4 & 1) + 8 * (q4 >> 1)) * 64 + p4 * 8;
#pragma unroll
        for (int ks = 0; ks < 2; ++ks) {
#pragma unroll
            for (int q = 0; q < 12; ++q) *(LAS v4u*)(img + q * 1024 + lane * 16) = sa[ks][q];
            asm volatile("s_waitcnt lgkmcnt(0)" ::: "memory");
#pragma unroll
            for (int t = 0; t < 6; ++t)
#pragma unroll
                for (int m = 0; m < 4; ++m) {
                    const v2u r = trrd(img + (t >> 1) * 4096 + (t & 1) * 32 + (32 * (m >> 1) + 2 * (m & 1)) * 64 + rbase);
                    WA[t][ks][2 * m] = r.x; WA[t][ks][2 * m + 1] = r.y;
                }
            asm volatile("s_waitcnt lgkmcnt(0)" ::: "memory");
        }
#pragma unroll
        for (int q = 0; q < 8; ++q) *(LAS v4u*)(img + q * 1024 + lane * 16) = so[q];
        asm volatile("s_waitcnt lgkmcnt(0)" ::: "memory");
#pragma unroll
        for (int t = 0; t < 2; ++t)
#pragma unroll
            for (int ks = 0; ks < 2; ++ks)
#pragma unroll
                for (int m = 0; m < 4; ++m) {
                    const v2u r = trrd(img + t * 32 + (64 * ks + 32 * (m >> 1) + 2 * (m & 1)) * 64 + rbase);
                    WO[t][ks][2 * m] = r.x; WO[t][ks][2 * m + 1] = r.y;
                }
    }
#define STAMP(k)
#define STAMPN(k)
#define ROLL(n)
#define NSP(k, v)
#define DUMP()
#define LATE(k, c)
#define XREC()
#define HAND_ADDR(s) do { const unsigned pg = P_HAND + ((s) >> 2); sb = pg * PAGE + ((s) & 3) * 524288u; sx = ((sidemask >> pg) & 1u) << 13; } while (0)
#define RETRY(K, RELOAD, BAD) do { unsigned spins = 0;                                                                    \
                while (!dead) { if (poll_fail(spins, ctl, tm.back)) { dead = true; break; } RELOAD; if (!(BAD)) break; }  \
                NSP(K, spins); } while (0)
#define ZERO_A() f32x4 c0 = {0, 0, 0, 0}, c1 = c0, c2 = c0, c3 = c0, c4 = c0, c5 = c0; PIN2(c0, c1); PIN2(c2, c3); PIN2(c4, c5)
#define GATHER_MEM(EXTRA, NX) v4u a0, a1;                                                                                 \
            ALD16(a0, o_gmx, sb, 0); ALD16(a1, o_gmx, sb, 1024); EXTRA; WAITV2(NX, a0, a1);                               \
            if (__builtin_expect(any_sent(mx4(a1, mx4(a0, 0u))), 0))                                                      \
                RETRY(0, ALD16(a0, o_gmx, sb, 0); ALD16(a1, o_gmx, sb, 1024); WAITV2(0, a0, a1), any_sent(mx4(a1, mx4(a0, 0u))))
#define QK_MFMA() do {                                                                                                    \
            c0 = smf(a0, WA[0][0], c0, sidx); c1 = smf(a0, WA[1][0], c1, sidx); c0 = smf(a1, WA[0][1], c0, sidx); c1 = smf(a1, WA[1][1], c1, sidx); \
            SB();                                                                                                         \
            c2 = smf(a0, WA[2][0], c2, sidx); c3 = smf(a0, WA[3][0], c3, sidx); c2 = smf(a1, WA[2][1], c2, sidx); c3 = smf(a1, WA[3][1], c3, sidx); \
            SB(); *(LAS f32x4*)pw = fold(c0, c1); SB(); *(LAS f32x4*)(pw + 512) = fold(c2, c3); SB(); } while (0)
#define V_PART() do { c4 = smf(a0, WA[4][0], c4, sidx); c5 = smf(a0, WA[5][0], c5, sidx); c4 = smf(a1, WA[4][1], c4, sidx); c5 = smf(a1, WA[5][1], c5, sidx); \
            *(LAS f32x4*)(pw + 1024) = fold(c4, c5);                                                                      \
            if (lane == 0) __hip_atomic_fetch_add((LAS unsigned*)(lds + LC_MISC + 4), 1u, __ATOMIC_RELAXED, __HIP_MEMORY_SCOPE_WORKGROUP); } while (0)
#define ZERO_D() f32x4 d0 = {0, 0, 0, 0}, d1 = d0; const unsigned pp = P_PART + (s & 3), px = ((sidemask >> pp) & 1u) << 13; unsigned o_ppx = o_pp ^ px; PIN2(d0, d1); asm volatile("" : "+v"(o_ppx))
#define WO_STAGE() do {                                                                                                   \
            const v4u o0 = *(LAS v4u*)ob, o1 = *(LAS v4u*)(ob + 128); SB();                                               \
            d0 = smf(o0, WO[0][0], d0, sidx); d1 = smf(o0, WO[1][0], d1, sidx); d0 = smf(o1, WO[0][1], d0, sidx); d1 = smf(o1, WO[1][1], d1, sidx); \
            const f32x4 f = fold(d0, d1);                                                                                 \
            st8((v2u){nosent(pk2(f.x, f.y)), nosent(pk2(f.z, f.w))}, rs, o_ppx, pp * PAGE); } while (0)
#define REDUCE4(LP, OFF) do {                                                                                             \
            const f32x4 r0 = *(LAS f32x4*)(LP), r1 = *(LAS f32x4*)((LP) + 3072), r2 = *(LAS f32x4*)((LP) + 6144), r3 = *(LAS f32x4*)((LP) + 9216); \
            const f32x4 t = (r0 + r1) + (r2 + r3);                                                                        \
            st4(nosent(pk2(sw32sum(t.x, t.z), sw32sum(t.y, t.w))), rs, (OFF), sb); } while (0)
    unsigned sb, sx;
    if (wave < 4) {
        const unsigned o_gq = amap(qkvb + wave * 1024 + lane * 16);
        const unsigned o_gp = amap(cid * 4096 + lane * 16);
        const int crow = 4 * ((lane >> 2) & 1) + g4;
        v4u sel = {0u, 0u, 0u, 0u};
        sel[(lane & 7) >> 1] = (lane & 1) ? 0x3f800000u : 0x00003f80u;
        const unsigned o_pm = amap(c8 * 16384 + cid * 128 + crow * 16 + (lane & 3) * 4);
        const unsigned o_gv = amap(qkvb + 4096 + wave * 512 + n16 * 16 + (4 * (g4 & 1) + 2 * (g4 >> 1)) * 2);
        LAS unsigned char* const qb = lds + LC_QK + (4 * g4 + ((lane & 15) >> 2)) * 32 + (lane & 1) * 8;
        LAS unsigned char* const ow = lds + LC_O + (lane & 7) * 320 + (32 * wave + 16 * c8 + 4 * g4) * 2;
        if (tid == 0) *(LAS unsigned*)(lds + LC_MISC + 4) = 0u;
        BAR();
        EV(3);
        if (wave == 0) {
            unsigned plan, spins = 0;
            for (;;) { plan = __builtin_amdgcn_readfirstlane(__hip_atomic_load(ctl + C_T0, RLX_AGENT)); if (plan) break; if (poll_fail(spins, ctl, 64)) { dead = true; break; } }
            { const unsigned now = rt(); plan = now + ((int)((plan - now) << 1) >> 1); }
            wait_l(plan);
        }
        EV(4);
        v4u kx[8], vx[2];
        unsigned tq;
#define A_STAGE_A(S1)                                                                                                     \
            BAR();                                                                                  \
            STAMPN(0);                                                                                                    \
            GATHER_MEM({ LAS unsigned char* kvb = lds + LC_KV + ((S1) & 1) * 16384 + lane * 16;                           \
                _Pragma("unroll") for (int q = 0; q < 8; ++q) kx[q] = *(LAS v4u*)(kvb + q * 1024);                        \
                vx[0] = *(LAS v4u*)(kvb + 8192 + wave * 2048); vx[1] = *(LAS v4u*)(kvb + 8192 + wave * 2048 + 1024); SB(); }, 0); \
            STAMPN(1);                                                                                                    \
            QK_MFMA();                                                                                                    \
            BAR();                                                                                                 \
            tq = rt() + tm.ta;                                                                                            \
            STAMPN(2);                                                                                                    \
            V_PART()
        {
            HAND_ADDR(0);
            ZERO_A();
            const unsigned o_gmx = o_gm ^ sx;
            A_STAGE_A(0);
        }
        for (int s = 0; s < NSEG - 1; ++s) {
            __builtin_amdgcn_s_setprio(3);
            ROLL(3); STAMP(3);
            v4u gq;
            ZERO_D();
            unsigned o_gqx = o_gq ^ sx, o_gvx = o_gv ^ sx; PIN2(o_gqx, o_gvx);
            wait_l(tq);
            STAMP(4);
            ALD16(gq, o_gqx, sb, 0); WAITV1(0, gq);
            if (__builtin_expect(any_sent(mx4(gq, 0u)), 0)) RETRY(1, ALD16(gq, o_gqx, sb, 0); WAITV1(0, gq), any_sent(mx4(gq, 0u)));
            *(LAS v4u*)(lds + LC_QK + wave * 1024 + lane * 16) = gq;
            STAMP(5);
            BAR();
            {
                unsigned va, vc;
                ALD4(va, o_gvx, sb, 0); ALD4(vc, o_gvx, sb, 256);
                v4u qf[4], kf[4];
#pragma unroll
                for (int c = 0; c < 4; ++c) { const v2u qa = trrd(qb + c * 1024), qc = trrd(qb + c * 1024 + 512); qf[c] = (v4u){qa.x, qa.y, qc.x, qc.y}; }
#pragma unroll
                for (int c = 0; c < 4; ++c) { const v2u ka = trrd(qb + c * 1024 + 16), kc = trrd(qb + c * 1024 + 528); kf[c] = (v4u){ka.x, ka.y, kc.x, kc.y}; }
                SB();
                f32x4 s0 = {0, 0, 0, 0}, s1 = s0, sm = s0;
#pragma unroll
                for (int c = 0; c < 4; ++c) { s0 = mfma32(kx[c], qf[c], s0); s1 = mfma32(kx[4 + c], qf[c], s1); sm = mfma32(kf[c], qf[c], sm); }
                SB();
                float x0 = c8 ? s1.x : s0.x, x1 = c8 ? s1.y : s0.y, x2 = c8 ? s1.z : s0.z, x3 = c8 ? s1.w : s0.w;
                float xm = tau ? (c8 ? sm.w : sm.z) : (c8 ? sm.y : sm.x);
                constexpr float CS = SCALE * LOG2E;
                const float ml = -CS * max5x8(x0, x1, x2, x3, xm);
                STAMP(6);
                x0 = __builtin_amdgcn_exp2f(fmaf(x0, CS, ml)); x1 = __builtin_amdgcn_exp2f(fmaf(x1, CS, ml));
                x2 = __builtin_amdgcn_exp2f(fmaf(x2, CS, ml)); x3 = __builtin_amdgcn_exp2f(fmaf(x3, CS, ml));
                xm = __builtin_amdgcn_exp2f(fmaf(xm, CS, ml));
                const unsigned e01 = pk2(x0, x1), e23 = pk2(x2, x3), em = pk2(xm, xm);
                const v4u b1 = {c8 ? 0u : e01, c8 ? 0u : e23, c8 ? e01 : 0u, c8 ? e23 : 0u};
                unsigned b2 = em & (c8 ? 0xffff0000u : 0x0000ffffu);
                asm volatile("" : "+v"(b2));
                f32x4 o0 = {0, 0, 0, 0}, o1 = o0;
                o0 = mfma32(vx[0], b1, o0); o1 = mfma32(vx[1], b1, o1);
                SB();
                WAITV2(0, va, vc);
                if (__builtin_expect(any_sent(max(va, vc)), 0)) RETRY(1, ALD4(va, o_gvx, sb, 0); ALD4(vc, o_gvx, sb, 256); WAITV2(0, va, vc), any_sent(max(va, vc)));
                o0 = mfma16((v2u){va, 0u}, (v2u){b2, 0u}, o0); o1 = mfma16((v2u){vc, 0u}, (v2u){b2, 0u}, o1);
                SB();
                float l = ((x0 + x1) + (x2 + x3)) + xm;
                l += ror8(l); l = sw16sum(l, l); l = sw32sum(l, l);
                const float rinv = __builtin_amdgcn_rcpf(l);
                SB();
                float y[4];
#pragma unroll
                for (int e = 0; e < 4; ++e) y[e] = ((c8 ? o1[e] : o0[e]) + ror8(c8 ? o0[e] : o1[e])) * rinv;
                *(LAS v2u*)ow = (v2u){pk2(y[0], y[1]), pk2(y[2], y[3])};
            }
            BAR();
            WO_STAGE();
            const unsigned t3 = rt() + tm.tb;
            STAMP(7);
            __builtin_amdgcn_s_setprio(0);
            HAND_ADDR(s + 1);
            ZERO_A();
            unsigned o_gmx = o_gm ^ sx; asm volatile("" : "+v"(o_gmx));
            if (__builtin_expect(wave == 0, 1)) {
                unsigned o_pmx = o_pm ^ sx, o_gpx = o_gp ^ px; PIN2(o_pmx, o_gpx);
                wait_l(t3);
                STAMP(8);
                v4u r0, r1, r2, r3; f32x4 u; unsigned m;
#define C_LOAD() ALD16(r0, o_gpx, pp * PAGE, 0); ALD16(r1, o_gpx, pp * PAGE, 1024); ALD16(r2, o_gpx, pp * PAGE, 2048); ALD16(r3, o_gpx, pp * PAGE, 3072);  \
                WAITV1(3, r0); u = mfma32(sel, r0, (f32x4){0, 0, 0, 0}); m = mx4(r0, 0u); SB(); WAITV1(2, r1); u = mfma32(sel, r1, u); m = mx4(r1, m); SB(); \
                WAITV1(1, r2); u = mfma32(sel, r2, u); m = mx4(r2, m); SB(); WAITV1(0, r3); u = mfma32(sel, r3, u); m = mx4(r3, m)
                C_LOAD();
                if (__builtin_expect(any_sent(m), 0)) RETRY(2, C_LOAD(), any_sent(m));
                float z0 = tau ? u.z : u.x, z1 = tau ? u.w : u.y;
                z0 += ror8(z0); z1 += ror8(z1);
                { auto sw = __builtin_amdgcn_permlane16_swap(__float_as_uint(z0), __float_as_uint(z1), false, false); z0 = __uint_as_float(sw[0]); z1 = __uint_as_float(sw[1]); }
                const unsigned w2 = nosent(pk2(z0, z1));
                st4(w2, rs, o_pmx, sb); st4(w2, rs, o_pmx + 65536, sb);
                STAMP(9);
                const unsigned now = rt(), plan = now + tm.tc;
                DUMP();
                wait_l(plan);
            } else { DUMP(); }
            A_STAGE_A(s + 1);
        }
    } else {
        const int hw = wave - 4;
        const int sl_d = lane & 7, sl_m = (lane >> 3) & 1, sl_r = (lane >> 4) & 1;
        LAS unsigned char* const l_qk = lds + LC_PA + tau * 12288 + (sl_r * 96 + sl_m * 32 + 8 * hw + sl_d) * 16;
        const unsigned o_pqk = amap(qkvb + j * 1024 + (8 * hw + sl_d) * 32 + sl_m * 16 + sl_r * 8 + tau * 4);
        LAS unsigned char* const l_v = lds + LC_PA + tau * 12288 + (rh * 96 + 64 + 16 * (hw & 1) + n16) * 16;
        const unsigned o_pv = amap(qkvb + 4096 + j * 512 + (16 * (hw & 1) + n16) * 16 + rh * 8 + tau * 4);
        LAS unsigned char* const l_kv = lds + LC_KV + hw * 4096 + lane * 16;
        WAITV2(0, p0, p1); WAITV2(0, p2, p3);
        BAR();
        *(LAS v4u*)l_kv = p0; *(LAS v4u*)(l_kv + 1024) = p1; *(LAS v4u*)(l_kv + 2048) = p2; *(LAS v4u*)(l_kv + 3072) = p3;
        __builtin_amdgcn_s_setprio(2);
        for (int s = -1; s < NSEG - 1; ++s) {
            if (s >= 0) {
                ROLL(7);
                ZERO_D();
                BAR();
                __builtin_amdgcn_s_sleep(3);
                if (((s + 1) & 3) == 0) wait_ge(ctl + C_KV + 8 * (((s + 1) >> 2) * 32 + h), 2u, ctl, dead);
                ALD16(p0, o_kv, (s + 1) * 262144u, 0); ALD16(p1, o_kv, (s + 1) * 262144u, 1024); ALD16(p2, o_kv, (s + 1) * 262144u, 2048); ALD16(p3, o_kv, (s + 1) * 262144u, 3072);
                {
                    const unsigned rp = P_PART + ((s + 2) & 3), rx = ((sidemask >> rp) & 1u) << 13;
                    st8((v2u){SENT, SENT}, rs, o_pp ^ rx, rp * PAGE); st8((v2u){SENT, SENT}, rs, (o_pp - 8 * 16384) ^ rx, rp * PAGE);
                    if (hw >= 2 && s + 2 < NSEG) {
                        const unsigned pg = P_HAND + ((s + 2) >> 2);
                        st16((v4u){SENT, SENT, SENT, SENT}, rs, (hw == 2 ? o_i1 : o_i2) ^ (((sidemask >> pg) & 1u) << 13), pg * PAGE + ((s + 2) & 3) * 524288u);
                    }
                }
                STAMP(7); STAMP(8);
                BAR();
                STAMP(9);
                WO_STAGE();
                STAMP(10);
                DUMP();
                if (j == 0 && tid == 256) __hip_atomic_store(ctl + C_PROG + 32 * h, (unsigned)(s + 1), RLX_AGENT);
                WAITV2(0, p0, p1); WAITV2(0, p2, p3);
                LAS unsigned char* kw = l_kv + ((s + 1) & 1) * 16384;
                *(LAS v4u*)kw = p0; *(LAS v4u*)(kw + 1024) = p1; *(LAS v4u*)(kw + 2048) = p2; *(LAS v4u*)(kw + 3072) = p3;
            }
            HAND_ADDR(s + 1);
            ZERO_A();
            unsigned o_pqkx = o_pqk ^ sx, o_gmx = o_gm ^ sx; PIN2(o_pqkx, o_gmx);
            BAR();
            STAMPN(0);
            GATHER_MEM(, 0);
            STAMPN(1);
            QK_MFMA();
            STAMPN(2);
            BAR();
            STAMPN(3);
            REDUCE4(l_qk, o_pqkx);
            SB();
            STAMPN(4);
            if (s == NSEG - 2 && j == 0 && tid == 256) __hip_atomic_store(ctl + C_PROG + 32 * h, (unsigned)NSEG, RLX_AGENT);
            V_PART();
            STAMPN(5);
            if (hw < 2) {
                while (__builtin_amdgcn_readfirstlane(*(volatile LAS unsigned*)(lds + LC_MISC + 4)) < 8u * (unsigned)(s + 2)) { }
                asm volatile("" ::: "memory");
                REDUCE4(l_v, o_pv ^ sx);
            }
            STAMPN(6);
        }
    }
}

DEVI int claim_slot(GAS unsigned* ctl) {
    const unsigned x = xcc_id() & 7u;
    const unsigned r = __hip_atomic_fetch_add(ctl + C_XR + 32 * x, 1u, RLX_AGENT);
    if (r < 32 && __hip_atomic_exchange(ctl + C_SLOT + x * 32 + r, 1u, RLX_AGENT) == 0u) return (int)(x * 32 + r);
    for (int k = 255;; k = (k + 255) & 255) if (__hip_atomic_exchange(ctl + C_SLOT + k, 1u, RLX_AGENT) == 0u) return k;
}

DEVI void glds(unsigned voff, const void* sbase, unsigned ldsaddr) {
    unsigned keep;
    asm volatile("s_mov_b32 %0, m0\n\ts_mov_b32 m0, %3\n\ts_nop 2\n\tglobal_load_lds_dwordx4 %1, %2\n\ts_mov_b32 m0, %0" : "=&s"(keep) : "v"(voff), "s"(sbase), "s"(ldsaddr) : "memory");
}
template <int BM, int BN, int WM, bool TR, bool PERMK>
struct Gemm {
    static constexpr int WN = 8 / WM, MT = BM / WM / 16, NT = BN / WN / 16, NA = BM / 64, NB = BN / 64, STAGE = (BM + BN) * 128, NS = 147456 / STAGE, LPR = BN / 8;
    f32x4 acc[MT][NT];
    DEVI void run(LAS unsigned char* lds, const u16* A, int lda, const u16* B, int ldb, int nk) {
        const int tid = otid(), lane = tid & 63, wave = __builtin_amdgcn_readfirstlane(tid >> 6);
        const int wm = wave / WN, wn = wave % WN;
        unsigned va[NA], vb[NB];
#pragma unroll
        for (int q = 0; q < NA; ++q) va[q] = ((wave * NA * 8 + q * 8 + (lane >> 3)) * lda + (((lane & 7) ^ ((lane >> 3) & 7)) * 8)) * 2;
#pragma unroll
        for (int q = 0; q < NB; ++q) {
            const int k = (wave * NB + q) * (64 / LPR) + lane / LPR, sl = lane % LPR;
            const int pk = PERMK ? ((k & 32) + 16 * ((k >> 2) & 1) + 4 * ((k >> 3) & 3) + (k & 3)) : k;
            const int f = (k & 3) | (((k >> 3) & 1) << 2);
            vb[q] = (pk * ldb + (((sl >> 1) ^ f) * 16 + (sl & 1) * 8)) * 2;
        }
        const unsigned lbase = (unsigned)(uintptr_t)lds;
        auto piece = [&](int kt, int buf, int q) {
            if (q < NA) glds(va[q], A + kt * 64, lbase + buf * STAGE + (wave * NA + q) * 1024);
            else glds(vb[q - NA], B + (size_t)kt * 64 * ldb, lbase + buf * STAGE + BM * 128 + (wave * NB + q - NA) * 1024);
        };
        const int fr = lane & 15, fg = lane >> 4;
        const unsigned ra = (wm * (BM / WM) + fr) * 128;
        const unsigned ra0 = ra + ((fg ^ (fr & 7)) * 16), ra1 = ra + (((4 + fg) ^ (fr & 7)) * 16);
        const int q4 = fr >> 2, p4 = fr & 3, fsw = q4 | ((fg & 1) << 2);
        const unsigned rb = BM * 128 + (8 * fg + q4) * (BN * 2) + p4 * 8 + (((wn * NT) ^ fsw) * 32);
        v4u a0[MT], b0[NT], a1[MT], b1[NT];
        auto frag = [&](LAS unsigned char* sp, int ks, v4u* af, v4u* bf, int r) {
            if (r < NT) { LAS unsigned char* q = sp + (rb ^ (r * 32)) + ks * (BN * 64); const v2u x = trrd(q), y = trrd(q + BN * 8); bf[r] = (v4u){x.x, x.y, y.x, y.y}; }
            else af[r - NT] = *(LAS v4u*)(sp + (ks ? ra1 : ra0) + (r - NT) * 2048);
        };
        constexpr int NM = MT * NT, NF = MT + NT, NP = NA + NB;
        auto half = [&](const v4u* af, const v4u* bf, LAS unsigned char* np, int nks, v4u* naf, v4u* nbf, bool rd, bool stg, int skt, int sbuf) {
#pragma unroll
            for (int i = 0; i < NM; ++i) {
                const int m = i / NT, n = i % NT;
                acc[m][n] = TR ? mfma32(bf[n], af[m], acc[m][n]) : mfma32(af[m], bf[n], acc[m][n]);
#pragma unroll
                for (int r = i * NF / NM; r < (i + 1) * NF / NM; ++r) if (rd) frag(np, nks, naf, nbf, r);
#pragma unroll
                for (int q = i * NP / NM; q < (i + 1) * NP / NM; ++q) if (stg) piece(skt, sbuf, q);
                SB();
            }
        };
#pragma unroll
        for (int m = 0; m < MT; ++m)
#pragma unroll
            for (int n = 0; n < NT; ++n) acc[m][n] = (f32x4){0, 0, 0, 0};
#pragma unroll
        for (int q = 0; q < NS * NP; ++q) piece(q / NP, q / NP, q % NP);
        asm volatile("s_waitcnt vmcnt(%0)\n\ts_barrier" :: "n"((NS - 1) * NP) : "memory");
#pragma unroll
        for (int r = 0; r < NF; ++r) frag(lds, 0, a0, b0, r);
        int buf = 0;
        for (int kt = 0; kt < nk - NS; ++kt) {
            const int nb = buf == NS - 1 ? 0 : buf + 1;
            half(a0, b0, lds + buf * STAGE, 1, a1, b1, true, false, 0, 0);
            asm volatile("s_waitcnt vmcnt(%0) lgkmcnt(0)\n\ts_barrier" :: "n"((NS - 2) * NP) : "memory");
            half(a1, b1, lds + nb * STAGE, 0, a0, b0, true, true, kt + NS, buf);
            buf = nb;
        }
#pragma unroll
        for (int t = NS; t >= 1; --t) {
            const int nb = buf == NS - 1 ? 0 : buf + 1;
            half(a0, b0, lds + buf * STAGE, 1, a1, b1, true, false, 0, 0);
            asm volatile("s_waitcnt vmcnt(%0) lgkmcnt(0)\n\ts_barrier" :: "n"(t >= 2 ? (t - 2) * NP : 0) : "memory");
            half(a1, b1, lds + nb * STAGE, 0, a0, b0, t > 1, false, 0, 0);
            buf = nb;
        }
    }
};
DEVI v4u pk8(f32x4 a, f32x4 b) { return (v4u){pk2(a.x, a.y), pk2(a.z, a.w), pk2(b.x, b.y), pk2(b.z, b.w)}; }
DEVI void announce(GAS unsigned* w) {
    asm volatile("s_waitcnt vmcnt(0) lgkmcnt(0)\n\ts_barrier" ::: "memory");
    if (otid() == 0) __hip_atomic_fetch_add(w, 1u, RLX_AGENT);
}

template <int BM, int BN>
DEVI void kv_unit(const Params& p, LAS unsigned char* lds, int r0, int hh, int mat) {
    constexpr int WM = BM >= 128 ? 4 : 2, WN = 8 / WM, MT = BM / WM / 16, NT = BN / WN / 16;
    const int lane = otid() & 63, wave = __builtin_amdgcn_readfirstlane(otid() >> 6), wm = wave / WN, wn = wave % WN;
    const __amdgpu_buffer_rsrc_t rs = __builtin_amdgcn_make_buffer_rsrc(p.ws, 0, 0xFFFFFF00u, 0x00020000);
    const int row0 = r0 + wm * (BM / WM), hw = hh + wn * NT / 8, n0 = (wn * NT) & 7;
    if (mat == 0) {
        Gemm<BM, BN, WM, true, false> g; g.run(lds, p.x + (size_t)r0 * D_, D_, p.wk + hh * 128, 4096, 16);
#pragma unroll
        for (int m = 0; m < MT; ++m)
#pragma unroll
            for (int c = 0; c < NT / 2; ++c) {
                const int row = row0 + m * 16;
                st16(pk8(g.acc[m][2 * c], g.acc[m][2 * c + 1]), rs, lane * 16, O_KX + ((row >> 5) * 32 + hw) * 8192 + (((row >> 4) & 1) * 4 + n0 / 2 + c) * 1024);
            }
    } else {
        Gemm<BM, BN, WM, false, false> g; g.run(lds, p.x + (size_t)r0 * D_, D_, p.wv + hh * 128, 4096, 16);
#pragma unroll
        for (int m = 0; m < MT; m += 2)
#pragma unroll
            for (int n = 0; n < NT; ++n) {
                const int row = row0 + m * 16;
                st16(pk8(g.acc[m][n], g.acc[m + 1][n]), rs, lane * 16, O_VX + ((row >> 5) * 32 + hw) * 8192 + (n0 + n) * 1024);
            }
    }
    asm volatile("s_waitcnt vmcnt(0) lgkmcnt(0)\n\ts_barrier" ::: "memory");
    const int t = otid();
    if (t < (BM == 256 ? 2 : 1) * (BN / 128))
        __hip_atomic_fetch_add((GAS unsigned*)(p.ws + O_CTL) + C_KV + 8 * (((r0 >> 7) + t / (BN / 128)) * 32 + hh + t % (BN / 128)), 1u, RLX_AGENT);
}

#define FS(k)
#define QS(k)
DEVI void qa_unit(const Params& p, LAS unsigned char* lds, int panel, int hh, unsigned sidemask, bool& dead) {
    const int lane = otid() & 63, wave = __builtin_amdgcn_readfirstlane(otid() >> 6);
    const int g4 = lane >> 4, n16 = lane & 15;
    const __amdgpu_buffer_rsrc_t rs = __builtin_amdgcn_make_buffer_rsrc(p.ws, 0, 0xFFFFFF00u, 0x00020000);
    GAS unsigned* ctl = (GAS unsigned*)(p.ws + O_CTL);
    v4u qf[2][4];
    {
        Gemm<256, 128, 8, true, false> g; g.run(lds, p.x + (size_t)panel * 256 * D_, D_, p.wq + hh * 128, 4096, 16);
#pragma unroll
        for (int m = 0; m < 2; ++m)
#pragma unroll
            for (int c = 0; c < 4; ++c) qf[m][c] = pk8(g.acc[m][2 * c], g.acc[m][2 * c + 1]);
    }
    const int seg = panel * 8 + wave;
    const unsigned kvo = (seg * 32 + hh) * 8192;
    wait_ge(ctl + C_KV + 8 * ((seg >> 2) * 32 + hh), seg >> 2 ? 2u : 4u, ctl, dead);
    v4u kx[8], vx[8];
#pragma unroll
    for (int q = 0; q < 8; ++q) kx[q] = ld16(rs, lane * 16 + q * 1024, O_KX + kvo);
#pragma unroll
    for (int q = 0; q < 8; ++q) vx[q] = ld16(rs, lane * 16 + q * 1024, O_VX + kvo);
    f32x4 z = {0, 0, 0, 0};
    f32x4 s00 = z, s10 = z, s11 = z, sm0 = z, sm1 = z;
#pragma unroll
    for (int c = 0; c < 4; ++c) { s00 = mfma32(kx[c], qf[0][c], s00); s10 = mfma32(kx[c], qf[1][c], s10); s11 = mfma32(kx[4 + c], qf[1][c], s11); }
    LAS unsigned char* img = lds + wave * 6144;
    wait_ge(ctl + C_PROG + 32 * hh, (unsigned)(seg + 1), ctl, dead);
    QS(0);
    {
        const unsigned pg = P_HAND + (seg >> 2), sb = pg * PAGE + (seg & 3) * 524288u, sx = ((sidemask >> pg) & 1u) << 13;
        v4u t[6];
        for (unsigned spins = 0;;) {
            unsigned m = 0;
#pragma unroll
            for (int q = 0; q < 6; ++q) t[q] = ld16(rs, amap(L_QKV + hh * 6144 + q * 1024 + lane * 16) ^ sx, sb);
#pragma unroll
            for (int q = 0; q < 6; ++q) m = mx4(t[q], m);
            if (!any_sent(m) || dead) break;
            if (poll_fail(spins, ctl, 128)) dead = true;
            asm volatile("" ::: "memory");
        }
#pragma unroll
        for (int q = 0; q < 6; ++q) *(LAS v4u*)(img + q * 1024 + lane * 16) = t[q];
        asm volatile("s_waitcnt lgkmcnt(0)" ::: "memory");
    }
    QS(1);
    LAS unsigned char* kb = img + (4 * g4 + (n16 >> 2)) * 32 + 16 + (lane & 1) * 8;
#pragma unroll
    for (int c = 0; c < 4; ++c) {
        const v2u ka = trrd(kb + c * 1024), kc = trrd(kb + c * 1024 + 512);
        const v4u kf = {ka.x, ka.y, kc.x, kc.y};
        sm0 = mfma32(kf, qf[0][c], sm0); sm1 = mfma32(kf, qf[1][c], sm1);
    }
    v4u b1[2]; v2u b2[2];
#pragma unroll
    for (int m = 0; m < 2; ++m) {
        float v[12];
#pragma unroll
        for (int e = 0; e < 4; ++e) {
            const bool diag = 4 * g4 + e <= n16;
            v[e] = rnd(rnd(m ? s10[e] : s00[e]) * SCALE); if (m == 0 && !diag) v[e] = -INFINITY;
            v[4 + e] = (m && diag) ? rnd(rnd(s11[e]) * SCALE) : -INFINITY;
            v[8 + e] = g4 < 2 ? rnd(rnd(m ? sm1[e] : sm0[e]) * SCALE) : -INFINITY;
        }
        float mx = v[0];
#pragma unroll
        for (int e = 1; e < 12; ++e) mx = fmaxf(mx, v[e]);
        mx = sw16max(mx); mx = sw32max(mx);
        const float ml = -mx * LOG2E; float l = 0.f;
#pragma unroll
        for (int e = 0; e < 12; ++e) { v[e] = __builtin_amdgcn_exp2f(fmaf(v[e], LOG2E, ml)); l += v[e]; }
        l = sw16sum(l, l); l = sw32sum(l, l);
        const float ri = 1.f / l;
        b1[m] = (v4u){pk2(v[0] * ri, v[1] * ri), pk2(v[2] * ri, v[3] * ri), pk2(v[4] * ri, v[5] * ri), pk2(v[6] * ri, v[7] * ri)};
        b2[m] = (v2u){pk2(v[8] * ri, v[9] * ri), pk2(v[10] * ri, v[11] * ri)};
    }
#pragma unroll
    for (int c = 0; c < 4; ++c) {
        f32x4 o[2][2];
#pragma unroll
        for (int t = 0; t < 2; ++t) {
            const v2u va = *(LAS v2u*)(img + 4096 + ((2 * c + t) * 16 + n16) * 16 + (g4 & 1) * 8);
#pragma unroll
            for (int m = 0; m < 2; ++m) { o[m][t] = mfma32(vx[2 * c + t], b1[m], z); o[m][t] = mfma16(va, b2[m], o[m][t]); }
        }
#pragma unroll
        for (int m = 0; m < 2; ++m) st16(pk8(o[m][0], o[m][1]), rs, (n16 * 4096 + 8 * g4) * 2, O_OX + ((seg * 32 + m * 16) * 4096 + hh * 128 + c * 32) * 2);
    }
    QS(2);
    asm volatile("s_waitcnt vmcnt(0)" ::: "memory");
    QS(3);
    if (lane == 0) __hip_atomic_fetch_add(ctl + (seg < SF0 ? C_OC + 16 * ((seg >> 2) * 4 + (hh >> 3)) : C_OF + 16 * ((seg - SF0) * 8 + (hh >> 2))), 1u, RLX_AGENT);
}

DEVI void top2(const float* R, int& i0, int& i1) {
    const int lane = otid() & 63;
    float b0 = -INFINITY, b1 = -INFINITY; int j0 = 0x7fffffff, j1 = 0x7fffffff;
    f32x4 rv[8];
#pragma unroll
    for (int q = 0; q < 8; ++q) rv[q] = *(const f32x4*)(R + lane * 32 + q * 4);
#pragma unroll
    for (int q = 0; q < 32; ++q) {
        const float v = rv[q >> 2][q & 3]; const int k = lane * 32 + q;
        const bool f0 = v > b0 || j0 == 0x7fffffff, f1 = v > b1 || j1 == 0x7fffffff;
        b1 = f0 ? b0 : (f1 ? v : b1); j1 = f0 ? j0 : (f1 ? k : j1); b0 = f0 ? v : b0; j0 = f0 ? k : j0;
    }
    for (int m = 1; m < 64; m <<= 1) {
        const float c0 = __shfl_xor(b0, m), c1 = __shfl_xor(b1, m); const int k0 = __shfl_xor(j0, m), k1 = __shfl_xor(j1, m);
        auto gt = [](float a, int ja, float b, int jb) { return a > b || (!(b > a) && ja < jb); };
        if (gt(c0, k0, b0, j0)) { if (gt(b0, j0, c1, k1)) { b1 = b0; j1 = j0; } else { b1 = c1; j1 = k1; } b0 = c0; j0 = k0; }
        else if (gt(c0, k0, b1, j1)) { b1 = c0; j1 = k0; }
    }
    i0 = __builtin_amdgcn_readfirstlane(j0); i1 = __builtin_amdgcn_readfirstlane(j1);
}
DEVI void ffn1_unit(const Params& p, LAS unsigned char* lds, int u, int i0, int i1) {
    const int tid = otid(), co = tid & 7, ksl = tid >> 3, col = u * 64 + co * 8;
    LAS float* red = (LAS float*)lds;
    float a[4][8] = {};
#pragma unroll 4
    for (int k = ksl; k < D_; k += 64) {
        const v4u wg = *(const v4u*)(p.wg + (size_t)k * F_ + col), wu = *(const v4u*)(p.wu + (size_t)k * F_ + col);
        const float x0 = bf2f(p.x[i0 * D_ + k]), x1 = bf2f(p.x[i1 * D_ + k]);
#pragma unroll
        for (int e = 0; e < 4; ++e) {
            a[0][2 * e] += x0 * lo16(wg[e]); a[0][2 * e + 1] += x0 * hi16(wg[e]); a[1][2 * e] += x1 * lo16(wg[e]); a[1][2 * e + 1] += x1 * hi16(wg[e]);
            a[2][2 * e] += x0 * lo16(wu[e]); a[2][2 * e + 1] += x0 * hi16(wu[e]); a[3][2 * e] += x1 * lo16(wu[e]); a[3][2 * e + 1] += x1 * hi16(wu[e]);
        }
    }
#pragma unroll
    for (int q = 0; q < 4; ++q)
#pragma unroll
        for (int e = 0; e < 8; ++e) red[(ksl * 4 + q) * 64 + co * 8 + e] = a[q][e];
    BAR();
    if (tid < 128) {
        const int t = tid >> 6, c = tid & 63; float g = 0.f, uu = 0.f;
        for (int k = 0; k < 64; ++k) { g += red[(k * 4 + t) * 64 + c]; uu += red[(k * 4 + 2 + t) * 64 + c]; }
        g = rnd(g); uu = rnd(uu); g = rnd(g / (1.f + __expf(-g)));
        __hip_atomic_store((GAS float*)(p.ws + O_FH) + t * F_ + u * 64 + c, rnd(g * uu), RLX_AGENT);
    }
    announce((GAS unsigned*)(p.ws + O_CTL) + C_F1);
}
DEVI void ffn2_unit(const Params& p, LAS unsigned char* lds, int u, bool& dead) {
    const int tid = otid(), co = tid & 3, ksl = tid >> 2, col = u * 32 + co * 8;
    GAS unsigned* ctl = (GAS unsigned*)(p.ws + O_CTL);
    LAS float* red = (LAS float*)lds;
    v4u w[28];
#pragma unroll
    for (int q = 0; q < 28; ++q) w[q] = *(const v4u*)(p.wd + (size_t)(ksl + q * 128) * D_ + col);
    wait_ge(ctl + C_F1, 56u, ctl, dead);
    GAS float* hb = (GAS float*)(p.ws + O_FH);
    float a[2][8] = {};
#pragma unroll
    for (int q = 0; q < 28; ++q) {
        const float x0 = __hip_atomic_load(hb + ksl + q * 128, RLX_AGENT), x1 = __hip_atomic_load(hb + F_ + ksl + q * 128, RLX_AGENT);
#pragma unroll
        for (int e = 0; e < 4; ++e) { a[0][2 * e] += x0 * lo16(w[q][e]); a[0][2 * e + 1] += x0 * hi16(w[q][e]); a[1][2 * e] += x1 * lo16(w[q][e]); a[1][2 * e + 1] += x1 * hi16(w[q][e]); }
    }
#pragma unroll
    for (int q = 0; q < 2; ++q)
#pragma unroll
        for (int e = 0; e < 8; ++e) red[(ksl * 2 + q) * 32 + co * 8 + e] = a[q][e];
    BAR();
    if (tid < 64) {
        const int t = tid >> 5, c = tid & 31; float y = 0.f;
        for (int k = 0; k < 128; ++k) y += red[(k * 2 + t) * 32 + c];
        __hip_atomic_store((GAS float*)(p.ws + O_FY) + t * D_ + u * 32 + c, rnd(y), RLX_AGENT);
    }
    announce(ctl + C_F2);
}

DEVI void wo_unit(const Params& p, LAS unsigned char* lds, int rbk, int ct, int kq, int i0, int i1, bool& dead) {
    const int tid = otid(), lane = tid & 63, wave = __builtin_amdgcn_readfirstlane(tid >> 6), wm = wave >> 1, wn = wave & 1;
    GAS unsigned* ctl = (GAS unsigned*)(p.ws + O_CTL);
    const __amdgpu_buffer_rsrc_t rs = __builtin_amdgcn_make_buffer_rsrc(p.ws, 0, 0xFFFFFF00u, 0x00020000);
    const int r0 = rbk * 128, c0 = ct * 128, tile = rbk * 8 + ct;
    if (wave == 0) {
        wait_ge(ctl + C_OC + 16 * (rbk * 4 + kq), 32u, ctl, dead);
        __builtin_amdgcn_fence(__ATOMIC_ACQUIRE, "agent");
        asm volatile("s_waitcnt vmcnt(0)" ::: "memory");
    }
    BAR();
    Gemm<128, 128, 4, true, true> g; g.run(lds, (const u16*)(p.ws + O_OX) + (size_t)r0 * 4096 + kq * 1024, 4096, p.wo + (size_t)kq * 1024 * D_ + c0, 1024, 16);
    const unsigned slab = O_SLAB + tile * 262144u;
#pragma unroll
    for (int m = 0; m < 2; ++m)
#pragma unroll
        for (int n = 0; n < 4; ++n) st16(__builtin_bit_cast(v4u, g.acc[m][n]), rs, tid * 16 + (m * 4 + n) * 8192, slab + kq * 65536u);
    asm volatile("s_waitcnt vmcnt(0) lgkmcnt(0)\n\ts_barrier" ::: "memory");
    if (tid == 0) *(LAS unsigned*)lds = __hip_atomic_fetch_add(ctl + C_TK + 16 * tile, 1u, RLX_AGENT);
    BAR();
    const unsigned tk = __builtin_amdgcn_readfirstlane(*(LAS unsigned*)lds);
    if (tk != 3u) return;
    const float* R = (const float*)(p.ws + O_R); const float* G = (const float*)(p.ws + O_G); GAS float* Y = (GAS float*)(p.ws + O_FY);
    if ((i0 >> 7) == rbk || (i1 >> 7) == rbk) wait_ge(ctl + C_F2, 32u, ctl, dead);
#pragma unroll
    for (int m = 0; m < 2; ++m) {
        f32x4 s[4][4];
#pragma unroll
        for (int q = 0; q < 4; ++q)
#pragma unroll
            for (int n = 0; n < 4; ++n) s[q][n] = __builtin_bit_cast(f32x4, ld16(rs, tid * 16 + (m * 4 + n) * 8192, slab + q * 65536u));
        const int row = r0 + wm * 32 + m * 16 + (lane & 15);
        const float gv = G[row], og = rnd(1.f - gv);
        const bool sel = row == i0 || row == i1;
        const float rs_ = sel ? rnd(1.f / (1.f + __expf(-R[row]))) : 0.f;
#pragma unroll
        for (int n = 0; n < 4; ++n) {
            const f32x4 a = (s[0][n] + s[1][n]) + (s[2][n] + s[3][n]);
            const int col = c0 + wn * 64 + n * 16 + (lane >> 4) * 4;
            const v2u xv = *(const v2u*)(p.x + row * D_ + col);
            float b[4] = {lo16(xv.x), hi16(xv.x), lo16(xv.y), hi16(xv.y)}, o[4];
#pragma unroll
            for (int e = 0; e < 4; ++e) {
                if (sel) b[e] = rnd(b[e] + rnd(__hip_atomic_load(Y + (row == i0 ? 0 : D_) + col + e, RLX_AGENT) * rs_));
                o[e] = rnd(gv * rnd(a[e])) + rnd(og * b[e]);
            }
            *(v2u*)(p.out + row * D_ + col) = (v2u){pk2(o[0], o[1]), pk2(o[2], o[3])};
        }
    }
}

DEVI void wo_fast_unit(const Params& p, LAS unsigned char* lds, int ct, int k8, int i0, int i1, bool& dead) {
    const int tid = otid(), lane = tid & 63, wave = __builtin_amdgcn_readfirstlane(tid >> 6), fr = lane & 15, fg = lane >> 4;
    GAS unsigned* ctl = (GAS unsigned*)(p.ws + O_CTL);
    const __amdgpu_buffer_rsrc_t rs = __builtin_amdgcn_make_buffer_rsrc(p.ws, 0, 0xFFFFFF00u, 0x00020000);
    {
        const unsigned lbase = (unsigned)(uintptr_t)lds + wave * 2048;
        const u16* gb = p.wo + (size_t)k8 * 512 * D_ + ct * 128;
#pragma unroll
        for (int q = 0; q < 2; ++q) {
            const int k = wave * 8 + (lane >> 4) + 4 * q;
            const int pk = (k & 32) + 16 * ((k >> 2) & 1) + 4 * ((k >> 3) & 3) + (k & 3), f = (k & 3) | (((k >> 3) & 1) << 2);
            const unsigned vb = (pk * D_ + ((((lane & 15) >> 1) ^ f) * 16 + (lane & 1) * 8)) * 2;
#pragma unroll
            for (int kt = 0; kt < 8; ++kt) glds(vb, gb + (size_t)kt * 64 * D_, lbase + kt * 16384 + q * 1024);
        }
    }
    wait_ge(ctl + C_INIT, 128u, ctl, dead);
    if (i0 >= SF0 * 32 || i1 >= SF0 * 32) wait_ge(ctl + C_F2, 32u, ctl, dead);
    asm volatile("s_waitcnt vmcnt(0)\n\ts_barrier" ::: "memory");
    v4u bf[16];
    {
        const int q4 = fr >> 2, p4 = fr & 3, fsw = q4 | ((fg & 1) << 2);
        LAS unsigned char* bp = lds + (8 * fg + q4) * 256 + p4 * 8 + ((fsw ^ wave) * 32);
#pragma unroll
        for (int q = 0; q < 16; ++q) { const v2u x = trrd(bp + q * 8192), y = trrd(bp + q * 8192 + 1024); bf[q] = (v4u){x.x, x.y, y.x, y.y}; }
    }
    BAR();
    const int col = ct * 128 + k8 * 16 + fg * 4, mt = wave & 1;
    v4u t[8]; float gv; v2u xv;
    auto request = [&](int sg) {
        const int row = sg * 32 + mt * 16 + fr;
#pragma unroll
        for (int q = 0; q < 8; ++q) t[q] = ld16(rs, lane * 16 + q * 1024, O_S2 + (((((sg - SF0) * 8 + ct) * 2 + mt) * 8 + k8) * 8) * 1024u);
        gv = ((const float*)(p.ws + O_G))[row];
        xv = *(const v2u*)(p.x + row * D_ + col);
    };
    auto finish = [&](int sg) {
        const int row = sg * 32 + mt * 16 + fr;
        for (unsigned spins = 0;;) {
            unsigned m = 0;
#pragma unroll
            for (int q = 0; q < 8; ++q) m = mx4(t[q], m);
            if (!any_sent(m) || dead) break;
            if (poll_fail(spins, ctl, 64)) dead = true;
            asm volatile("" ::: "memory");
            request(sg);
        }
        float bb[4] = {lo16(xv.x), hi16(xv.x), lo16(xv.y), hi16(xv.y)};
        if (row == i0 || row == i1) {
            const float rs_ = rnd(1.f / (1.f + __expf(-((const float*)(p.ws + O_R))[row])));
#pragma unroll
            for (int e = 0; e < 4; ++e) bb[e] = rnd(bb[e] + rnd(__hip_atomic_load((GAS float*)(p.ws + O_FY) + (row == i0 ? 0 : D_) + col + e, RLX_AGENT) * rs_));
        }
        f32x4 a = __builtin_bit_cast(f32x4, t[0]);
#pragma unroll
        for (int q = 1; q < 8; ++q) a += __builtin_bit_cast(f32x4, t[q]);
        const float og = rnd(1.f - gv);
        float o[4];
#pragma unroll
        for (int e = 0; e < 4; ++e) o[e] = rnd(gv * rnd(a[e])) + rnd(og * bb[e]);
        *(v2u*)(p.out + row * D_ + col) = (v2u){pk2(o[0], o[1]), pk2(o[2], o[3])};
    };
    int ready = SF0;
    v4u ar[4];
    auto aload = [&](int sg) {
#pragma unroll
        for (int r = 0; r < 4; ++r) ar[r] = ld16(rs, lane * 16 + r * 8192, O_OX + (sg * 32 + wave * 4) * 8192u + k8 * 1024u);
    };
    for (int seg = SF0; seg < NSEG; ++seg) {
        const int rnd_ = seg - 56; (void)rnd_;
        const bool fin = seg >= SF0 + 2 && (wave >> 1) == (seg & 3);
        if (seg >= ready) {
            if (wave == ((2 * seg + 4) & 7)) {
                unsigned long long ok;
                for (unsigned spins = 0;;) {
                    const unsigned c = seg + lane < NSEG ? __hip_atomic_load(ctl + C_OF + 16 * ((seg + lane - SF0) * 8 + k8), RLX_AGENT) : 0u;
                    ok = __builtin_amdgcn_ballot_w64(c >= 4u);
                    if ((ok & 1ull) || dead) break;
                    if (poll_fail(spins, ctl, 64)) dead = true;
                }
                if (lane == 0) *(LAS int*)(lds + 65536) = seg + max(1, (int)__builtin_ctzll(~ok));
            }
            BAR();
            ready = __builtin_amdgcn_readfirstlane(*(LAS int*)(lds + 65536));
            aload(seg);
        }
        FS(0);
        LAS unsigned char* img = lds + (seg & 1) * 32768;
#pragma unroll
        for (int r = 0; r < 4; ++r) *(LAS v4u*)(img + (wave * 4 + r) * 1024 + ((lane ^ ((wave * 4 + r) & 15)) * 16)) = ar[r];
        SB();
        if (seg + 1 < ready) aload(seg + 1);
        if (fin) request(seg - 2);
        BAR();
        f32x4 c0 = {0, 0, 0, 0}, c1 = c0;
#pragma unroll
        for (int hq = 0; hq < 2; ++hq) {
            v4u a0[8], a1[8];
#pragma unroll
            for (int q = 0; q < 8; ++q) { LAS unsigned char* ap = img + fr * 1024 + ((((hq * 8 + q) * 4 + fg) ^ fr) * 16); a0[q] = *(LAS v4u*)ap; a1[q] = *(LAS v4u*)(ap + 16384); }
            SB();
#pragma unroll
            for (int q = 0; q < 8; ++q) { c0 = mfma32(bf[hq * 8 + q], a0[q], c0); c1 = mfma32(bf[hq * 8 + q], a1[q], c1); }
            SB();
        }
        FS(1);
        const unsigned sb2 = O_S2 + ((((seg - SF0) * 8 + ct) * 16 + wave) * 8 + k8) * 1024u;
        st16(nosent4(__builtin_bit_cast(v4u, c0)), rs, lane * 16, sb2);
        st16(nosent4(__builtin_bit_cast(v4u, c1)), rs, lane * 16, sb2 + 65536u);
        if (fin) finish(seg - 2);
    }
    if ((wave >> 1) < 2) { const int rnd_ = 6 + (wave >> 1); (void)rnd_; if (wave >> 1) __builtin_amdgcn_s_sleep(5); request(NSEG - 2 + (wave >> 1)); finish(NSEG - 2 + (wave >> 1)); FS(2); }
}

constexpr int Q_K2 = 64, Q_F1 = Q_K2 + 64, Q_QA = Q_F1 + 56, Q_F2 = Q_QA + 64, Q_MID = Q_F2 + 32, Q_WF = Q_MID + 5 * 96, Q_Q7 = Q_WF + 64, Q_W5 = Q_Q7 + 32, Q_END = Q_W5;
DEVI void worker_body(const Params& p, LAS unsigned char* lds, int w, int wdel) {
    const int tid = otid(), lane = tid & 63;
    GAS unsigned* ctl = (GAS unsigned*)(p.ws + O_CTL);
    const unsigned sv = lane < 21 ? (ctl[C_SIDE + lane] & 1u) : 0u;
    const unsigned sidemask = (unsigned)__builtin_amdgcn_ballot_w64(sv != 0);
    bool dead = false;
    const int hm = (w >> 5) * 16 + ((w & 31) >> 1);
    wait_l(rt() + wdel);
    kv_unit<64, 128>(p, lds, (w & 1) * 64, hm >> 1, hm & 1);
    EV(9);
    kv_unit<128, 128>(p, lds, 128 + (w & 1) * 128, hm >> 1, hm & 1);
    EV(10);
    { const int hp = (w >> 5) * 8 + ((w & 31) >> 2); kv_unit<256, 256>(p, lds, 384 + (w & 3) * 256, (hp >> 1) * 2, hp & 1); }
    EV(11);
    {
        const __amdgpu_buffer_rsrc_t rs = __builtin_amdgcn_make_buffer_rsrc(p.ws, 0, 0xFFFFFF00u, 0x00020000);
        const v4u zz = {SENT, SENT, SENT, SENT};
#pragma unroll
        for (int q = 0; q < 24; ++q) st16(zz, rs, otid() * 16 + q * 8192, O_S2 + w * 196608u);
        announce(ctl + C_INIT);
    }
    int i0, i1; top2((const float*)(p.ws + O_R), i0, i1);
    for (;;) {
        BAR();
        if (tid == 0) *(LAS unsigned*)(lds + 147456) = __hip_atomic_fetch_add(ctl + C_QUEUE, 1u, RLX_AGENT);
        BAR();
        int t = __builtin_amdgcn_readfirstlane(*(LAS unsigned*)(lds + 147456));
        if (t >= Q_END) break;
        EV(1000 + t);
        if (t < Q_K2) kv_unit<256, 256>(p, lds, 1408 + (t & 1) * 256, (t >> 2) * 2, (t >> 1) & 1);
        else if (t < Q_F1) kv_unit<128, 128>(p, lds, 1920, (t - Q_K2) >> 1, t & 1);
        else if (t < Q_QA) ffn1_unit(p, lds, t - Q_F1, i0, i1);
        else if (t >= Q_F2 && t < Q_MID) ffn2_unit(p, lds, t - Q_F2, dead);
        else if (t >= Q_WF && t < Q_Q7) wo_fast_unit(p, lds, (t - Q_WF) >> 3, (t - Q_WF) & 7, i0, i1, dead);
        else {
            int qa = -1, wo = 0;
            if (t < Q_F2) qa = t - Q_QA;
            else if (t < Q_WF) { const int pp = (t - Q_MID) / 96, r = t - Q_MID - pp * 96; if (r >= 64) qa = (pp + 2) * 32 + r - 64; else wo = pp * 64 + r; }
            else qa = 7 * 32 + t - Q_Q7;
            if (qa >= 0) qa_unit(p, lds, qa >> 5, qa & 31, sidemask, dead);
            else wo_unit(p, lds, wo >> 5, (wo >> 2) & 7, wo & 3, i0, i1, dead);
        }
    }
}

__global__ void __launch_bounds__(512, 2) k_main(Params p, Tim tm) {
    extern __shared__ __attribute__((aligned(16))) unsigned char smem[];
    LAS unsigned char* lds = (LAS unsigned char*)smem;
    if (threadIdx.x == 0) { *(LAS unsigned*)(lds + 147460) = rt(); *(LAS int*)(lds + 147456) = claim_slot((GAS unsigned*)(p.ws + O_CTL)); }
    __syncthreads();
    const int slot = __builtin_amdgcn_readfirstlane(*(LAS int*)(lds + 147456));
    EV(1);
    if (slot < 128) chain_body(p, tm, lds, slot >> 5, slot & 31, __builtin_amdgcn_readfirstlane(*(LAS unsigned*)(lds + 147460)));
    else worker_body(p, lds, slot - 128, tm.qs);
    EV(99);
}

#define TIM_INIT {32, 28, 40, 0, 0, 0, 0, 140, 0, 0}
extern "C" void kernel_launch(void* const* d_in, const int* in_sizes, int n_in, void* d_out, int out_size, void* d_ws, size_t ws_size, hipStream_t stream) {
    Params p;
    p.x = (const u16*)d_in[0]; p.mem0 = (const u16*)d_in[1]; p.wq = (const u16*)d_in[2]; p.wk = (const u16*)d_in[3]; p.wv = (const u16*)d_in[4]; p.wo = (const u16*)d_in[5];
    p.wr = (const u16*)d_in[8]; p.wg = (const u16*)d_in[9]; p.wu = (const u16*)d_in[10]; p.wd = (const u16*)d_in[11]; p.gw = (const u16*)d_in[12];
    p.out = (u16*)d_out;
    p.ws = (unsigned char*)(((uintptr_t)d_ws + PAGE - 1) & ~(uintptr_t)(PAGE - 1));
    Tim tm = TIM_INIT;
    constexpr int LDS_M = 147456 + 64;
    (void)hipFuncSetAttribute((const void*)k_main, hipFuncAttributeMaxDynamicSharedMemorySize, LDS_M);
    hipLaunchKernelGGL(k_prep, dim3(256), dim3(256), 0, stream, p);
    hipLaunchKernelGGL(k_main, dim3(256), dim3(512), LDS_M, stream, p, tm);
}
```

```cpp
#include <hip/hip_runtime.h>
#include <hip/hip_bf16.h>
#include <stdint.h>

typedef unsigned short u16;
typedef unsigned v4u __attribute__((ext_vector_type(4)));
typedef unsigned v2u __attribute__((ext_vector_type(2)));
typedef unsigned v8u __attribute__((ext_vector_type(8)));
typedef float f32x4 __attribute__((ext_vector_type(4)));
typedef __bf16 bf16x2 __attribute__((ext_vector_type(2)));
typedef __bf16 bf16x8 __attribute__((ext_vector_type(8)));
typedef __bf16 bf16x16 __attribute__((ext_vector_type(16)));
typedef short s16x4 __attribute__((ext_vector_type(4)));
#define GAS __attribute__((address_space(1)))
#define LAS __attribute__((address_space(3)))
#define RLX_AGENT __ATOMIC_RELAXED, __HIP_MEMORY_SCOPE_AGENT
#define DEVI __device__ __forceinline__
#define BAR() asm volatile("s_waitcnt lgkmcnt(0)\n\ts_barrier" ::: "memory")

constexpr int S_ = 2048, D_ = 1024, H_ = 32, DH = 128, NSEG = 64, F_ = 3584;
constexpr float SCALE = 0.08838834764831845f, LOG2E = 1.4426950408889634f;
constexpr unsigned SENT = 0xFFFFFFFFu;
constexpr unsigned PAGE = 2u << 20;
constexpr unsigned O_CTL = 0, O_R = 65536, O_G = O_R + 8192, O_FH = 131072, O_FY = O_FH + 32768;
constexpr unsigned P_HAND = 1, P_PART = 17;
constexpr unsigned O_KX = 22 * PAGE, O_VX = 38 * PAGE, O_OX = 54 * PAGE, O_S2 = 70 * PAGE  , O_DBG = 78 * PAGE;
constexpr unsigned L_QKV = 65536;
constexpr int C_ABORT = 0, C_ARRIVE = 64, C_T0 = 128, C_QUEUE = 192, C_XR = 256  , C_F1 = 768, C_F2 = 800, C_INIT = 832, C_SLOT = 1024  ,
              C_KV = 2048  , C_PROG = 6144  , C_OF = 7168  , C_ZEND = 15360,
              C_SIDE = 15360  ;
constexpr int SF0 = 0;

struct Params { const u16 *x, *mem0, *wq, *wk, *wv, *wo, *wr, *wg, *wu, *wd, *gw; u16* out; unsigned char* ws; };
struct Tim { int ta, tb, tc, m1, mq, m3, back, t0, qs, mr; };

#define FAR_A 0u
#define FAR_B 1u
DEVI float bf2f(u16 b) { return __uint_as_float((unsigned)b << 16); }
DEVI unsigned pk2(float lo, float hi) { bf16x2 v = {(__bf16)lo, (__bf16)hi}; return __builtin_bit_cast(unsigned, v); }
DEVI float rnd(float f) { return __uint_as_float(pk2(f, 0.f) << 16); }
DEVI float lo16(unsigned u) { return __uint_as_float(u << 16); }
DEVI float hi16(unsigned u) { return __uint_as_float(u & 0xffff0000u); }
DEVI int otid() { int t = threadIdx.x; asm volatile("" : "+v"(t)); return t; }
DEVI unsigned xcc_id() { return (unsigned)__builtin_amdgcn_s_getreg(20 | (3 << 11)) & 0xfu; }
DEVI unsigned rt() { return (unsigned)__builtin_amdgcn_s_memrealtime(); }
DEVI unsigned amap(unsigned lo) { return ((lo >> 13) << 14) + (lo & 8191); }
DEVI unsigned fb(unsigned blk, unsigned par) { return blk * 16384u + (((par ^ (blk >> 7)) & 1u) << 13); }
DEVI unsigned far_par(unsigned sidemask) { return __builtin_popcount((sidemask ^ 0x0AAAAAu) & 0x1FFFFEu) > 10 ? FAR_A : FAR_B; }
DEVI v4u ld16(__amdgpu_buffer_rsrc_t r, unsigned vo, unsigned so) { return __builtin_amdgcn_raw_buffer_load_b128(r, vo, so, 16); }
DEVI void st16(v4u v, __amdgpu_buffer_rsrc_t r, unsigned vo, unsigned so) {
    __builtin_amdgcn_raw_buffer_store_b128(v, r, vo, so, 16);
    asm volatile("s_nop 1" :: "v"(v));
}
DEVI void st8(v2u v, __amdgpu_buffer_rsrc_t r, unsigned vo, unsigned so) { __builtin_amdgcn_raw_buffer_store_b64(v, r, vo, so, 16); }
DEVI void st4(unsigned v, __amdgpu_buffer_rsrc_t r, unsigned vo, unsigned so) { __builtin_amdgcn_raw_buffer_store_b32(v, r, vo, so, 16); }
DEVI unsigned umax3(unsigned a, unsigned b, unsigned c) { unsigned r; asm("v_max3_u32 %0, %1, %2, %3" : "=v"(r) : "v"(a), "v"(b), "v"(c)); return r; }
DEVI unsigned mx4(v4u v, unsigned m) { return umax3(umax3(v.x, v.y, m), v.z, v.w); }
DEVI bool any_sent(unsigned m) { return __builtin_amdgcn_ballot_w64(m == SENT) != 0; }
DEVI unsigned nosent(unsigned u) { return u < 0xFFFFFFFEu ? u : 0xFFFFFFFEu; }
DEVI v4u nosent4(v4u v) { return (v4u){nosent(v.x), nosent(v.y), nosent(v.z), nosent(v.w)}; }
DEVI f32x4 mfma32(v4u a, v4u b, f32x4 c) { return __builtin_amdgcn_mfma_f32_16x16x32_bf16(__builtin_bit_cast(bf16x8, a), __builtin_bit_cast(bf16x8, b), c, 0, 0, 0); }
DEVI f32x4 mfma16(v2u a, v2u b, f32x4 c) { return __builtin_amdgcn_mfma_f32_16x16x16bf16_1k(__builtin_bit_cast(s16x4, a), __builtin_bit_cast(s16x4, b), c, 0, 0, 0); }
DEVI f32x4 smf(v4u a, v8u b, f32x4 c, int idx) { return __builtin_amdgcn_smfmac_f32_16x16x64_bf16(__builtin_bit_cast(bf16x8, a), __builtin_bit_cast(bf16x16, b), c, idx, 0, 0); }
DEVI v2u trrd(LAS unsigned char* p) { return __builtin_bit_cast(v2u, __builtin_amdgcn_ds_read_tr16_b64_v4i16((LAS s16x4*)p)); }
DEVI float ror8(float v) { return __int_as_float(__builtin_amdgcn_update_dpp(0, __float_as_int(v), 0x128, 0xf, 0xf, false)); }
DEVI float xor1(float v) { return __int_as_float(__builtin_amdgcn_update_dpp(0, __float_as_int(v), 0xB1, 0xf, 0xf, false)); }
DEVI float sw32sum(float a, float b) { auto r = __builtin_amdgcn_permlane32_swap(__float_as_uint(a), __float_as_uint(b), false, false); return __uint_as_float(r[0]) + __uint_as_float(r[1]); }
DEVI float sw16sum(float a, float b) { auto r = __builtin_amdgcn_permlane16_swap(__float_as_uint(a), __float_as_uint(b), false, false); return __uint_as_float(r[0]) + __uint_as_float(r[1]); }
DEVI float sw32max(float a) { auto r = __builtin_amdgcn_permlane32_swap(__float_as_uint(a), __float_as_uint(a), false, false); return fmaxf(__uint_as_float(r[0]), __uint_as_float(r[1])); }
DEVI float sw16max(float a) { auto r = __builtin_amdgcn_permlane16_swap(__float_as_uint(a), __float_as_uint(a), false, false); return fmaxf(__uint_as_float(r[0]), __uint_as_float(r[1])); }
DEVI f32x4 fold(f32x4 a, f32x4 b) { f32x4 r; r.x = sw32sum(a.x, b.x); r.y = sw32sum(a.y, b.y); r.z = sw32sum(a.z, b.z); r.w = sw32sum(a.w, b.w); return r; }
DEVI float dot2(unsigned a, unsigned b, float c) { return __builtin_amdgcn_fdot2_f32_bf16(__builtin_bit_cast(bf16x2, a), __builtin_bit_cast(bf16x2, b), c, false); }
DEVI unsigned cvtpk(float lo, float hi) { unsigned r; asm("v_cvt_pk_bf16_f32 %0, %1, %2" : "=v"(r) : "v"(lo), "v"(hi)); return r; }
DEVI float max5x8(float a, float b, float c, float d, float e) {
    float m, t;
    asm("v_max3_f32 %0, %2, %3, %4\n\tv_max3_f32 %0, %0, %5, %6\n\ts_nop 1\n\t"
        "v_max_f32_dpp %0, %0, %0 row_ror:8 row_mask:0xf bank_mask:0xf\n\tv_mov_b32 %1, %0\n\ts_nop 1\n\t"
        "v_permlane16_swap_b32 %0, %1\n\tv_max_f32 %0, %0, %1\n\tv_mov_b32 %1, %0\n\ts_nop 1\n\t"
        "v_permlane32_swap_b32 %0, %1\n\tv_max_f32 %0, %0, %1"
        : "=&v"(m), "=&v"(t) : "v"(a), "v"(b), "v"(c), "v"(d), "v"(e));
    return m;
}
#define ALD16(D, VO, SO, IMM) asm volatile("buffer_load_dwordx4 %0, %1, %2, %3 offen offset:" #IMM " sc1" : "=v"(D) : "v"(VO), "s"(rd), "s"(SO) : "memory")
#define ALD4(D, VO, SO, IMM) asm volatile("buffer_load_dword %0, %1, %2, %3 offen offset:" #IMM " sc1" : "=v"(D) : "v"(VO), "s"(rd), "s"(SO) : "memory")
#define WAITV1(N, A) asm volatile("s_waitcnt vmcnt(" #N ")" : "+v"(A) :: "memory")
#define WAITV2(N, A, B) asm volatile("s_waitcnt vmcnt(" #N ")" : "+v"(A), "+v"(B) :: "memory")
#define SB() __builtin_amdgcn_sched_barrier(0)
#define PIN2(a, b) asm volatile("" : "+v"(a), "+v"(b))
DEVI void sleep_units(int n) {
    if (n & 32) __builtin_amdgcn_s_sleep(32);
    if (n & 16) __builtin_amdgcn_s_sleep(16);
    if (n & 8) __builtin_amdgcn_s_sleep(8);
    if (n & 4) __builtin_amdgcn_s_sleep(4);
    if (n & 2) __builtin_amdgcn_s_sleep(3);
    if (n & 1) __builtin_amdgcn_s_sleep(1);
}
DEVI void sleep_cyc(int n) { sleep_units(min(n >> 6, 63)); }
DEVI unsigned mt() { return (unsigned)__builtin_amdgcn_s_memtime(); }
DEVI void wait_l(unsigned t) {
    unsigned d;
    asm volatile("1:\n\ts_memrealtime vcc\n\ts_waitcnt lgkmcnt(0)\n\ts_sub_i32 %0, vcc_lo, %1\n\ts_cmp_lt_i32 %0, 0\n\ts_cbranch_scc1 1b"
                 : "=&s"(d) : "s"(__builtin_amdgcn_readfirstlane(t)) : "memory", "scc", "vcc");
}
constexpr unsigned SPINCAP = 40000;
DEVI bool poll_fail(unsigned& spins, GAS unsigned* ctl, int back) {
    sleep_cyc(back);
    ++spins;
    if ((spins & 63u) == 0u) {
        if (spins > SPINCAP) { __hip_atomic_store(ctl + C_ABORT, 1u, RLX_AGENT); return true; }
        if (__builtin_amdgcn_readfirstlane(__hip_atomic_load(ctl + C_ABORT, RLX_AGENT))) return true;
    }
    return false;
}

#define EV(code)
#define EVT 0
DEVI void wait_ge(GAS unsigned* w, unsigned v, GAS unsigned* ctl, bool& dead) {
    unsigned spins = 0;
    while (!dead && (unsigned)__builtin_amdgcn_readfirstlane(__hip_atomic_load(w, RLX_AGENT)) < v) { if (poll_fail(spins, ctl, 64)) dead = true; }
    asm volatile("" ::: "memory");
}

__global__ void __launch_bounds__(256) k_prep(Params p) {
    const int tid = threadIdx.x, lane = tid & 63, wave = tid >> 6, b = blockIdx.x;
    unsigned* ctl = (unsigned*)(p.ws + O_CTL);
    if (b >= 196) ctl[(b - 196) * 256 + tid] = 0u;
    if (b < 160 && tid == 192) {
        const unsigned page = 1 + (b >> 3);
        __amdgpu_buffer_rsrc_t rs = __builtin_amdgcn_make_buffer_rsrc(p.ws, 0, 0xFFFFFF00u, 0x00020000);
        unsigned t[2] = {0xffffffffu, 0xffffffffu};
        for (int rep = 0; rep < 6; ++rep)
            for (int side = 0; side < 2; ++side) {
                const unsigned long long c0 = __builtin_amdgcn_s_memtime();
                asm volatile("s_waitcnt lgkmcnt(0)" ::: "memory");
                st4(SENT, rs, page * PAGE + 480 * 1024 + side * 8192 + (b & 7) * 512, 0);
                asm volatile("s_waitcnt vmcnt(0)" ::: "memory");
                const unsigned long long c1 = __builtin_amdgcn_s_memtime();
                t[side] = min(t[side], (unsigned)(c1 - c0));
            }
        ctl[C_SIDE + page] = (t[1] < t[0] ? 1u : 0u) ^ (xcc_id() >= 4 ? 1u : 0u);
    }
    float* R = (float*)(p.ws + O_R); float* G = (float*)(p.ws + O_G);
    for (int rr = 0; rr < 2; ++rr) {
        const int row = b * 8 + wave * 2 + rr;
        float a = 0.f, g = 0.f;
#pragma unroll
        for (int q = 0; q < 2; ++q) {
            const int k = q * 512 + lane * 8;
            const v4u xv = *(const v4u*)(p.x + row * D_ + k), wr = *(const v4u*)(p.wr + k), gw = *(const v4u*)(p.gw + k);
#pragma unroll
            for (int e = 0; e < 4; ++e) {
                a = fmaf(lo16(xv[e]), lo16(wr[e]), a); a = fmaf(hi16(xv[e]), hi16(wr[e]), a);
                const unsigned pr = pk2(lo16(xv[e]) * lo16(gw[e]), hi16(xv[e]) * hi16(gw[e]));
                g += lo16(pr); g += hi16(pr);
            }
        }
#pragma unroll
        for (int m = 32; m >= 1; m >>= 1) { a += __shfl_xor(a, m); g += __shfl_xor(g, m); }
        if (lane == 0) { R[row] = rnd(a); g = rnd(g); G[row] = rnd(1.f / (1.f + __expf(-g))); }
    }
}

constexpr unsigned LC_PA = 0, LC_QK = 24576, LC_V = LC_QK + 4096, LC_O = LC_V + 2048, LC_MISC = 98304 + 16, LC_KV = 33792  , LC_END = LC_KV + 32768;

DEVI void chain_body(const Params& p, const Tim& tm, LAS unsigned char* lds, const int g, const int i, const unsigned tbase) {
    const int tid = threadIdx.x, lane = tid & 63, wave = __builtin_amdgcn_readfirstlane(tid >> 6);
    const int h = g * 8 + (i >> 2), j = i & 3, cid = h * 4 + j;
    const int kw = (wave + i) & 7;
    GAS unsigned* ctl = (GAS unsigned*)(p.ws + O_CTL);
    const __amdgpu_buffer_rsrc_t rs = __builtin_amdgcn_make_buffer_rsrc(p.ws, 0, 0xFFFFFF00u, 0x00020000);
    unsigned sidemask = 0;
    { const unsigned sv = lane < 21 ? (ctl[C_SIDE + lane] & 1u) : 0u; sidemask = (unsigned)__builtin_amdgcn_ballot_w64(sv != 0); }
    v8u WA[6][2], WO[2][2];
    v4u sa[2][12], so[8];
    {
        const u16* wm[3] = {p.wq, p.wk, p.wv};
#pragma unroll
        for (int ks = 0; ks < 2; ++ks)
#pragma unroll
            for (int m = 0; m < 3; ++m)
#pragma unroll
                for (int q = 0; q < 4; ++q)
                    sa[ks][m * 4 + q] = *(const v4u*)(wm[m] + (size_t)(128 * kw + 64 * ks + 16 * q + (lane >> 2)) * 4096 + h * 128 + 32 * j + (lane & 3) * 8);
#pragma unroll
        for (int q = 0; q < 8; ++q) so[q] = *(const v4u*)(p.wo + (size_t)(h * 128 + 16 * q + (lane >> 2)) * 1024 + 256 * j + 32 * wave + (lane & 3) * 8);
    }
    SB();
    const int tau = lane >> 5, rh = (lane >> 4) & 1, n16 = lane & 15, c8 = (lane >> 3) & 1, g4 = lane >> 4;
    const unsigned o_gm = amap(g * 16384 + kw * 2048 + lane * 16);
    const unsigned qkvb = L_QKV + h * 6144;
    const int pcol = 256 * j + 32 * wave + 16 * tau + n16;
    const unsigned o_pp = amap((pcol >> 3) * 4096 + h * 128 + (rh * 8 + (pcol & 7)) * 8);
    const int sidx = c8 ? 0xEEEE : 0x4444;
    LAS unsigned char* const pw = lds + LC_PA + ((wave * 2 + rh) * 96 + 16 * tau + n16) * 16;
    LAS unsigned char* const ob = lds + LC_O + (lane & 7) * 320 + c8 * 16 + g4 * 32;
    const v4u rd = {(unsigned)(uintptr_t)p.ws, (unsigned)((uintptr_t)p.ws >> 32) & 0xffffu, 0xFFFFFF00u, 0x00020000u};
    const unsigned o_i1 = amap(qkvb + j * 1024 + lane * 16);
    const unsigned o_i2 = lane < 32 ? amap(qkvb + 4096 + j * 512 + lane * 16) : amap(((lane >> 3) & 3) * 16384 + cid * 128 + (lane & 7) * 16);
    {
        const v4u zz = {SENT, SENT, SENT, SENT};
        if (wave < 2) {
            const unsigned sb = P_HAND * PAGE + wave * 524288u, sx = ((sidemask >> P_HAND) & 1u) << 13;
            st16(zz, rs, o_i1 ^ sx, sb);
            v4u iv = zz;
            if (wave == 0 && lane >= 32) {
                iv = *(const v4u*)(p.mem0 + (lane & 7) * 1024 + cid * 8);
                iv = (v4u){nosent(iv.x), nosent(iv.y), nosent(iv.z), nosent(iv.w)};
            }
            st16(iv, rs, o_i2 ^ sx, sb);
        }
#pragma unroll
        for (int sl = 0; sl < 2; ++sl) st8((v2u){SENT, SENT}, rs, o_pp ^ (((sidemask >> (P_PART + sl)) & 1u) << 13), (P_PART + sl) * PAGE);
    }
    bool dead = false;
    const unsigned o_kv = (wave & 2 ? O_VX : O_KX) + (wave & 1) * 4096 + lane * 16, fpar = far_par(sidemask);
    v4u p0, p1, p2, p3;
    if (wave >= 4) wait_ge(ctl + C_KV + 8 * h, 4u, ctl, dead);
    asm volatile("s_waitcnt vmcnt(0)" ::: "memory");
    if (wave >= 4) { const unsigned so = fb(h, fpar); ALD16(p0, o_kv, so, 0); ALD16(p1, o_kv, so, 1024); ALD16(p2, o_kv, so, 2048); ALD16(p3, o_kv, so, 3072); }
    BAR();
    EV(2);
    if (tid == 0) {
        if (__hip_atomic_fetch_add(ctl + C_ARRIVE, 1u, RLX_AGENT) == 127u) __hip_atomic_store(ctl + C_T0, ((rt() + tm.t0) & ~3u) | 0x80000000u, RLX_AGENT);
    }
    SB();
    {
        LAS unsigned char* img = lds + wave * 12288;
        const int gB = lane >> 4, q4 = (lane & 15) >> 2, p4 = lane & 3;
        const unsigned rbase = (16 * (gB >> 1) + 4 * (gB & 1) + (q4 & 1) + 8 * (q4 >> 1)) * 64 + p4 * 8;
#pragma unroll
        for (int ks = 0; ks < 2; ++ks) {
#pragma unroll
            for (int q = 0; q < 12; ++q) *(LAS v4u*)(img + q * 1024 + lane * 16) = sa[ks][q];
            asm volatile("s_waitcnt lgkmcnt(0)" ::: "memory");
#pragma unroll
            for (int t = 0; t < 6; ++t)
#pragma unroll
                for (int m = 0; m < 4; ++m) {
                    const v2u r = trrd(img + (t >> 1) * 4096 + (t & 1) * 32 + (32 * (m >> 1) + 2 * (m & 1)) * 64 + rbase);
                    WA[t][ks][2 * m] = r.x; WA[t][ks][2 * m + 1] = r.y;
                }
            asm volatile("s_waitcnt lgkmcnt(0)" ::: "memory");
        }
#pragma unroll
        for (int q = 0; q < 8; ++q) *(LAS v4u*)(img + q * 1024 + lane * 16) = so[q];
        asm volatile("s_waitcnt lgkmcnt(0)" ::: "memory");
#pragma unroll
        for (int t = 0; t < 2; ++t)
#pragma unroll
            for (int ks = 0; ks < 2; ++ks)
#pragma unroll
                for (int m = 0; m < 4; ++m) {
                    const v2u r = trrd(img + t * 32 + (64 * ks + 32 * (m >> 1) + 2 * (m & 1)) * 64 + rbase);
                    WO[t][ks][2 * m] = r.x; WO[t][ks][2 * m + 1] = r.y;
                }
    }
#define STAMP(k)
#define STAMPN(k)
#define ROLL(n)
#define NSP(k, v)
#define DUMP()
#define LATE(k, c)
#define XREC()
#define HAND_ADDR(s) do { const unsigned pg = P_HAND + ((s) >> 2); sb = pg * PAGE + ((s) & 3) * 524288u; sx = ((sidemask >> pg) & 1u) << 13; } while (0)
#define RETRY(K, RELOAD, BAD) do { unsigned spins = 0;                                                                    \
                while (!dead) { if (poll_fail(spins, ctl, tm.back)) { dead = true; break; } RELOAD; if (!(BAD)) break; }  \
                NSP(K, spins); } while (0)
#define ZERO_A() f32x4 c0 = {0, 0, 0, 0}, c1 = c0, c2 = c0, c3 = c0, c4 = c0, c5 = c0; PIN2(c0, c1); PIN2(c2, c3); PIN2(c4, c5)
#define GATHER_MEM(EXTRA, NX) v4u a0, a1;                                                                                 \
            ALD16(a0, o_gmx, sb, 0); ALD16(a1, o_gmx, sb, 1024); EXTRA; WAITV2(NX, a0, a1);                               \
            if (__builtin_expect(any_sent(mx4(a1, mx4(a0, 0u))), 0))                                                      \
                RETRY(0, ALD16(a0, o_gmx, sb, 0); ALD16(a1, o_gmx, sb, 1024); WAITV2(0, a0, a1), any_sent(mx4(a1, mx4(a0, 0u))))
#define QK_MFMA() do {                                                                                                    \
            c0 = smf(a0, WA[0][0], c0, sidx); c1 = smf(a0, WA[1][0], c1, sidx); c0 = smf(a1, WA[0][1], c0, sidx); c1 = smf(a1, WA[1][1], c1, sidx); \
            SB();                                                                                                         \
            c2 = smf(a0, WA[2][0], c2, sidx); c3 = smf(a0, WA[3][0], c3, sidx); c2 = smf(a1, WA[2][1], c2, sidx); c3 = smf(a1, WA[3][1], c3, sidx); \
            SB(); *(LAS f32x4*)pw = fold(c0, c1); SB(); *(LAS f32x4*)(pw + 512) = fold(c2, c3); SB(); } while (0)
#define V_PART() do { c4 = smf(a0, WA[4][0], c4, sidx); c5 = smf(a0, WA[5][0], c5, sidx); c4 = smf(a1, WA[4][1], c4, sidx); c5 = smf(a1, WA[5][1], c5, sidx); \
            *(LAS f32x4*)(pw + 1024) = fold(c4, c5);                                                                      \
            if (lane == 0) __hip_atomic_fetch_add((LAS unsigned*)(lds + LC_MISC + 4), 1u, __ATOMIC_RELAXED, __HIP_MEMORY_SCOPE_WORKGROUP); } while (0)
#define ZERO_D() f32x4 d0 = {0, 0, 0, 0}, d1 = d0; const unsigned pp = P_PART + (s & 3), px = ((sidemask >> pp) & 1u) << 13; unsigned o_ppx = o_pp ^ px; PIN2(d0, d1); asm volatile("" : "+v"(o_ppx))
#define WO_STAGE() do {                                                                                                   \
            const v4u o0 = *(LAS v4u*)ob, o1 = *(LAS v4u*)(ob + 128); SB();                                               \
            d0 = smf(o0, WO[0][0], d0, sidx); d1 = smf(o0, WO[1][0], d1, sidx); d0 = smf(o1, WO[0][1], d0, sidx); d1 = smf(o1, WO[1][1], d1, sidx); \
            const f32x4 f = fold(d0, d1);                                                                                 \
            st8((v2u){nosent(pk2(f.x, f.y)), nosent(pk2(f.z, f.w))}, rs, o_ppx, pp * PAGE); } while (0)
#define REDUCE4(LP, OFF) do {                                                                                             \
            const f32x4 r0 = *(LAS f32x4*)(LP), r1 = *(LAS f32x4*)((LP) + 3072), r2 = *(LAS f32x4*)((LP) + 6144), r3 = *(LAS f32x4*)((LP) + 9216); \
            const f32x4 t = (r0 + r1) + (r2 + r3);                                                                        \
            st4(nosent(pk2(sw32sum(t.x, t.z), sw32sum(t.y, t.w))), rs, (OFF), sb); } while (0)
    unsigned sb, sx;
    if (wave < 4) {
        const unsigned o_gq = amap(qkvb + wave * 1024 + lane * 16);
        const unsigned o_gp = amap(cid * 4096 + lane * 16);
        const int crow = 4 * ((lane >> 2) & 1) + g4;
        v4u sel = {0u, 0u, 0u, 0u};
        sel[(lane & 7) >> 1] = (lane & 1) ? 0x3f800000u : 0x00003f80u;
        const unsigned o_pm = amap(c8 * 16384 + cid * 128 + crow * 16 + (lane & 3) * 4);
        const unsigned o_gv = amap(qkvb + 4096 + wave * 512 + n16 * 16 + (4 * (g4 & 1) + 2 * (g4 >> 1)) * 2);
        LAS unsigned char* const qb = lds + LC_QK + (4 * g4 + ((lane & 15) >> 2)) * 32 + (lane & 1) * 8;
        LAS unsigned char* const ow = lds + LC_O + (lane & 7) * 320 + (32 * wave + 16 * c8 + 4 * g4) * 2;
        if (tid == 0) *(LAS unsigned*)(lds + LC_MISC + 4) = 0u;
        BAR();
        EV(3);
        if (wave == 0) {
            unsigned plan, spins = 0;
            for (;;) { plan = __builtin_amdgcn_readfirstlane(__hip_atomic_load(ctl + C_T0, RLX_AGENT)); if (plan) break; if (poll_fail(spins, ctl, 64)) { dead = true; break; } }
            { const unsigned now = rt(); plan = now + ((int)((plan - now) << 1) >> 1); }
            wait_l(plan);
        }
        EV(4);
        v4u kx[8], vx[2];
        unsigned tq;
#define A_STAGE_A(S1)                                                                                                     \
            BAR();                                                                                  \
            STAMPN(0);                                                                                                    \
            GATHER_MEM({ LAS unsigned char* kvb = lds + LC_KV + ((S1) & 1) * 16384 + lane * 16;                           \
                _Pragma("unroll") for (int q = 0; q < 8; ++q) kx[q] = *(LAS v4u*)(kvb + q * 1024);                        \
                vx[0] = *(LAS v4u*)(kvb + 8192 + wave * 2048); vx[1] = *(LAS v4u*)(kvb + 8192 + wave * 2048 + 1024); SB(); }, 0); \
            STAMPN(1);                                                                                                    \
            QK_MFMA();                                                                                                    \
            BAR();                                                                                                 \
            tq = rt() + tm.ta;                                                                                            \
            STAMPN(2);                                                                                                    \
            V_PART()
        {
            HAND_ADDR(0);
            ZERO_A();
            const unsigned o_gmx = o_gm ^ sx;
            A_STAGE_A(0);
        }
        for (int s = 0; s < NSEG - 1; ++s) {
            __builtin_amdgcn_s_setprio(3);
            ROLL(3); STAMP(3);
            v4u gq;
            ZERO_D();
            unsigned o_gqx = o_gq ^ sx, o_gvx = o_gv ^ sx; PIN2(o_gqx, o_gvx);
            wait_l(tq);
            STAMP(4);
            ALD16(gq, o_gqx, sb, 0); WAITV1(0, gq);
            if (__builtin_expect(any_sent(mx4(gq, 0u)), 0)) RETRY(1, ALD16(gq, o_gqx, sb, 0); WAITV1(0, gq), any_sent(mx4(gq, 0u)));
            *(LAS v4u*)(lds + LC_QK + wave * 1024 + lane * 16) = gq;
            STAMP(5);
            BAR();
            {
                unsigned va, vc;
                ALD4(va, o_gvx, sb, 0); ALD4(vc, o_gvx, sb, 256);
                v4u qf[4], kf[4];
#pragma unroll
                for (int c = 0; c < 4; ++c) { const v2u qa = trrd(qb + c * 1024), qc = trrd(qb + c * 1024 + 512); qf[c] = (v4u){qa.x, qa.y, qc.x, qc.y}; }
#pragma unroll
                for (int c = 0; c < 4; ++c) { const v2u ka = trrd(qb + c * 1024 + 16), kc = trrd(qb + c * 1024 + 528); kf[c] = (v4u){ka.x, ka.y, kc.x, kc.y}; }
                SB();
                f32x4 s0 = {0, 0, 0, 0}, s1 = s0, sm = s0;
#pragma unroll
                for (int c = 0; c < 4; ++c) { s0 = mfma32(kx[c], qf[c], s0); s1 = mfma32(kx[4 + c], qf[c], s1); sm = mfma32(kf[c], qf[c], sm); }
                SB();
                float x0 = c8 ? s1.x : s0.x, x1 = c8 ? s1.y : s0.y, x2 = c8 ? s1.z : s0.z, x3 = c8 ? s1.w : s0.w;
                float xm = tau ? (c8 ? sm.w : sm.z) : (c8 ? sm.y : sm.x);
                constexpr float CS = SCALE * LOG2E;
                const float ml = -CS * max5x8(x0, x1, x2, x3, xm);
                STAMP(6);
                x0 = __builtin_amdgcn_exp2f(fmaf(x0, CS, ml)); x1 = __builtin_amdgcn_exp2f(fmaf(x1, CS, ml));
                x2 = __builtin_amdgcn_exp2f(fmaf(x2, CS, ml)); x3 = __builtin_amdgcn_exp2f(fmaf(x3, CS, ml));
                xm = __builtin_amdgcn_exp2f(fmaf(xm, CS, ml));
                const unsigned e01 = pk2(x0, x1), e23 = pk2(x2, x3), em = pk2(xm, xm);
                const v4u b1 = {c8 ? 0u : e01, c8 ? 0u : e23, c8 ? e01 : 0u, c8 ? e23 : 0u};
                unsigned b2 = em & (c8 ? 0xffff0000u : 0x0000ffffu);
                asm volatile("" : "+v"(b2));
                f32x4 o0 = {0, 0, 0, 0}, o1 = o0;
                o0 = mfma32(vx[0], b1, o0); o1 = mfma32(vx[1], b1, o1);
                SB();
                WAITV2(0, va, vc);
                if (__builtin_expect(any_sent(max(va, vc)), 0)) RETRY(1, ALD4(va, o_gvx, sb, 0); ALD4(vc, o_gvx, sb, 256); WAITV2(0, va, vc), any_sent(max(va, vc)));
                o0 = mfma16((v2u){va, 0u}, (v2u){b2, 0u}, o0); o1 = mfma16((v2u){vc, 0u}, (v2u){b2, 0u}, o1);
                SB();
                float l = ((x0 + x1) + (x2 + x3)) + xm;
                l += ror8(l); l = sw16sum(l, l); l = sw32sum(l, l);
                const float rinv = __builtin_amdgcn_rcpf(l);
                SB();
                float y[4];
#pragma unroll
                for (int e = 0; e < 4; ++e) y[e] = ((c8 ? o1[e] : o0[e]) + ror8(c8 ? o0[e] : o1[e])) * rinv;
                *(LAS v2u*)ow = (v2u){pk2(y[0], y[1]), pk2(y[2], y[3])};
            }
            BAR();
            WO_STAGE();
            const unsigned t3 = rt() + tm.tb;
            STAMP(7);
            __builtin_amdgcn_s_setprio(0);
            HAND_ADDR(s + 1);
            ZERO_A();
            unsigned o_gmx = o_gm ^ sx; asm volatile("" : "+v"(o_gmx));
            if (__builtin_expect(wave == 0, 1)) {
                unsigned o_pmx = o_pm ^ sx, o_gpx = o_gp ^ px; PIN2(o_pmx, o_gpx);
                wait_l(t3);
                STAMP(8);
                v4u r0, r1, r2, r3; f32x4 u; unsigned m;
#define C_LOAD() ALD16(r0, o_gpx, pp * PAGE, 0); ALD16(r1, o_gpx, pp * PAGE, 1024); ALD16(r2, o_gpx, pp * PAGE, 2048); ALD16(r3, o_gpx, pp * PAGE, 3072);  \
                WAITV1(3, r0); u = mfma32(sel, r0, (f32x4){0, 0, 0, 0}); m = mx4(r0, 0u); SB(); WAITV1(2, r1); u = mfma32(sel, r1, u); m = mx4(r1, m); SB(); \
                WAITV1(1, r2); u = mfma32(sel, r2, u); m = mx4(r2, m); SB(); WAITV1(0, r3); u = mfma32(sel, r3, u); m = mx4(r3, m)
                C_LOAD();
                if (__builtin_expect(any_sent(m), 0)) RETRY(2, C_LOAD(), any_sent(m));
                float z0 = tau ? u.z : u.x, z1 = tau ? u.w : u.y;
                z0 += ror8(z0); z1 += ror8(z1);
                { auto sw = __builtin_amdgcn_permlane16_swap(__float_as_uint(z0), __float_as_uint(z1), false, false); z0 = __uint_as_float(sw[0]); z1 = __uint_as_float(sw[1]); }
                const unsigned w2 = nosent(pk2(z0, z1));
                st4(w2, rs, o_pmx, sb); st4(w2, rs, o_pmx + 65536, sb);
                STAMP(9);
                const unsigned now = rt(), plan = now + tm.tc;
                DUMP();
                wait_l(plan);
            } else { DUMP(); }
            A_STAGE_A(s + 1);
        }
    } else {
        const int hw = wave - 4;
        const int sl_d = lane & 7, sl_m = (lane >> 3) & 1, sl_r = (lane >> 4) & 1;
        LAS unsigned char* const l_qk = lds + LC_PA + tau * 12288 + (sl_r * 96 + sl_m * 32 + 8 * hw + sl_d) * 16;
        const unsigned o_pqk = amap(qkvb + j * 1024 + (8 * hw + sl_d) * 32 + sl_m * 16 + sl_r * 8 + tau * 4);
        LAS unsigned char* const l_v = lds + LC_PA + tau * 12288 + (rh * 96 + 64 + 16 * (hw & 1) + n16) * 16;
        const unsigned o_pv = amap(qkvb + 4096 + j * 512 + (16 * (hw & 1) + n16) * 16 + rh * 8 + tau * 4);
        LAS unsigned char* const l_kv = lds + LC_KV + hw * 4096 + lane * 16;
        WAITV2(0, p0, p1); WAITV2(0, p2, p3);
        BAR();
        *(LAS v4u*)l_kv = p0; *(LAS v4u*)(l_kv + 1024) = p1; *(LAS v4u*)(l_kv + 2048) = p2; *(LAS v4u*)(l_kv + 3072) = p3;
        __builtin_amdgcn_s_setprio(2);
        for (int s = -1; s < NSEG - 1; ++s) {
            if (s >= 0) {
                ROLL(7);
                ZERO_D();
                BAR();
                __builtin_amdgcn_s_sleep(3);
                if (((s + 1) & 3) == 0) wait_ge(ctl + C_KV + 8 * (((s + 1) >> 2) * 32 + h), 2u, ctl, dead);
                { const unsigned so = fb((s + 1) * 32 + h, fpar); ALD16(p0, o_kv, so, 0); ALD16(p1, o_kv, so, 1024); ALD16(p2, o_kv, so, 2048); ALD16(p3, o_kv, so, 3072); }
                {
                    const unsigned rp = P_PART + ((s + 2) & 3), rx = ((sidemask >> rp) & 1u) << 13;
                    st8((v2u){SENT, SENT}, rs, o_pp ^ rx, rp * PAGE); st8((v2u){SENT, SENT}, rs, (o_pp - 8 * 16384) ^ rx, rp * PAGE);
                    if (hw >= 2 && s + 2 < NSEG) {
                        const unsigned pg = P_HAND + ((s + 2) >> 2);
                        st16((v4u){SENT, SENT, SENT, SENT}, rs, (hw == 2 ? o_i1 : o_i2) ^ (((sidemask >> pg) & 1u) << 13), pg * PAGE + ((s + 2) & 3) * 524288u);
                    }
                }
                STAMP(7); STAMP(8);
                BAR();
                STAMP(9);
                WO_STAGE();
                STAMP(10);
                DUMP();
                if (j == 0 && tid == 256) __hip_atomic_store(ctl + C_PROG + 32 * h, (unsigned)(s + 1), RLX_AGENT);
                WAITV2(0, p0, p1); WAITV2(0, p2, p3);
                LAS unsigned char* kw = l_kv + ((s + 1) & 1) * 16384;
                *(LAS v4u*)kw = p0; *(LAS v4u*)(kw + 1024) = p1; *(LAS v4u*)(kw + 2048) = p2; *(LAS v4u*)(kw + 3072) = p3;
            }
            HAND_ADDR(s + 1);
            ZERO_A();
            unsigned o_pqkx = o_pqk ^ sx, o_gmx = o_gm ^ sx; PIN2(o_pqkx, o_gmx);
            BAR();
            STAMPN(0);
            GATHER_MEM(, 0);
            STAMPN(1);
            QK_MFMA();
            STAMPN(2);
            BAR();
            STAMPN(3);
            REDUCE4(l_qk, o_pqkx);
            SB();
            STAMPN(4);
            if (s == NSEG - 2 && j == 0 && tid == 256) __hip_atomic_store(ctl + C_PROG + 32 * h, (unsigned)NSEG, RLX_AGENT);
            V_PART();
            STAMPN(5);
            if (hw < 2) {
                while (__builtin_amdgcn_readfirstlane(*(volatile LAS unsigned*)(lds + LC_MISC + 4)) < 8u * (unsigned)(s + 2)) { }
                asm volatile("" ::: "memory");
                REDUCE4(l_v, o_pv ^ sx);
            }
            STAMPN(6);
        }
    }
}

DEVI int claim_slot(GAS unsigned* ctl) {
    const unsigned x = xcc_id() & 7u;
    const unsigned r = __hip_atomic_fetch_add(ctl + C_XR + 32 * x, 1u, RLX_AGENT);
    if (r < 32 && __hip_atomic_exchange(ctl + C_SLOT + x * 32 + r, 1u, RLX_AGENT) == 0u) return (int)(x * 32 + r);
    for (int k = 255;; k = (k + 255) & 255) if (__hip_atomic_exchange(ctl + C_SLOT + k, 1u, RLX_AGENT) == 0u) return k;
}

DEVI void glds(unsigned voff, const void* sbase, unsigned ldsaddr) {
    unsigned keep;
    asm volatile("s_mov_b32 %0, m0\n\ts_mov_b32 m0, %3\n\ts_nop 2\n\tglobal_load_lds_dwordx4 %1, %2\n\ts_mov_b32 m0, %0" : "=&s"(keep) : "v"(voff), "s"(sbase), "s"(ldsaddr) : "memory");
}
template <int BM, int BN, int WM, bool TR, bool PERMK>
struct Gemm {
    static constexpr int WN = 8 / WM, MT = BM / WM / 16, NT = BN / WN / 16, NA = BM / 64, NB = BN / 64, STAGE = (BM + BN) * 128, NS = 147456 / STAGE, LPR = BN / 8;
    f32x4 acc[MT][NT];
    DEVI void run(LAS unsigned char* lds, const u16* A, int lda, const u16* B, int ldb, int nk) {
        const int tid = otid(), lane = tid & 63, wave = __builtin_amdgcn_readfirstlane(tid >> 6);
        const int wm = wave / WN, wn = wave % WN;
        unsigned va[NA], vb[NB];
#pragma unroll
        for (int q = 0; q < NA; ++q) va[q] = ((wave * NA * 8 + q * 8 + (lane >> 3)) * lda + (((lane & 7) ^ ((lane >> 3) & 7)) * 8)) * 2;
#pragma unroll
        for (int q = 0; q < NB; ++q) {
            const int k = (wave * NB + q) * (64 / LPR) + lane / LPR, sl = lane % LPR;
            const int pk = PERMK ? ((k & 32) + 16 * ((k >> 2) & 1) + 4 * ((k >> 3) & 3) + (k & 3)) : k;
            const int f = (k & 3) | (((k >> 3) & 1) << 2);
            vb[q] = (pk * ldb + (((sl >> 1) ^ f) * 16 + (sl & 1) * 8)) * 2;
        }
        const unsigned lbase = (unsigned)(uintptr_t)lds;
        auto piece = [&](int kt, int buf, int q) {
            if (q < NA) glds(va[q], A + kt * 64, lbase + buf * STAGE + (wave * NA + q) * 1024);
            else glds(vb[q - NA], B + (size_t)kt * 64 * ldb, lbase + buf * STAGE + BM * 128 + (wave * NB + q - NA) * 1024);
        };
        const int fr = lane & 15, fg = lane >> 4;
        const unsigned ra = (wm * (BM / WM) + fr) * 128;
        const unsigned ra0 = ra + ((fg ^ (fr & 7)) * 16), ra1 = ra + (((4 + fg) ^ (fr & 7)) * 16);
        const int q4 = fr >> 2, p4 = fr & 3, fsw = q4 | ((fg & 1) << 2);
        const unsigned rb = BM * 128 + (8 * fg + q4) * (BN * 2) + p4 * 8 + (((wn * NT) ^ fsw) * 32);
        v4u a0[MT], b0[NT], a1[MT], b1[NT];
        auto frag = [&](LAS unsigned char* sp, int ks, v4u* af, v4u* bf, int r) {
            if (r < NT) { LAS unsigned char* q = sp + (rb ^ (r * 32)) + ks * (BN * 64); const v2u x = trrd(q), y = trrd(q + BN * 8); bf[r] = (v4u){x.x, x.y, y.x, y.y}; }
            else af[r - NT] = *(LAS v4u*)(sp + (ks ? ra1 : ra0) + (r - NT) * 2048);
        };
        constexpr int NM = MT * NT, NF = MT + NT, NP = NA + NB;
        auto half = [&](const v4u* af, const v4u* bf, LAS unsigned char* np, int nks, v4u* naf, v4u* nbf, bool rd, bool stg, int skt, int sbuf) {
#pragma unroll
            for (int i = 0; i < NM; ++i) {
                const int m = i / NT, n = i % NT;
                acc[m][n] = TR ? mfma32(bf[n], af[m], acc[m][n]) : mfma32(af[m], bf[n], acc[m][n]);
#pragma unroll
                for (int r = i * NF / NM; r < (i + 1) * NF / NM; ++r) if (rd) frag(np, nks, naf, nbf, r);
#pragma unroll
                for (int q = i * NP / NM; q < (i + 1) * NP / NM; ++q) if (stg) piece(skt, sbuf, q);
                SB();
            }
        };
#pragma unroll
        for (int m = 0; m < MT; ++m)
#pragma unroll
            for (int n = 0; n < NT; ++n) acc[m][n] = (f32x4){0, 0, 0, 0};
#pragma unroll
        for (int q = 0; q < NS * NP; ++q) piece(q / NP, q / NP, q % NP);
        asm volatile("s_waitcnt vmcnt(%0)\n\ts_barrier" :: "n"((NS - 1) * NP) : "memory");
#pragma unroll
        for (int r = 0; r < NF; ++r) frag(lds, 0, a0, b0, r);
        int buf = 0;
        for (int kt = 0; kt < nk - NS; ++kt) {
            const int nb = buf == NS - 1 ? 0 : buf + 1;
            half(a0, b0, lds + buf * STAGE, 1, a1, b1, true, false, 0, 0);
            asm volatile("s_waitcnt vmcnt(%0) lgkmcnt(0)\n\ts_barrier" :: "n"((NS - 2) * NP) : "memory");
            half(a1, b1, lds + nb * STAGE, 0, a0, b0, true, true, kt + NS, buf);
            buf = nb;
        }
#pragma unroll
        for (int t = NS; t >= 1; --t) {
            const int nb = buf == NS - 1 ? 0 : buf + 1;
            half(a0, b0, lds + buf * STAGE, 1, a1, b1, true, false, 0, 0);
            asm volatile("s_waitcnt vmcnt(%0) lgkmcnt(0)\n\ts_barrier" :: "n"(t >= 2 ? (t - 2) * NP : 0) : "memory");
            half(a1, b1, lds + nb * STAGE, 0, a0, b0, t > 1, false, 0, 0);
            buf = nb;
        }
    }
};
DEVI v4u pk8(f32x4 a, f32x4 b) { return (v4u){pk2(a.x, a.y), pk2(a.z, a.w), pk2(b.x, b.y), pk2(b.z, b.w)}; }
DEVI void announce(GAS unsigned* w) {
    asm volatile("s_waitcnt vmcnt(0) lgkmcnt(0)\n\ts_barrier" ::: "memory");
    if (otid() == 0) __hip_atomic_fetch_add(w, 1u, RLX_AGENT);
}

template <int BM, int BN>
DEVI void kv_unit(const Params& p, LAS unsigned char* lds, int r0, int hh, int mat, unsigned fpar) {
    constexpr int WM = BM >= 128 ? 4 : 2, WN = 8 / WM, MT = BM / WM / 16, NT = BN / WN / 16;
    const int lane = otid() & 63, wave = __builtin_amdgcn_readfirstlane(otid() >> 6), wm = wave / WN, wn = wave % WN;
    const __amdgpu_buffer_rsrc_t rs = __builtin_amdgcn_make_buffer_rsrc(p.ws, 0, 0xFFFFFF00u, 0x00020000);
    const int row0 = r0 + wm * (BM / WM), hw = hh + wn * NT / 8, n0 = (wn * NT) & 7;
    if (mat == 0) {
        Gemm<BM, BN, WM, true, false> g; g.run(lds, p.x + (size_t)r0 * D_, D_, p.wk + hh * 128, 4096, 16);
#pragma unroll
        for (int m = 0; m < MT; ++m)
#pragma unroll
            for (int c = 0; c < NT / 2; ++c) {
                const int row = row0 + m * 16;
                st16(pk8(g.acc[m][2 * c], g.acc[m][2 * c + 1]), rs, lane * 16, O_KX + fb((row >> 5) * 32 + hw, fpar) + (((row >> 4) & 1) * 4 + n0 / 2 + c) * 1024);
            }
    } else {
        Gemm<BM, BN, WM, false, false> g; g.run(lds, p.x + (size_t)r0 * D_, D_, p.wv + hh * 128, 4096, 16);
#pragma unroll
        for (int m = 0; m < MT; m += 2)
#pragma unroll
            for (int n = 0; n < NT; ++n) {
                const int row = row0 + m * 16;
                st16(pk8(g.acc[m][n], g.acc[m + 1][n]), rs, lane * 16, O_VX + fb((row >> 5) * 32 + hw, fpar) + (n0 + n) * 1024);
            }
    }
    asm volatile("s_waitcnt vmcnt(0) lgkmcnt(0)\n\ts_barrier" ::: "memory");
    const int t = otid();
    if (t < (BM == 256 ? 2 : 1) * (BN / 128))
        __hip_atomic_fetch_add((GAS unsigned*)(p.ws + O_CTL) + C_KV + 8 * (((r0 >> 7) + t / (BN / 128)) * 32 + hh + t % (BN / 128)), 1u, RLX_AGENT);
}

#define FS(k)
#define QS(k)
DEVI void qa_unit(const Params& p, LAS unsigned char* lds, int panel, int hh, unsigned sidemask, bool& dead) {
    const int lane = otid() & 63, wave = __builtin_amdgcn_readfirstlane(otid() >> 6);
    const int g4 = lane >> 4, n16 = lane & 15;
    const __amdgpu_buffer_rsrc_t rs = __builtin_amdgcn_make_buffer_rsrc(p.ws, 0, 0xFFFFFF00u, 0x00020000);
    GAS unsigned* ctl = (GAS unsigned*)(p.ws + O_CTL);
    v4u qf[2][4];
    {
        Gemm<256, 128, 8, true, false> g; g.run(lds, p.x + (size_t)panel * 256 * D_, D_, p.wq + hh * 128, 4096, 16);
#pragma unroll
        for (int m = 0; m < 2; ++m)
#pragma unroll
            for (int c = 0; c < 4; ++c) qf[m][c] = pk8(g.acc[m][2 * c], g.acc[m][2 * c + 1]);
    }
    const int seg = panel * 8 + wave;
    const unsigned fpar = far_par(sidemask), kvo = fb(seg * 32 + hh, fpar);
    wait_ge(ctl + C_KV + 8 * ((seg >> 2) * 32 + hh), seg >> 2 ? 2u : 4u, ctl, dead);
    v4u kx[8], vx[8];
#pragma unroll
    for (int q = 0; q < 8; ++q) kx[q] = ld16(rs, lane * 16 + q * 1024, O_KX + kvo);
#pragma unroll
    for (int q = 0; q < 8; ++q) vx[q] = ld16(rs, lane * 16 + q * 1024, O_VX + kvo);
    f32x4 z = {0, 0, 0, 0};
    f32x4 s00 = z, s10 = z, s11 = z, sm0 = z, sm1 = z;
#pragma unroll
    for (int c = 0; c < 4; ++c) { s00 = mfma32(kx[c], qf[0][c], s00); s10 = mfma32(kx[c], qf[1][c], s10); s11 = mfma32(kx[4 + c], qf[1][c], s11); }
    LAS unsigned char* img = lds + wave * 6144;
    wait_ge(ctl + C_PROG + 32 * hh, (unsigned)(seg + 1), ctl, dead);
    QS(0);
    {
        const unsigned pg = P_HAND + (seg >> 2), sb = pg * PAGE + (seg & 3) * 524288u, sx = ((sidemask >> pg) & 1u) << 13;
        v4u t[6];
        for (unsigned spins = 0;;) {
            unsigned m = 0;
#pragma unroll
            for (int q = 0; q < 6; ++q) t[q] = ld16(rs, amap(L_QKV + hh * 6144 + q * 1024 + lane * 16) ^ sx, sb);
#pragma unroll
            for (int q = 0; q < 6; ++q) m = mx4(t[q], m);
            if (!any_sent(m) || dead) break;
            if (poll_fail(spins, ctl, 128)) dead = true;
            asm volatile("" ::: "memory");
        }
#pragma unroll
        for (int q = 0; q < 6; ++q) *(LAS v4u*)(img + q * 1024 + lane * 16) = t[q];
        asm volatile("s_waitcnt lgkmcnt(0)" ::: "memory");
    }
    QS(1);
    LAS unsigned char* kb = img + (4 * g4 + (n16 >> 2)) * 32 + 16 + (lane & 1) * 8;
#pragma unroll
    for (int c = 0; c < 4; ++c) {
        const v2u ka = trrd(kb + c * 1024), kc = trrd(kb + c * 1024 + 512);
        const v4u kf = {ka.x, ka.y, kc.x, kc.y};
        sm0 = mfma32(kf, qf[0][c], sm0); sm1 = mfma32(kf, qf[1][c], sm1);
    }
    v4u b1[2]; v2u b2[2];
#pragma unroll
    for (int m = 0; m < 2; ++m) {
        float v[12];
#pragma unroll
        for (int e = 0; e < 4; ++e) {
            const bool diag = 4 * g4 + e <= n16;
            v[e] = rnd(rnd(m ? s10[e] : s00[e]) * SCALE); if (m == 0 && !diag) v[e] = -INFINITY;
            v[4 + e] = (m && diag) ? rnd(rnd(s11[e]) * SCALE) : -INFINITY;
            v[8 + e] = g4 < 2 ? rnd(rnd(m ? sm1[e] : sm0[e]) * SCALE) : -INFINITY;
        }
        float mx = v[0];
#pragma unroll
        for (int e = 1; e < 12; ++e) mx = fmaxf(mx, v[e]);
        mx = sw16max(mx); mx = sw32max(mx);
        const float ml = -mx * LOG2E; float l = 0.f;
#pragma unroll
        for (int e = 0; e < 12; ++e) { v[e] = __builtin_amdgcn_exp2f(fmaf(v[e], LOG2E, ml)); l += v[e]; }
        l = sw16sum(l, l); l = sw32sum(l, l);
        const float ri = 1.f / l;
        b1[m] = (v4u){pk2(v[0] * ri, v[1] * ri), pk2(v[2] * ri, v[3] * ri), pk2(v[4] * ri, v[5] * ri), pk2(v[6] * ri, v[7] * ri)};
        b2[m] = (v2u){pk2(v[8] * ri, v[9] * ri), pk2(v[10] * ri, v[11] * ri)};
    }
#pragma unroll
    for (int c = 0; c < 4; ++c) {
        f32x4 o[2][2];
#pragma unroll
        for (int t = 0; t < 2; ++t) {
            const v2u va = *(LAS v2u*)(img + 4096 + ((2 * c + t) * 16 + n16) * 16 + (g4 & 1) * 8);
#pragma unroll
            for (int m = 0; m < 2; ++m) { o[m][t] = mfma32(vx[2 * c + t], b1[m], z); o[m][t] = mfma16(va, b2[m], o[m][t]); }
        }
#pragma unroll
        for (int m = 0; m < 2; ++m) st16(pk8(o[m][0], o[m][1]), rs, n16 * 16384 + 16 * g4, O_OX + fb(seg * 32 + m * 16, fpar) + (hh * 128 + c * 32) * 2);
    }
    QS(2);
    asm volatile("s_waitcnt vmcnt(0)" ::: "memory");
    QS(3);
    if (lane == 0) __hip_atomic_fetch_add(ctl + C_OF + 16 * (seg * 8 + (hh >> 2)), 1u, RLX_AGENT);
}

DEVI void top2(const float* R, int& i0, int& i1) {
    const int lane = otid() & 63;
    float b0 = -INFINITY, b1 = -INFINITY; int j0 = 0x7fffffff, j1 = 0x7fffffff;
    f32x4 rv[8];
#pragma unroll
    for (int q = 0; q < 8; ++q) rv[q] = *(const f32x4*)(R + lane * 32 + q * 4);
#pragma unroll
    for (int q = 0; q < 32; ++q) {
        const float v = rv[q >> 2][q & 3]; const int k = lane * 32 + q;
        const bool f0 = v > b0 || j0 == 0x7fffffff, f1 = v > b1 || j1 == 0x7fffffff;
        b1 = f0 ? b0 : (f1 ? v : b1); j1 = f0 ? j0 : (f1 ? k : j1); b0 = f0 ? v : b0; j0 = f0 ? k : j0;
    }
    for (int m = 1; m < 64; m <<= 1) {
        const float c0 = __shfl_xor(b0, m), c1 = __shfl_xor(b1, m); const int k0 = __shfl_xor(j0, m), k1 = __shfl_xor(j1, m);
        auto gt = [](float a, int ja, float b, int jb) { return a > b || (!(b > a) && ja < jb); };
        if (gt(c0, k0, b0, j0)) { if (gt(b0, j0, c1, k1)) { b1 = b0; j1 = j0; } else { b1 = c1; j1 = k1; } b0 = c0; j0 = k0; }
        else if (gt(c0, k0, b1, j1)) { b1 = c0; j1 = k0; }
    }
    i0 = __builtin_amdgcn_readfirstlane(j0); i1 = __builtin_amdgcn_readfirstlane(j1);
}
DEVI void ffn1_unit(const Params& p, LAS unsigned char* lds, int u, int i0, int i1) {
    const int tid = otid(), co = tid & 7, ksl = tid >> 3, col = u * 64 + co * 8;
    LAS float* red = (LAS float*)lds;
    v4u wg[16], wu[16];
#pragma unroll
    for (int q = 0; q < 16; ++q) { wg[q] = *(const v4u*)(p.wg + (size_t)(ksl * 16 + q) * F_ + col); wu[q] = *(const v4u*)(p.wu + (size_t)(ksl * 16 + q) * F_ + col); }
    v4u xa[2], xb[2];
#pragma unroll
    for (int q = 0; q < 2; ++q) { xa[q] = *(const v4u*)(p.x + i0 * D_ + ksl * 16 + q * 8); xb[q] = *(const v4u*)(p.x + i1 * D_ + ksl * 16 + q * 8); }
    float a[4][8] = {};
#pragma unroll
    for (int q = 0; q < 16; ++q) {
        const unsigned ua = xa[q >> 3][(q >> 1) & 3], ub = xb[q >> 3][(q >> 1) & 3];
        const float x0 = (q & 1) ? hi16(ua) : lo16(ua), x1 = (q & 1) ? hi16(ub) : lo16(ub);
#pragma unroll
        for (int e = 0; e < 4; ++e) {
            a[0][2 * e] += x0 * lo16(wg[q][e]); a[0][2 * e + 1] += x0 * hi16(wg[q][e]); a[1][2 * e] += x1 * lo16(wg[q][e]); a[1][2 * e + 1] += x1 * hi16(wg[q][e]);
            a[2][2 * e] += x0 * lo16(wu[q][e]); a[2][2 * e + 1] += x0 * hi16(wu[q][e]); a[3][2 * e] += x1 * lo16(wu[q][e]); a[3][2 * e + 1] += x1 * hi16(wu[q][e]);
        }
    }
#pragma unroll
    for (int q = 0; q < 4; ++q)
#pragma unroll
        for (int e = 0; e < 8; ++e) red[(ksl * 4 + q) * 64 + co * 8 + e] = a[q][e];
    BAR();
    if (tid < 128) {
        const int t = tid >> 6, c = tid & 63; float g = 0.f, uu = 0.f;
        for (int k = 0; k < 64; ++k) { g += red[(k * 4 + t) * 64 + c]; uu += red[(k * 4 + 2 + t) * 64 + c]; }
        g = rnd(g); uu = rnd(uu); g = rnd(g / (1.f + __expf(-g)));
        __hip_atomic_store((GAS float*)(p.ws + O_FH) + t * F_ + u * 64 + c, rnd(g * uu), RLX_AGENT);
    }
    announce((GAS unsigned*)(p.ws + O_CTL) + C_F1);
}
DEVI void ffn2_unit(const Params& p, LAS unsigned char* lds, int u, bool& dead) {
    const int tid = otid(), co = tid & 3, ksl = tid >> 2, col = u * 32 + co * 8;
    GAS unsigned* ctl = (GAS unsigned*)(p.ws + O_CTL);
    LAS float* red = (LAS float*)lds;
    v4u w[28];
#pragma unroll
    for (int q = 0; q < 28; ++q) w[q] = *(const v4u*)(p.wd + (size_t)(ksl * 28 + q) * D_ + col);
    wait_ge(ctl + C_F1, 56u, ctl, dead);
    const __amdgpu_buffer_rsrc_t rs = __builtin_amdgcn_make_buffer_rsrc(p.ws, 0, 0xFFFFFF00u, 0x00020000);
    v4u h0[7], h1[7];
#pragma unroll
    for (int q = 0; q < 7; ++q) { h0[q] = ld16(rs, ksl * 112 + q * 16, O_FH); h1[q] = ld16(rs, ksl * 112 + q * 16, O_FH + F_ * 4); }
    float a[2][8] = {};
#pragma unroll
    for (int q = 0; q < 28; ++q) {
        const float x0 = __uint_as_float(h0[q >> 2][q & 3]), x1 = __uint_as_float(h1[q >> 2][q & 3]);
#pragma unroll
        for (int e = 0; e < 4; ++e) { a[0][2 * e] += x0 * lo16(w[q][e]); a[0][2 * e + 1] += x0 * hi16(w[q][e]); a[1][2 * e] += x1 * lo16(w[q][e]); a[1][2 * e + 1] += x1 * hi16(w[q][e]); }
    }
#pragma unroll
    for (int q = 0; q < 2; ++q)
#pragma unroll
        for (int e = 0; e < 8; ++e) red[(ksl * 2 + q) * 32 + co * 8 + e] = a[q][e];
    BAR();
    if (tid < 64) {
        const int t = tid >> 5, c = tid & 31; float y = 0.f;
        for (int k = 0; k < 128; ++k) y += red[(k * 2 + t) * 32 + c];
        __hip_atomic_store((GAS float*)(p.ws + O_FY) + t * D_ + u * 32 + c, rnd(y), RLX_AGENT);
    }
    announce(ctl + C_F2);
}

DEVI void wo_fast_unit(const Params& p, LAS unsigned char* lds, int ct, int k8, int i0, int i1, unsigned fpar, bool& dead) {
    const int tid = otid(), lane = tid & 63, wave = __builtin_amdgcn_readfirstlane(tid >> 6), fr = lane & 15, fg = lane >> 4;
    GAS unsigned* ctl = (GAS unsigned*)(p.ws + O_CTL);
    const __amdgpu_buffer_rsrc_t rs = __builtin_amdgcn_make_buffer_rsrc(p.ws, 0, 0xFFFFFF00u, 0x00020000);
    {
        const unsigned lbase = (unsigned)(uintptr_t)lds + wave * 2048;
        const u16* gb = p.wo + (size_t)k8 * 512 * D_ + ct * 128;
#pragma unroll
        for (int q = 0; q < 2; ++q) {
            const int k = wave * 8 + (lane >> 4) + 4 * q;
            const int pk = (k & 32) + 16 * ((k >> 2) & 1) + 4 * ((k >> 3) & 3) + (k & 3), f = (k & 3) | (((k >> 3) & 1) << 2);
            const unsigned vb = (pk * D_ + ((((lane & 15) >> 1) ^ f) * 16 + (lane & 1) * 8)) * 2;
#pragma unroll
            for (int kt = 0; kt < 8; ++kt) glds(vb, gb + (size_t)kt * 64 * D_, lbase + kt * 16384 + q * 1024);
        }
    }
    wait_ge(ctl + C_INIT, 128u, ctl, dead);
    wait_ge(ctl + C_F2, 32u, ctl, dead);
    asm volatile("s_waitcnt vmcnt(0)\n\ts_barrier" ::: "memory");
    const int mt = wave & 1, np = wave >> 1;
    v4u bf[16][2];
    {
        const int q4 = fr >> 2, p4 = fr & 3, fsw = q4 | ((fg & 1) << 2);
        const unsigned bpo = (8 * fg + q4) * 256 + p4 * 8 + ((fsw ^ (2 * np)) * 32);
#pragma unroll
        for (int q = 0; q < 16; ++q)
#pragma unroll
            for (int n = 0; n < 2; ++n) { LAS unsigned char* bp = lds + (bpo ^ (n * 32)) + q * 8192; const v2u x = trrd(bp), y = trrd(bp + 1024); bf[q][n] = (v4u){x.x, x.y, y.x, y.y}; }
    }
    BAR();
    const int col = ct * 128 + k8 * 16 + fg * 4;
    v4u t[8]; float gv; v2u xv;
    auto request = [&](int sg) {
        const int row = sg * 32 + mt * 16 + fr;
#pragma unroll
        for (int q = 0; q < 8; ++q) t[q] = ld16(rs, lane * 16 + q * 1024, O_S2 + fb((((sg & 7) * 8 + ct) * 2 + mt) * 8 + k8, fpar));
        gv = ((const float*)(p.ws + O_G))[row];
        xv = *(const v2u*)(p.x + row * D_ + col);
    };
    auto finish = [&](int sg) {
        const int row = sg * 32 + mt * 16 + fr;
        for (unsigned spins = 0;;) {
            v4u an = t[0], orr = t[0];
#pragma unroll
            for (int q = 1; q < 8; ++q) { an &= t[q]; orr |= t[q]; }
            const unsigned bad = (sg & 8) ? ~(an.x & an.y & an.z & an.w) : (orr.x | orr.y | orr.z | orr.w);
            if (__builtin_amdgcn_ballot_w64((bad & 1u) != 0u) == 0 || dead) break;
            if (poll_fail(spins, ctl, 64)) dead = true;
            asm volatile("" ::: "memory");
            request(sg);
        }
        float bb[4] = {lo16(xv.x), hi16(xv.x), lo16(xv.y), hi16(xv.y)};
        if (row == i0 || row == i1) {
            const float rs_ = rnd(1.f / (1.f + __expf(-((const float*)(p.ws + O_R))[row])));
#pragma unroll
            for (int e = 0; e < 4; ++e) bb[e] = rnd(bb[e] + rnd(__hip_atomic_load((GAS float*)(p.ws + O_FY) + (row == i0 ? 0 : D_) + col + e, RLX_AGENT) * rs_));
        }
        f32x4 a = __builtin_bit_cast(f32x4, t[0]);
#pragma unroll
        for (int q = 1; q < 8; ++q) a += __builtin_bit_cast(f32x4, t[q]);
        const float og = rnd(1.f - gv);
        float o[4];
#pragma unroll
        for (int e = 0; e < 4; ++e) o[e] = rnd(gv * rnd(a[e])) + rnd(og * bb[e]);
        *(v2u*)(p.out + row * D_ + col) = (v2u){pk2(o[0], o[1]), pk2(o[2], o[3])};
    };
    int ready = SF0;
    const v4u rd = {(unsigned)(uintptr_t)p.ws, (unsigned)((uintptr_t)p.ws >> 32) & 0xffffu, 0xFFFFFF00u, 0x00020000u};
    v4u ar0, ar1, ar2, ar3;
    const unsigned va0 = lane * 16, va1 = va0 + 16384, va2 = va0 + 32768, va3 = va0 + 49152;
    auto aload = [&](int sg) {
        const unsigned so = O_OX + fb(sg * 32 + wave * 4, fpar) + k8 * 1024u;
        ALD16(ar0, va0, so, 0); ALD16(ar1, va1, so, 0); ALD16(ar2, va2, so, 0); ALD16(ar3, va3, so, 0);
    };
    LAS unsigned char* const aw = lds + wave * 4096;
    const unsigned sw0 = (lane ^ ((wave * 4) & 15)) * 16;
    for (int seg = SF0; seg < NSEG; ++seg) {
        const int rnd_ = seg - 56; (void)rnd_;
        if (seg >= ready) {
            if (wave == ((2 * seg + 4) & 7)) {
                unsigned long long ok;
                for (unsigned spins = 0;;) {
                    const unsigned c = seg + lane < NSEG ? __hip_atomic_load(ctl + C_OF + 16 * ((seg + lane - SF0) * 8 + k8), RLX_AGENT) : 0u;
                    ok = __builtin_amdgcn_ballot_w64(c >= 4u);
                    if ((ok & 1ull) || dead) break;
                    if (poll_fail(spins, ctl, 64)) dead = true;
                }
                if (lane == 0) *(LAS int*)(lds + 65536) = seg + max(1, (int)__builtin_ctzll(~ok));
            }
            BAR();
            ready = __builtin_amdgcn_readfirstlane(*(LAS int*)(lds + 65536));
            aload(seg);
            asm volatile("s_waitcnt vmcnt(0)" ::: "memory");
        }
        WAITV2(2, ar0, ar1); WAITV2(2, ar2, ar3);
        FS(0);
        LAS unsigned char* img = lds + (seg & 1) * 32768;
        { LAS unsigned char* w_ = aw + (seg & 1) * 32768;
          *(LAS v4u*)(w_ + sw0) = ar0; *(LAS v4u*)(w_ + 1024 + (sw0 ^ 16)) = ar1; *(LAS v4u*)(w_ + 2048 + (sw0 ^ 32)) = ar2; *(LAS v4u*)(w_ + 3072 + (sw0 ^ 48)) = ar3; }
        asm volatile("s_waitcnt lgkmcnt(0)" ::: "memory");
        SB();
        if (seg >= 3 && np == ((seg - 3) & 3)) finish(seg - 3);
        if (seg >= 2 && np == ((seg - 2) & 3)) request(seg - 2);
        if (seg + 1 < ready) aload(seg + 1);
        BAR();
        f32x4 c0 = {0, 0, 0, 0}, c1 = c0;
        {
            v4u af[2][4];
            LAS unsigned char* ap = img + (mt * 16 + fr) * 1024;
#pragma unroll
            for (int q = 0; q < 4; ++q) af[0][q] = *(LAS v4u*)(ap + (((q * 4 + fg) ^ fr) * 16));
#pragma unroll
            for (int g = 0; g < 4; ++g) {
                if (g < 3) {
#pragma unroll
                    for (int q = 0; q < 4; ++q) af[(g + 1) & 1][q] = *(LAS v4u*)(ap + (g + 1) * 256 + (((q * 4 + fg) ^ fr) * 16));
                }
                SB();
#pragma unroll
                for (int q = 0; q < 4; ++q) { c0 = mfma32(bf[g * 4 + q][0], af[g & 1][q], c0); c1 = mfma32(bf[g * 4 + q][1], af[g & 1][q], c1); }
                SB();
            }
        }
        FS(1);
        const unsigned sb2 = O_S2 + fb((((seg & 7) * 8 + ct) * 2 + mt) * 8 + 2 * np, fpar) + k8 * 1024u;
        const unsigned tg = (seg >> 3) & 1u;
        st16((__builtin_bit_cast(v4u, c0) & ~1u) | tg, rs, lane * 16, sb2);
        st16((__builtin_bit_cast(v4u, c1) & ~1u) | tg, rs, lane * 16, sb2 + 16384u);
    }
    if (np == 1) finish(NSEG - 3);
    if (np >= 2) { const int rnd_ = 4 + np; (void)rnd_; if (np == 3) __builtin_amdgcn_s_sleep(5); request(NSEG - 4 + np); finish(NSEG - 4 + np); FS(2); }
}

constexpr int Q_K2 = 16, Q_F1 = Q_K2 + 16, Q_QA = Q_F1 + 14, Q_F2 = Q_QA + 16, Q_WF = Q_F2 + 8, Q_Q2 = Q_WF + 16, Q_END = Q_Q2 + 48;
DEVI void worker_body(const Params& p, LAS unsigned char* lds, int w, int wdel) {
    const int tid = otid(), lane = tid & 63;
    GAS unsigned* ctl = (GAS unsigned*)(p.ws + O_CTL);
    const unsigned sv = lane < 21 ? (ctl[C_SIDE + lane] & 1u) : 0u;
    const unsigned sidemask = (unsigned)__builtin_amdgcn_ballot_w64(sv != 0);
    bool dead = false;
    const unsigned fpar = far_par(sidemask);
    const int xq = w >> 5, hm = xq * 16 + ((w & 31) >> 1);
    wait_l(rt() + wdel);
    kv_unit<64, 128>(p, lds, (w & 1) * 64, hm >> 1, hm & 1, fpar);
    EV(9);
    kv_unit<128, 128>(p, lds, 128 + (w & 1) * 128, hm >> 1, hm & 1, fpar);
    EV(10);
    { const int hp = (w >> 5) * 8 + ((w & 31) >> 2); kv_unit<256, 256>(p, lds, 384 + (w & 3) * 256, (hp >> 1) * 2, hp & 1, fpar); }
    EV(11);
    {
        const __amdgpu_buffer_rsrc_t rs = __builtin_amdgcn_make_buffer_rsrc(p.ws, 0, 0xFFFFFF00u, 0x00020000);
        const v4u zz = {SENT, SENT, SENT, SENT};
#pragma unroll
        for (int q = 0; q < 8; ++q) st16(zz, rs, otid() * 16, O_S2 + fb(w * 8 + q, fpar));
        announce(ctl + C_INIT);
    }
    int i0, i1; top2((const float*)(p.ws + O_R), i0, i1);
    for (;;) {
        BAR();
        if (tid == 0) *(LAS unsigned*)(lds + 147456) = __hip_atomic_fetch_add(ctl + C_QUEUE + 16 * xq, 1u, RLX_AGENT);
        BAR();
        int t = __builtin_amdgcn_readfirstlane(*(LAS unsigned*)(lds + 147456));
        if (t >= Q_END) break;
        EV(1000 + t);
        if (t < Q_K2) kv_unit<256, 256>(p, lds, 1408 + (t & 1) * 256, xq * 8 + (t >> 2) * 2, (t >> 1) & 1, fpar);
        else if (t < Q_F1) kv_unit<128, 128>(p, lds, 1920, xq * 8 + ((t - Q_K2) >> 1), t & 1, fpar);
        else if (t < Q_QA) ffn1_unit(p, lds, xq * 14 + t - Q_F1, i0, i1);
        else if (t >= Q_F2 && t < Q_WF) ffn2_unit(p, lds, xq * 8 + t - Q_F2, dead);
        else if (t >= Q_WF && t < Q_Q2) wo_fast_unit(p, lds, (t - Q_WF) >> 1, 2 * xq + ((t - Q_WF) & 1), i0, i1, fpar, dead);
        else { const int qa = t < Q_F2 ? t - Q_QA : t - Q_Q2 + 16; qa_unit(p, lds, qa >> 3, xq * 8 + (qa & 7), sidemask, dead); }
    }
}

__global__ void __launch_bounds__(512, 2) k_main(Params p, Tim tm) {
    extern __shared__ __attribute__((aligned(16))) unsigned char smem[];
    LAS unsigned char* lds = (LAS unsigned char*)smem;
    if (threadIdx.x == 0) { *(LAS unsigned*)(lds + 147460) = rt(); *(LAS int*)(lds + 147456) = claim_slot((GAS unsigned*)(p.ws + O_CTL)); }
    __syncthreads();
    const int slot = __builtin_amdgcn_readfirstlane(*(LAS int*)(lds + 147456));
    EV(1);
    if (slot < 128) chain_body(p, tm, lds, slot >> 5, slot & 31, __builtin_amdgcn_readfirstlane(*(LAS unsigned*)(lds + 147460)));
    else worker_body(p, lds, slot - 128, tm.qs);
    EV(99);
}

#define TIM_INIT {36, 40, 36, 0, 0, 0, 0, 140, 0, 0}
extern "C" void kernel_launch(void* const* d_in, const int* in_sizes, int n_in, void* d_out, int out_size, void* d_ws, size_t ws_size, hipStream_t stream) {
    Params p;
    p.x = (const u16*)d_in[0]; p.mem0 = (const u16*)d_in[1]; p.wq = (const u16*)d_in[2]; p.wk = (const u16*)d_in[3]; p.wv = (const u16*)d_in[4]; p.wo = (const u16*)d_in[5];
    p.wr = (const u16*)d_in[8]; p.wg = (const u16*)d_in[9]; p.wu = (const u16*)d_in[10]; p.wd = (const u16*)d_in[11]; p.gw = (const u16*)d_in[12];
    p.out = (u16*)d_out;
    p.ws = (unsigned char*)(((uintptr_t)d_ws + PAGE - 1) & ~(uintptr_t)(PAGE - 1));
    Tim tm = TIM_INIT;
    constexpr int LDS_M = 147456 + 64;
    (void)hipFuncSetAttribute((const void*)k_main, hipFuncAttributeMaxDynamicSharedMemorySize, LDS_M);
    hipLaunchKernelGGL(k_prep, dim3(256), dim3(256), 0, stream, p);
    hipLaunchKernelGGL(k_main, dim3(256), dim3(512), LDS_M, stream, p, tm);
}
```

```cpp
#include <hip/hip_runtime.h>
#include <hip/hip_bf16.h>
#include <stdint.h>

typedef unsigned short u16;
typedef unsigned v4u __attribute__((ext_vector_type(4)));
typedef unsigned v2u __attribute__((ext_vector_type(2)));
typedef unsigned v8u __attribute__((ext_vector_type(8)));
typedef float f32x4 __attribute__((ext_vector_type(4)));
typedef __bf16 bf16x2 __attribute__((ext_vector_type(2)));
typedef __bf16 bf16x8 __attribute__((ext_vector_type(8)));
typedef __bf16 bf16x16 __attribute__((ext_vector_type(16)));
typedef short s16x4 __attribute__((ext_vector_type(4)));
#define GAS __attribute__((address_space(1)))
#define LAS __attribute__((address_space(3)))
#define RLX_AGENT __ATOMIC_RELAXED, __HIP_MEMORY_SCOPE_AGENT
#define DEVI __device__ __forceinline__
#define BAR() asm volatile("s_waitcnt lgkmcnt(0)\n\ts_barrier" ::: "memory")

constexpr int S_ = 2048, D_ = 1024, H_ = 32, DH = 128, NSEG = 64, F_ = 3584;
constexpr float SCALE = 0.08838834764831845f, LOG2E = 1.4426950408889634f;
constexpr unsigned SENT = 0xFFFFFFFFu;
constexpr unsigned PAGE = 2u << 20;
constexpr unsigned O_CTL = 0, O_R = 65536, O_G = O_R + 8192, O_FH = 131072, O_FY = O_FH + 32768;
constexpr unsigned P_HAND = 1, P_PART = 17;
constexpr unsigned O_KX = 22 * PAGE, O_VX = 38 * PAGE, O_OX = 54 * PAGE, O_S2 = 70 * PAGE  , O_DBG = 78 * PAGE;
constexpr unsigned L_QKV = 65536;
constexpr int C_ABORT = 0, C_ARRIVE = 64, C_T0 = 128, C_QUEUE = 192, C_XR = 256  , C_F1 = 768, C_F2 = 800, C_INIT = 832, C_SLOT = 1024  ,
              C_KV = 2048  , C_PROG = 6144  , C_OF = 7168  , C_ZEND = 15360,
              C_SIDE = 15360  ;
constexpr int SF0 = 0;

struct Params { const u16 *x, *mem0, *wq, *wk, *wv, *wo, *wr, *wg, *wu, *wd, *gw; u16* out; unsigned char* ws; };
struct Tim { int dq, d3, d1, t0; };

DEVI float bf2f(u16 b) { return __uint_as_float((unsigned)b << 16); }
DEVI unsigned pk2(float lo, float hi) { bf16x2 v = {(__bf16)lo, (__bf16)hi}; return __builtin_bit_cast(unsigned, v); }
DEVI float rnd(float f) { return __uint_as_float(pk2(f, 0.f) << 16); }
DEVI float lo16(unsigned u) { return __uint_as_float(u << 16); }
DEVI float hi16(unsigned u) { return __uint_as_float(u & 0xffff0000u); }
DEVI int otid() { int t = threadIdx.x; asm volatile("" : "+v"(t)); return t; }
DEVI unsigned xcc_id() { return (unsigned)__builtin_amdgcn_s_getreg(20 | (3 << 11)) & 0xfu; }
DEVI unsigned rt() { return (unsigned)__builtin_amdgcn_s_memrealtime(); }
DEVI unsigned amap(unsigned lo) { return ((lo >> 13) << 14) + (lo & 8191); }
DEVI unsigned fb(unsigned blk, unsigned par) { return blk * 16384u + (((par ^ (blk >> 7)) & 1u) << 13); }
DEVI unsigned far_par(unsigned sidemask) { return (sidemask & 1u) ^ 1u; }
DEVI unsigned side_mask(GAS unsigned* ctl, int lane) {
    const v4u v = *(GAS v4u*)(ctl + C_SIDE + lane * 4);
    int n = 0;
#pragma unroll
    for (int e = 0; e < 4; ++e) n += __builtin_popcountll(__builtin_amdgcn_ballot_w64((v[e] & 1u) != 0u));
    return n > 128 ? 0x55555555u : 0xAAAAAAAAu;
}
DEVI v4u ld16(__amdgpu_buffer_rsrc_t r, unsigned vo, unsigned so) { return __builtin_amdgcn_raw_buffer_load_b128(r, vo, so, 16); }
DEVI void st16(v4u v, __amdgpu_buffer_rsrc_t r, unsigned vo, unsigned so) {
    __builtin_amdgcn_raw_buffer_store_b128(v, r, vo, so, 16);
    asm volatile("s_nop 1" :: "v"(v));
}
DEVI void st8(v2u v, __amdgpu_buffer_rsrc_t r, unsigned vo, unsigned so) { __builtin_amdgcn_raw_buffer_store_b64(v, r, vo, so, 16); }
DEVI void st4(unsigned v, __amdgpu_buffer_rsrc_t r, unsigned vo, unsigned so) { __builtin_amdgcn_raw_buffer_store_b32(v, r, vo, so, 16); }
DEVI unsigned umax3(unsigned a, unsigned b, unsigned c) { unsigned r; asm("v_max3_u32 %0, %1, %2, %3" : "=v"(r) : "v"(a), "v"(b), "v"(c)); return r; }
DEVI unsigned mx4(v4u v, unsigned m) { return umax3(umax3(v.x, v.y, m), v.z, v.w); }
DEVI bool any_sent(unsigned m) { return __builtin_amdgcn_ballot_w64(m == SENT) != 0; }
DEVI unsigned nosent(unsigned u) { return u < 0xFFFFFFFEu ? u : 0xFFFFFFFEu; }
DEVI v4u nosent4(v4u v) { return (v4u){nosent(v.x), nosent(v.y), nosent(v.z), nosent(v.w)}; }
DEVI f32x4 mfma32(v4u a, v4u b, f32x4 c) { return __builtin_amdgcn_mfma_f32_16x16x32_bf16(__builtin_bit_cast(bf16x8, a), __builtin_bit_cast(bf16x8, b), c, 0, 0, 0); }
DEVI f32x4 mfma16(v2u a, v2u b, f32x4 c) { return __builtin_amdgcn_mfma_f32_16x16x16bf16_1k(__builtin_bit_cast(s16x4, a), __builtin_bit_cast(s16x4, b), c, 0, 0, 0); }
DEVI f32x4 smf(v4u a, v8u b, f32x4 c, int idx) { return __builtin_amdgcn_smfmac_f32_16x16x64_bf16(__builtin_bit_cast(bf16x8, a), __builtin_bit_cast(bf16x16, b), c, idx, 0, 0); }
DEVI v2u trrd(LAS unsigned char* p) { return __builtin_bit_cast(v2u, __builtin_amdgcn_ds_read_tr16_b64_v4i16((LAS s16x4*)p)); }
DEVI float ror8(float v) { return __int_as_float(__builtin_amdgcn_update_dpp(0, __float_as_int(v), 0x128, 0xf, 0xf, false)); }
DEVI float xor1(float v) { return __int_as_float(__builtin_amdgcn_update_dpp(0, __float_as_int(v), 0xB1, 0xf, 0xf, false)); }
DEVI float sw32sum(float a, float b) { auto r = __builtin_amdgcn_permlane32_swap(__float_as_uint(a), __float_as_uint(b), false, false); return __uint_as_float(r[0]) + __uint_as_float(r[1]); }
DEVI float sw16sum(float a, float b) { auto r = __builtin_amdgcn_permlane16_swap(__float_as_uint(a), __float_as_uint(b), false, false); return __uint_as_float(r[0]) + __uint_as_float(r[1]); }
DEVI float sw32max(float a) { auto r = __builtin_amdgcn_permlane32_swap(__float_as_uint(a), __float_as_uint(a), false, false); return fmaxf(__uint_as_float(r[0]), __uint_as_float(r[1])); }
DEVI float sw16max(float a) { auto r = __builtin_amdgcn_permlane16_swap(__float_as_uint(a), __float_as_uint(a), false, false); return fmaxf(__uint_as_float(r[0]), __uint_as_float(r[1])); }
DEVI f32x4 fold(f32x4 a, f32x4 b) { f32x4 r; r.x = sw32sum(a.x, b.x); r.y = sw32sum(a.y, b.y); r.z = sw32sum(a.z, b.z); r.w = sw32sum(a.w, b.w); return r; }
DEVI float dot2(unsigned a, unsigned b, float c) { return __builtin_amdgcn_fdot2_f32_bf16(__builtin_bit_cast(bf16x2, a), __builtin_bit_cast(bf16x2, b), c, false); }
DEVI unsigned cvtpk(float lo, float hi) { unsigned r; asm("v_cvt_pk_bf16_f32 %0, %1, %2" : "=v"(r) : "v"(lo), "v"(hi)); return r; }
DEVI float max5x8(float a, float b, float c, float d, float e) {
    float m, t;
    asm("v_max3_f32 %0, %2, %3, %4\n\tv_max3_f32 %0, %0, %5, %6\n\ts_nop 1\n\t"
        "v_max_f32_dpp %0, %0, %0 row_ror:8 row_mask:0xf bank_mask:0xf\n\tv_mov_b32 %1, %0\n\ts_nop 1\n\t"
        "v_permlane16_swap_b32 %0, %1\n\tv_max_f32 %0, %0, %1\n\tv_mov_b32 %1, %0\n\ts_nop 1\n\t"
        "v_permlane32_swap_b32 %0, %1\n\tv_max_f32 %0, %0, %1"
        : "=&v"(m), "=&v"(t) : "v"(a), "v"(b), "v"(c), "v"(d), "v"(e));
    return m;
}
#define ALD16(D, VO, SO, IMM) asm volatile("buffer_load_dwordx4 %0, %1, %2, %3 offen offset:" #IMM " sc1" : "=v"(D) : "v"(VO), "s"(rd), "s"(SO) : "memory")
#define ALD4(D, VO, SO, IMM) asm volatile("buffer_load_dword %0, %1, %2, %3 offen offset:" #IMM " sc1" : "=v"(D) : "v"(VO), "s"(rd), "s"(SO) : "memory")
#define WAITV1(N, A) asm volatile("s_waitcnt vmcnt(" #N ")" : "+v"(A) :: "memory")
#define WAITV2(N, A, B) asm volatile("s_waitcnt vmcnt(" #N ")" : "+v"(A), "+v"(B) :: "memory")
#define SB() __builtin_amdgcn_sched_barrier(0)
#define PIN2(a, b) asm volatile("" : "+v"(a), "+v"(b))
DEVI void sleep_units(int n) {
    if (n & 32) __builtin_amdgcn_s_sleep(32);
    if (n & 16) __builtin_amdgcn_s_sleep(16);
    if (n & 8) __builtin_amdgcn_s_sleep(8);
    if (n & 4) __builtin_amdgcn_s_sleep(4);
    if (n & 2) __builtin_amdgcn_s_sleep(3);
    if (n & 1) __builtin_amdgcn_s_sleep(1);
}
DEVI void sleep_cyc(int n) { sleep_units(min(n >> 6, 63)); }
DEVI unsigned tmax(unsigned a, unsigned b) { return (int)(a - b) > 0 ? a : b; }
DEVI unsigned mt() { return (unsigned)__builtin_amdgcn_s_memtime(); }
DEVI void wait_l(unsigned t) {
    unsigned d;
    asm volatile("1:\n\ts_memrealtime vcc\n\ts_waitcnt lgkmcnt(0)\n\ts_sub_i32 %0, vcc_lo, %1\n\ts_cmp_lt_i32 %0, 0\n\ts_cbranch_scc1 1b"
                 : "=&s"(d) : "s"(__builtin_amdgcn_readfirstlane(t)) : "memory", "scc", "vcc");
}
DEVI void wait_c(unsigned t) {
    unsigned d;
    asm volatile("1:\n\ts_memtime vcc\n\ts_waitcnt lgkmcnt(0)\n\ts_sub_i32 %0, vcc_lo, %1\n\ts_cmp_lt_i32 %0, 0\n\ts_cbranch_scc1 1b"
                 : "=&s"(d) : "s"(__builtin_amdgcn_readfirstlane(t)) : "memory", "scc", "vcc");
}
constexpr unsigned SPINCAP = 40000;
DEVI bool poll_fail(unsigned& spins, GAS unsigned* ctl, int back) {
    sleep_cyc(back);
    ++spins;
    if ((spins & 63u) == 0u) {
        if (spins > SPINCAP) { __hip_atomic_store(ctl + C_ABORT, 1u, RLX_AGENT); return true; }
        if (__builtin_amdgcn_readfirstlane(__hip_atomic_load(ctl + C_ABORT, RLX_AGENT))) return true;
    }
    return false;
}

#define EV(code)
#define EVT 0
DEVI void wait_ge(GAS unsigned* w, unsigned v, GAS unsigned* ctl, bool& dead) {
    unsigned spins = 0;
    while (!dead && (unsigned)__builtin_amdgcn_readfirstlane(__hip_atomic_load(w, RLX_AGENT)) < v) { if (poll_fail(spins, ctl, 64)) dead = true; }
    asm volatile("" ::: "memory");
}

__global__ void __launch_bounds__(320) k_prep(Params p) {
    const int tid = threadIdx.x, lane = tid & 63, wave = tid >> 6, b = blockIdx.x;
    unsigned* ctl = (unsigned*)(p.ws + O_CTL);
    if (b >= 196 && tid < 256) ctl[(b - 196) * 256 + tid] = 0u;
    if (tid == 256) {
        const unsigned page = 1 + (b >> 3) % 20;
        __amdgpu_buffer_rsrc_t rs = __builtin_amdgcn_make_buffer_rsrc(p.ws, 0, 0xFFFFFF00u, 0x00020000);
        unsigned t[2] = {0xffffffffu, 0xffffffffu};
        for (int rep = 0; rep < 3; ++rep)
            for (int side = 0; side < 2; ++side) {
                const unsigned long long c0 = __builtin_amdgcn_s_memtime();
                asm volatile("s_waitcnt lgkmcnt(0)" ::: "memory");
                st4(SENT, rs, page * PAGE + 480 * 1024 + side * 8192 + (b & 7) * 512 + (b >= 160 ? 256 : 0), 0);
                asm volatile("s_waitcnt vmcnt(0)" ::: "memory");
                const unsigned long long c1 = __builtin_amdgcn_s_memtime();
                t[side] = min(t[side], (unsigned)(c1 - c0));
            }
        ctl[C_SIDE + b] = (t[1] < t[0] ? 1u : 0u) ^ (xcc_id() >= 4 ? 1u : 0u) ^ (page & 1u);
    }
    if (wave == 4) return;
    float* R = (float*)(p.ws + O_R); float* G = (float*)(p.ws + O_G);
    v4u xv[2][2], wr[2], gw[2];
#pragma unroll
    for (int q = 0; q < 2; ++q) {
        const int k = q * 512 + lane * 8;
        xv[0][q] = *(const v4u*)(p.x + (b * 8 + wave * 2) * D_ + k); xv[1][q] = *(const v4u*)(p.x + (b * 8 + wave * 2 + 1) * D_ + k);
        wr[q] = *(const v4u*)(p.wr + k); gw[q] = *(const v4u*)(p.gw + k);
    }
    float a[2] = {0.f, 0.f}, g[2] = {0.f, 0.f};
#pragma unroll
    for (int rr = 0; rr < 2; ++rr)
#pragma unroll
        for (int q = 0; q < 2; ++q)
#pragma unroll
            for (int e = 0; e < 4; ++e) {
                const unsigned u = xv[rr][q][e];
                a[rr] = fmaf(lo16(u), lo16(wr[q][e]), a[rr]); a[rr] = fmaf(hi16(u), hi16(wr[q][e]), a[rr]);
                const unsigned pr = pk2(lo16(u) * lo16(gw[q][e]), hi16(u) * hi16(gw[q][e]));
                g[rr] += lo16(pr); g[rr] += hi16(pr);
            }
#pragma unroll
    for (int m = 32; m >= 1; m >>= 1) { a[0] += __shfl_xor(a[0], m); g[0] += __shfl_xor(g[0], m); a[1] += __shfl_xor(a[1], m); g[1] += __shfl_xor(g[1], m); }
    if (lane < 2) { const int row = b * 8 + wave * 2 + lane; R[row] = rnd(lane ? a[1] : a[0]); G[row] = rnd(1.f / (1.f + __expf(-rnd(lane ? g[1] : g[0])))); }
}

constexpr unsigned LC_PA = 0, LC_QK = 24576, LC_V = LC_QK + 4096, LC_O = LC_V + 2048, LC_MISC = 98304 + 16, LC_KV = 33792  , LC_END = LC_KV + 32768;

DEVI void chain_body(const Params& p, const Tim& tm, LAS unsigned char* lds, const int g, const int i, const unsigned tbase) {
    const int tid = threadIdx.x, lane = tid & 63, wave = __builtin_amdgcn_readfirstlane(tid >> 6);
    const int h = g * 8 + (i >> 2), j = i & 3, cid = h * 4 + j;
    const int kw = (wave + i) & 7;
    GAS unsigned* ctl = (GAS unsigned*)(p.ws + O_CTL);
    const __amdgpu_buffer_rsrc_t rs = __builtin_amdgcn_make_buffer_rsrc(p.ws, 0, 0xFFFFFF00u, 0x00020000);
    const unsigned sidemask = side_mask(ctl, lane);
    v8u WA[6][2], WO[2][2];
    v4u sa[2][12], so[8];
    {
        const u16* wqk = (j < 2 ? p.wq : p.wk) + h * 128 + 64 * (j & 1) + (lane & 7) * 8;
        const u16* wvv = p.wv + h * 128 + 32 * j + (lane & 3) * 8;
#pragma unroll
        for (int ks = 0; ks < 2; ++ks) {
#pragma unroll
            for (int q = 0; q < 8; ++q) sa[ks][q] = *(const v4u*)(wqk + (size_t)(128 * kw + 64 * ks + 8 * q + (lane >> 3)) * 4096);
#pragma unroll
            for (int q = 0; q < 4; ++q) sa[ks][8 + q] = *(const v4u*)(wvv + (size_t)(128 * kw + 64 * ks + 16 * q + (lane >> 2)) * 4096);
        }
#pragma unroll
        for (int q = 0; q < 8; ++q) so[q] = *(const v4u*)(p.wo + (size_t)(h * 128 + 16 * wave + 2 * q + (lane >> 5)) * 1024 + 256 * j + (lane & 31) * 8);
    }
    SB();
    const int tau = lane >> 5, rh = (lane >> 4) & 1, n16 = lane & 15, c8 = (lane >> 3) & 1, g4 = lane >> 4;
    const unsigned o_gm = amap(g * 16384 + kw * 2048 + lane * 16);
    const unsigned qkvb = L_QKV + h * 6144;
    const int pcol = 256 * j + 32 * wave + 16 * tau + n16;
    const unsigned o_pp = amap((pcol >> 3) * 4096 + h * 128 + (rh * 8 + (pcol & 7)) * 8);
    const int sidx = c8 ? 0xEEEE : 0x4444;
    LAS unsigned char* const pw = lds + LC_PA + ((wave * 2 + rh) * 96 + 16 * tau + n16) * 16;
    LAS unsigned char* const ob = lds + LC_O + (lane & 7) * 320 + c8 * 16 + g4 * 32;
    const v4u rd = {(unsigned)(uintptr_t)p.ws, (unsigned)((uintptr_t)p.ws >> 32) & 0xffffu, 0xFFFFFF00u, 0x00020000u};
    const unsigned o_i1 = amap(qkvb + j * 1024 + lane * 16);
    const unsigned o_i2 = lane < 32 ? amap(qkvb + 4096 + j * 512 + lane * 16) : amap(((lane >> 3) & 3) * 16384 + cid * 128 + (lane & 7) * 16);
    {
        const v4u zz = {SENT, SENT, SENT, SENT};
        if (wave < 2) {
            const unsigned sb = P_HAND * PAGE + wave * 524288u, sx = ((sidemask >> P_HAND) & 1u) << 13;
            st16(zz, rs, o_i1 ^ sx, sb);
            v4u iv = zz;
            if (wave == 0 && lane >= 32) {
                iv = *(const v4u*)(p.mem0 + (lane & 7) * 1024 + cid * 8);
                iv = (v4u){nosent(iv.x), nosent(iv.y), nosent(iv.z), nosent(iv.w)};
            }
            st16(iv, rs, o_i2 ^ sx, sb);
        }
#pragma unroll
        for (int sl = 0; sl < 2; ++sl) st8((v2u){SENT, SENT}, rs, o_pp ^ (((sidemask >> (P_PART + sl)) & 1u) << 13), (P_PART + sl) * PAGE);
    }
    bool dead = false;
    const unsigned o_kv = (wave & 2 ? O_VX : O_KX) + (wave & 1) * 4096 + lane * 16, fpar = far_par(sidemask);
    v4u p0, p1, p2, p3;
    {
        LAS unsigned char* img = lds + wave * 12288;
        const int wr = lane >> 2;
        LAS unsigned char* iw = img + ((wr & 9) | ((wr >> 1) & 2) | ((wr << 1) & 4)) * 64 + (((lane & 3) ^ ((wr >> 2) & 2)) * 16);
        const int gB = lane >> 4, q4 = (lane & 15) >> 2, p4 = lane & 3;
        LAS unsigned char* ir = img + (16 * (gB >> 1) + 2 * (gB & 1) + (q4 & 1) + 8 * (q4 >> 1)) * 64 + p4 * 8;
        const unsigned hs = (q4 >> 1) * 32;
        LAS unsigned char* jw = img + (lane >> 3) * 128 + (lane & 1) * 16;
        const int jc = ((lane >> 1) & 3) ^ (lane >> 5);
        LAS unsigned char* jr = img + (16 * (gB >> 1) + 4 * (gB & 1) + (q4 & 1) + 8 * (q4 >> 1)) * 128 + p4 * 8;
        const int js = (gB & 1) | ((q4 >> 1) << 1);
        auto shuffleA = [&](int ks) {
#pragma unroll
            for (int q = 0; q < 8; ++q) *(LAS v4u*)(jw + q * 1024 + ((jc ^ ((q & 1) << 1)) * 32)) = sa[ks][q];
#pragma unroll
            for (int q = 0; q < 4; ++q) *(LAS v4u*)(iw + 8192 + q * 1024) = sa[ks][8 + q];
            asm volatile("s_waitcnt lgkmcnt(0)" ::: "memory");
#pragma unroll
            for (int t = 0; t < 6; ++t)
#pragma unroll
                for (int m = 0; m < 4; ++m) {
                    const v2u r = t < 4 ? trrd(jr + ((t ^ js) * 32) + (32 * (m >> 1) + 2 * (m & 1)) * 128) : trrd(ir + 8192 + (((t & 1) * 32) ^ hs) + (32 * (m >> 1) + 4 * (m & 1)) * 64);
                    WA[t][ks][2 * m] = r.x; WA[t][ks][2 * m + 1] = r.y;
                }
            asm volatile("s_waitcnt lgkmcnt(0)" ::: "memory");
        };
        shuffleA(0);
        SB();
        if (wave >= 4) wait_ge(ctl + C_KV + 8 * h, 4u, ctl, dead);
        asm volatile("s_waitcnt vmcnt(0)" ::: "memory");
        if (wave >= 4) { const unsigned so = fb(h, fpar); ALD16(p0, o_kv, so, 0); ALD16(p1, o_kv, so, 1024); ALD16(p2, o_kv, so, 2048); ALD16(p3, o_kv, so, 3072); }
        BAR();
        EV(2);
        if (tid == 0) {
            if (__hip_atomic_fetch_add(ctl + C_ARRIVE, 1u, RLX_AGENT) == 127u) __hip_atomic_store(ctl + C_T0, ((rt() + tm.t0) & ~3u) | 0x80000000u, RLX_AGENT);
        }
        SB();
        shuffleA(1);
        BAR();
#pragma unroll
        for (int q = 0; q < 8; ++q) {
            const int f = (lane >> 5) | (((q >> 1) & 3) << 1);
            *(LAS v4u*)(lds + wave * 8192 + q * 1024 + (lane >> 5) * 512 + ((((lane >> 1) & 15) ^ f) * 32) + (lane & 1) * 16) = so[q];
        }
        BAR();
        const int fo = (q4 & 1) | (js << 1);
        LAS unsigned char* orp = lds + (16 * (gB >> 1) + 4 * (gB & 1) + (q4 & 1) + 8 * (q4 >> 1)) * 512 + p4 * 8;
#pragma unroll
        for (int t = 0; t < 2; ++t)
#pragma unroll
            for (int ks = 0; ks < 2; ++ks)
#pragma unroll
                for (int m = 0; m < 4; ++m) {
                    const v2u r = trrd(orp + (((2 * wave + t) ^ fo) * 32) + (64 * ks + 32 * (m >> 1) + 2 * (m & 1)) * 512);
                    WO[t][ks][2 * m] = r.x; WO[t][ks][2 * m + 1] = r.y;
                }
    }
#define STAMP(k)
#define STAMPN(k)
#define ROLL(n)
#define NSP(k, v)
#define DUMP()
#define LATE(k, c)
#define XREC()
#define HAND_ADDR(s) do { const unsigned pg = P_HAND + ((s) >> 2); sb = pg * PAGE + ((s) & 3) * 524288u; sx = ((sidemask >> pg) & 1u) << 13; } while (0)
#define RETRY(K, RELOAD, BAD) do { unsigned spins = 0;                                                                    \
                while (!dead) { if (poll_fail(spins, ctl, 0)) { dead = true; break; } RELOAD; if (!(BAD)) break; }  \
                NSP(K, spins); } while (0)
#define ZERO_A() f32x4 c0 = {0, 0, 0, 0}, c1 = c0, c2 = c0, c3 = c0, c4 = c0, c5 = c0; PIN2(c0, c1); PIN2(c2, c3); PIN2(c4, c5)
#define GATHER_MEM(EXTRA, NX) v4u a0, a1;                                                                                 \
            ALD16(a0, o_gmx, sb, 0); ALD16(a1, o_gmx, sb, 1024); EXTRA; WAITV2(NX, a0, a1);                               \
            if (__builtin_expect(any_sent(mx4(a1, mx4(a0, 0u))), 0))                                                      \
                RETRY(0, ALD16(a0, o_gmx, sb, 0); ALD16(a1, o_gmx, sb, 1024); WAITV2(0, a0, a1), any_sent(mx4(a1, mx4(a0, 0u))))
#define QK_MFMA() do {                                                                                                    \
            c0 = smf(a0, WA[0][0], c0, sidx); c1 = smf(a0, WA[1][0], c1, sidx); c0 = smf(a1, WA[0][1], c0, sidx); c1 = smf(a1, WA[1][1], c1, sidx); \
            SB();                                                                                                         \
            c2 = smf(a0, WA[2][0], c2, sidx); c3 = smf(a0, WA[3][0], c3, sidx); c2 = smf(a1, WA[2][1], c2, sidx); c3 = smf(a1, WA[3][1], c3, sidx); \
            SB(); *(LAS f32x4*)pw = fold(c0, c1); SB(); *(LAS f32x4*)(pw + 512) = fold(c2, c3); SB(); } while (0)
#define V_PART() do { c4 = smf(a0, WA[4][0], c4, sidx); c5 = smf(a0, WA[5][0], c5, sidx); c4 = smf(a1, WA[4][1], c4, sidx); c5 = smf(a1, WA[5][1], c5, sidx); \
            *(LAS f32x4*)(pw + 1024) = fold(c4, c5);                                                                      \
            if (lane == 0) __hip_atomic_fetch_add((LAS unsigned*)(lds + LC_MISC + 4), 1u, __ATOMIC_RELAXED, __HIP_MEMORY_SCOPE_WORKGROUP); } while (0)
#define ZERO_D() f32x4 d0 = {0, 0, 0, 0}, d1 = d0; const unsigned pp = P_PART + (s & 3), px = ((sidemask >> pp) & 1u) << 13; unsigned o_ppx = o_pp ^ px; PIN2(d0, d1); asm volatile("" : "+v"(o_ppx))
#define WO_STAGE() do {                                                                                                   \
            const v4u o0 = *(LAS v4u*)ob, o1 = *(LAS v4u*)(ob + 128); SB();                                               \
            d0 = smf(o0, WO[0][0], d0, sidx); d1 = smf(o0, WO[1][0], d1, sidx); d0 = smf(o1, WO[0][1], d0, sidx); d1 = smf(o1, WO[1][1], d1, sidx); \
            const f32x4 f = fold(d0, d1);                                                                                 \
            st8((v2u){nosent(pk2(f.x, f.y)), nosent(pk2(f.z, f.w))}, rs, o_ppx, pp * PAGE); } while (0)
#define REDUCE4(LP, OFF) do {                                                                                             \
            const f32x4 r0 = *(LAS f32x4*)(LP), r1 = *(LAS f32x4*)((LP) + 3072), r2 = *(LAS f32x4*)((LP) + 6144), r3 = *(LAS f32x4*)((LP) + 9216); \
            const f32x4 t = (r0 + r1) + (r2 + r3);                                                                        \
            st4(nosent(pk2(sw32sum(t.x, t.z), sw32sum(t.y, t.w))), rs, (OFF), sb); } while (0)
    unsigned sb, sx;
    if (wave < 4) {
        const unsigned o_gq = amap(qkvb + wave * 1024 + lane * 16);
        const unsigned o_gp = amap(cid * 4096 + lane * 16);
        const int crow = 4 * ((lane >> 2) & 1) + g4;
        v4u sel = {0u, 0u, 0u, 0u};
        sel[(lane & 7) >> 1] = (lane & 1) ? 0x3f800000u : 0x00003f80u;
        const unsigned o_pm = amap(c8 * 16384 + cid * 128 + crow * 16 + (lane & 3) * 4);
        const unsigned o_gv = amap(qkvb + 4096 + wave * 512 + n16 * 16 + (4 * (g4 & 1) + 2 * (g4 >> 1)) * 2);
        LAS unsigned char* const qb = lds + LC_QK + (4 * g4 + ((lane & 15) >> 2)) * 16 + (lane & 1) * 8;
        LAS unsigned char* const qw = lds + LC_QK + wave * 1024 + lane * 16;
        LAS unsigned char* const ow = lds + LC_O + (lane & 7) * 320 + (32 * wave + 16 * c8 + 4 * g4) * 2;
        if (tid == 0) *(LAS unsigned*)(lds + LC_MISC + 4) = 0u;
        BAR();
        EV(3);
        if (wave == 0) {
            unsigned plan, spins = 0;
            for (;;) { plan = __builtin_amdgcn_readfirstlane(__hip_atomic_load(ctl + C_T0, RLX_AGENT)); if (plan) break; if (poll_fail(spins, ctl, 64)) { dead = true; break; } }
            { const unsigned now = rt(); plan = now + ((int)((plan - now) << 1) >> 1); }
            if (!dead) wait_l(plan);
        }
        EV(4);
        v4u kx[8], vx[2];
        unsigned tq;
#define A_STAGE_A(S1)                                                                                                     \
            BAR();                                                                                  \
            STAMPN(0);                                                                                                    \
            GATHER_MEM({ LAS unsigned char* kvb = lds + LC_KV + ((S1) & 1) * 16384 + lane * 16;                           \
                _Pragma("unroll") for (int q = 0; q < 8; ++q) kx[q] = *(LAS v4u*)(kvb + q * 1024);                        \
                vx[0] = *(LAS v4u*)(kvb + 8192 + wave * 2048); vx[1] = *(LAS v4u*)(kvb + 8192 + wave * 2048 + 1024); SB(); }, 0); \
            STAMPN(1);                                                                                                    \
            QK_MFMA();                                                                                                    \
            BAR();                                                                                                 \
            tq = mt() + tm.dq;                                                                                          \
            STAMPN(2);                                                                                                    \
            V_PART()
        {
            HAND_ADDR(0);
            ZERO_A();
            const unsigned o_gmx = o_gm ^ sx;
            A_STAGE_A(0);
        }
        for (int s = 0; s < NSEG - 1; ++s) {
            __builtin_amdgcn_s_setprio(3);
            ROLL(3); STAMP(3);
            v4u gq;
            ZERO_D();
            unsigned o_gqx = o_gq ^ sx, o_gvx = o_gv ^ sx; PIN2(o_gqx, o_gvx);
            wait_c(tq);
            STAMP(4);
            ALD16(gq, o_gqx, sb, 0); WAITV1(0, gq);
            *(LAS v4u*)qw = gq;
            SB();
            if (__builtin_expect(any_sent(mx4(gq, 0u)), 0)) { RETRY(1, ALD16(gq, o_gqx, sb, 0); WAITV1(0, gq), any_sent(mx4(gq, 0u))); *(LAS v4u*)qw = gq; }
            STAMP(5);
            BAR();
            {
                unsigned va, vc;
                ALD4(va, o_gvx, sb, 0); ALD4(vc, o_gvx, sb, 256);
                v4u qf[4], kf[4];
#pragma unroll
                for (int c = 0; c < 4; ++c) { const v2u qa = trrd(qb + c * 512), qc = trrd(qb + c * 512 + 256); qf[c] = (v4u){qa.x, qa.y, qc.x, qc.y}; }
#pragma unroll
                for (int c = 0; c < 4; ++c) { const v2u ka = trrd(qb + c * 512 + 2048), kc = trrd(qb + c * 512 + 2304); kf[c] = (v4u){ka.x, ka.y, kc.x, kc.y}; }
                SB();
                f32x4 s0 = {0, 0, 0, 0}, s1 = s0, sm = s0;
#pragma unroll
                for (int c = 0; c < 4; ++c) { s0 = mfma32(kx[c], qf[c], s0); s1 = mfma32(kx[4 + c], qf[c], s1); sm = mfma32(kf[c], qf[c], sm); }
                SB();
                float x0 = c8 ? s1.x : s0.x, x1 = c8 ? s1.y : s0.y, x2 = c8 ? s1.z : s0.z, x3 = c8 ? s1.w : s0.w;
                float xm = tau ? (c8 ? sm.w : sm.z) : (c8 ? sm.y : sm.x);
                constexpr float CS = SCALE * LOG2E;
                const float ml = -CS * max5x8(x0, x1, x2, x3, xm);
                STAMP(6);
                x0 = __builtin_amdgcn_exp2f(fmaf(x0, CS, ml)); x1 = __builtin_amdgcn_exp2f(fmaf(x1, CS, ml));
                x2 = __builtin_amdgcn_exp2f(fmaf(x2, CS, ml)); x3 = __builtin_amdgcn_exp2f(fmaf(x3, CS, ml));
                xm = __builtin_amdgcn_exp2f(fmaf(xm, CS, ml));
                const unsigned e01 = pk2(x0, x1), e23 = pk2(x2, x3), em = pk2(xm, xm);
                const unsigned f01 = __float_as_uint(ror8(__uint_as_float(e01))), f23 = __float_as_uint(ror8(__uint_as_float(e23)));
                const v4u b1 = {c8 ? f01 : e01, c8 ? f23 : e23, c8 ? e01 : f01, c8 ? e23 : f23};
                unsigned b2 = em & (c8 ? 0xffff0000u : 0x0000ffffu);
                b2 |= __float_as_uint(ror8(__uint_as_float(b2)));
                asm volatile("" : "+v"(b2));
                f32x4 o0 = {0, 0, 0, 0}, o1 = o0;
                o0 = mfma32(vx[0], b1, o0); o1 = mfma32(vx[1], b1, o1);
                SB();
                WAITV2(0, va, vc);
                if (__builtin_expect(any_sent(max(va, vc)), 0)) RETRY(1, ALD4(va, o_gvx, sb, 0); ALD4(vc, o_gvx, sb, 256); WAITV2(0, va, vc), any_sent(max(va, vc)));
                o0 = mfma16((v2u){va, 0u}, (v2u){b2, 0u}, o0); o1 = mfma16((v2u){vc, 0u}, (v2u){b2, 0u}, o1);
                SB();
                float l = ((x0 + x1) + (x2 + x3)) + xm;
                l += ror8(l); l = sw16sum(l, l); l = sw32sum(l, l);
                const float rinv = __builtin_amdgcn_rcpf(l);
                SB();
                float y[4];
#pragma unroll
                for (int e = 0; e < 4; ++e) y[e] = (c8 ? o1[e] : o0[e]) * rinv;
                *(LAS v2u*)ow = (v2u){pk2(y[0], y[1]), pk2(y[2], y[3])};
            }
            BAR();
            WO_STAGE();
            const unsigned t3 = mt() + tm.d3;
            STAMP(7);
            __builtin_amdgcn_s_setprio(0);
            HAND_ADDR(s + 1);
            ZERO_A();
            unsigned o_gmx = o_gm ^ sx; asm volatile("" : "+v"(o_gmx));
            if (__builtin_expect(wave == 0, 1)) {
                unsigned o_pmx = o_pm ^ sx, o_gpx = o_gp ^ px; PIN2(o_pmx, o_gpx);
                wait_c(t3);
                STAMP(8);
                v4u r0, r1, r2, r3; f32x4 u; unsigned m;
#define C_LOAD() ALD16(r0, o_gpx, pp * PAGE, 0); ALD16(r1, o_gpx, pp * PAGE, 1024); ALD16(r2, o_gpx, pp * PAGE, 2048); ALD16(r3, o_gpx, pp * PAGE, 3072);  \
                WAITV1(3, r0); u = mfma32(sel, r0, (f32x4){0, 0, 0, 0}); m = mx4(r0, 0u); SB(); WAITV1(2, r1); u = mfma32(sel, r1, u); m = mx4(r1, m); SB(); \
                WAITV1(1, r2); u = mfma32(sel, r2, u); m = mx4(r2, m); SB(); WAITV1(0, r3); u = mfma32(sel, r3, u); m = mx4(r3, m)
                C_LOAD();
                if (__builtin_expect(any_sent(m), 0)) RETRY(2, C_LOAD(), any_sent(m));
                float z0 = tau ? u.z : u.x, z1 = tau ? u.w : u.y;
                z0 += ror8(z0); z1 += ror8(z1);
                { auto sw = __builtin_amdgcn_permlane16_swap(__float_as_uint(z0), __float_as_uint(z1), false, false); z0 = __uint_as_float(sw[0]); z1 = __uint_as_float(sw[1]); }
                const unsigned w2 = nosent(pk2(z0, z1));
                st4(w2, rs, o_pmx, sb); st4(w2, rs, o_pmx + 65536, sb);
                STAMP(9);
                const unsigned plan = mt() + tm.d1;
                DUMP();
                wait_c(plan);
            } else { DUMP(); }
            A_STAGE_A(s + 1);
        }
    } else {
        const int hw = wave - 4;
        LAS unsigned char* const l_qk = lds + LC_PA + tau * 12288 + (rh * 96 + 16 * hw + n16) * 16;
        const unsigned o_pqk = amap(qkvb + j * 1024 + (16 * hw + n16) * 16 + rh * 8 + tau * 4);
        LAS unsigned char* const l_v = lds + LC_PA + tau * 12288 + (rh * 96 + 64 + 16 * (hw & 1) + n16) * 16;
        const unsigned o_pv = amap(qkvb + 4096 + j * 512 + (16 * (hw & 1) + n16) * 16 + rh * 8 + tau * 4);
        LAS unsigned char* const l_kv = lds + LC_KV + hw * 4096 + lane * 16;
        WAITV2(0, p0, p1); WAITV2(0, p2, p3);
        BAR();
        *(LAS v4u*)l_kv = p0; *(LAS v4u*)(l_kv + 1024) = p1; *(LAS v4u*)(l_kv + 2048) = p2; *(LAS v4u*)(l_kv + 3072) = p3;
        __builtin_amdgcn_s_setprio(2);
        for (int s = -1; s < NSEG - 1; ++s) {
            if (s >= 0) {
                ROLL(7);
                ZERO_D();
                BAR();
                __builtin_amdgcn_s_sleep(3);
                { const unsigned so = fb((s + 1) * 32 + h, fpar); ALD16(p0, o_kv, so, 0); ALD16(p1, o_kv, so, 1024); ALD16(p2, o_kv, so, 2048); ALD16(p3, o_kv, so, 3072); }
                {
                    const unsigned rp = P_PART + ((s + 2) & 3), rx = ((sidemask >> rp) & 1u) << 13;
                    st8((v2u){SENT, SENT}, rs, o_pp ^ rx, rp * PAGE); st8((v2u){SENT, SENT}, rs, (o_pp - 8 * 16384) ^ rx, rp * PAGE);
                    if (hw >= 2 && s + 2 < NSEG) {
                        const unsigned pg = P_HAND + ((s + 2) >> 2);
                        st16((v4u){SENT, SENT, SENT, SENT}, rs, (hw == 2 ? o_i1 : o_i2) ^ (((sidemask >> pg) & 1u) << 13), pg * PAGE + ((s + 2) & 3) * 524288u);
                    }
                }
                STAMP(7); STAMP(8);
                BAR();
                STAMP(9);
                WO_STAGE();
                STAMP(10);
                DUMP();
                if (j == 0 && tid == 256) __hip_atomic_store(ctl + C_PROG + 32 * h, (unsigned)(s + 1), RLX_AGENT);
                if (((s + 2) & 3) == 0 && s + 2 < NSEG) wait_ge(ctl + C_KV + 8 * (((s + 2) >> 2) * 32 + h), 2u, ctl, dead);
                WAITV2(0, p0, p1); WAITV2(0, p2, p3);
                LAS unsigned char* kw = l_kv + ((s + 1) & 1) * 16384;
                *(LAS v4u*)kw = p0; *(LAS v4u*)(kw + 1024) = p1; *(LAS v4u*)(kw + 2048) = p2; *(LAS v4u*)(kw + 3072) = p3;
            }
            HAND_ADDR(s + 1);
            ZERO_A();
            unsigned o_pqkx = o_pqk ^ sx, o_gmx = o_gm ^ sx; PIN2(o_pqkx, o_gmx);
            BAR();
            STAMPN(0);
            GATHER_MEM(, 0);
            STAMPN(1);
            QK_MFMA();
            STAMPN(2);
            BAR();
            STAMPN(3);
            REDUCE4(l_qk, o_pqkx);
            SB();
            STAMPN(4);
            if (s == NSEG - 2 && j == 0 && tid == 256) __hip_atomic_store(ctl + C_PROG + 32 * h, (unsigned)NSEG, RLX_AGENT);
            V_PART();
            STAMPN(5);
            if (hw < 2) {
                while (__builtin_amdgcn_readfirstlane(*(volatile LAS unsigned*)(lds + LC_MISC + 4)) < 8u * (unsigned)(s + 2)) { }
                asm volatile("" ::: "memory");
                REDUCE4(l_v, o_pv ^ sx);
            }
            STAMPN(6);
        }
    }
}

DEVI int claim_slot(GAS unsigned* ctl) {
    const unsigned x = xcc_id() & 7u;
    const unsigned r = __hip_atomic_fetch_add(ctl + C_XR + 32 * x, 1u, RLX_AGENT);
    if (r < 32 && __hip_atomic_exchange(ctl + C_SLOT + x * 32 + r, 1u, RLX_AGENT) == 0u) return (int)(x * 32 + r);
    for (int k = 255;; k = (k + 255) & 255) if (__hip_atomic_exchange(ctl + C_SLOT + k, 1u, RLX_AGENT) == 0u) return k;
}

DEVI void glds(unsigned voff, const void* sbase, unsigned ldsaddr) {
    unsigned keep;
    asm volatile("s_mov_b32 %0, m0\n\ts_mov_b32 m0, %3\n\ts_nop 2\n\tglobal_load_lds_dwordx4 %1, %2\n\ts_mov_b32 m0, %0" : "=&s"(keep) : "v"(voff), "s"(sbase), "s"(ldsaddr) : "memory");
}
template <int BM, int BN, int WM, bool TR, bool PERMK>
struct Gemm {
    static constexpr int WN = 8 / WM, MT = BM / WM / 16, NT = BN / WN / 16, NA = BM / 64, NB = BN / 64, STAGE = (BM + BN) * 128, NS = 147456 / STAGE, LPR = BN / 8;
    f32x4 acc[MT][NT];
    DEVI void run(LAS unsigned char* lds, const u16* A, int lda, const u16* B, int ldb, int nk) {
        const int tid = otid(), lane = tid & 63, wave = __builtin_amdgcn_readfirstlane(tid >> 6);
        const int wm = wave / WN, wn = wave % WN;
        unsigned va[NA], vb[NB];
#pragma unroll
        for (int q = 0; q < NA; ++q) va[q] = ((wave * NA * 8 + q * 8 + (lane >> 3)) * lda + (((lane & 7) ^ ((lane >> 3) & 7)) * 8)) * 2;
#pragma unroll
        for (int q = 0; q < NB; ++q) {
            const int k = (wave * NB + q) * (64 / LPR) + lane / LPR, sl = lane % LPR;
            const int pk = PERMK ? ((k & 32) + 16 * ((k >> 2) & 1) + 4 * ((k >> 3) & 3) + (k & 3)) : k;
            const int f = (k & 3) | (((k >> 3) & 1) << 2);
            vb[q] = (pk * ldb + (((sl >> 1) ^ f) * 16 + (sl & 1) * 8)) * 2;
        }
        const unsigned lbase = (unsigned)(uintptr_t)lds;
        auto piece = [&](int kt, int buf, int q) {
            if (q < NA) glds(va[q], A + kt * 64, lbase + buf * STAGE + (wave * NA + q) * 1024);
            else glds(vb[q - NA], B + (size_t)kt * 64 * ldb, lbase + buf * STAGE + BM * 128 + (wave * NB + q - NA) * 1024);
        };
        const int fr = lane & 15, fg = lane >> 4;
        const unsigned ra = (wm * (BM / WM) + fr) * 128;
        const unsigned ra0 = ra + ((fg ^ (fr & 7)) * 16), ra1 = ra + (((4 + fg) ^ (fr & 7)) * 16);
        const int q4 = fr >> 2, p4 = fr & 3, fsw = q4 | ((fg & 1) << 2);
        const unsigned rb = BM * 128 + (8 * fg + q4) * (BN * 2) + p4 * 8 + (((wn * NT) ^ fsw) * 32);
        v4u a0[MT], b0[NT], a1[MT], b1[NT];
        auto frag = [&](LAS unsigned char* sp, int ks, v4u* af, v4u* bf, int r) {
            if (r < NT) { LAS unsigned char* q = sp + (rb ^ (r * 32)) + ks * (BN * 64); const v2u x = trrd(q), y = trrd(q + BN * 8); bf[r] = (v4u){x.x, x.y, y.x, y.y}; }
            else af[r - NT] = *(LAS v4u*)(sp + (ks ? ra1 : ra0) + (r - NT) * 2048);
        };
        constexpr int NM = MT * NT, NF = MT + NT, NP = NA + NB;
        auto half = [&](const v4u* af, const v4u* bf, LAS unsigned char* np, int nks, v4u* naf, v4u* nbf, bool rd, bool stg, int skt, int sbuf) {
#pragma unroll
            for (int i = 0; i < NM; ++i) {
                const int m = i / NT, n = i % NT;
                acc[m][n] = TR ? mfma32(bf[n], af[m], acc[m][n]) : mfma32(af[m], bf[n], acc[m][n]);
#pragma unroll
                for (int r = i * NF / NM; r < (i + 1) * NF / NM; ++r) if (rd) frag(np, nks, naf, nbf, r);
#pragma unroll
                for (int q = i * NP / NM; q < (i + 1) * NP / NM; ++q) if (stg) piece(skt, sbuf, q);
                SB();
            }
        };
#pragma unroll
        for (int m = 0; m < MT; ++m)
#pragma unroll
            for (int n = 0; n < NT; ++n) acc[m][n] = (f32x4){0, 0, 0, 0};
#pragma unroll
        for (int q = 0; q < NS * NP; ++q) piece(q / NP, q / NP, q % NP);
        asm volatile("s_waitcnt vmcnt(%0)\n\ts_barrier" :: "n"((NS - 1) * NP) : "memory");
#pragma unroll
        for (int r = 0; r < NF; ++r) frag(lds, 0, a0, b0, r);
        int buf = 0;
        for (int kt = 0; kt < nk - NS; ++kt) {
            const int nb = buf == NS - 1 ? 0 : buf + 1;
            half(a0, b0, lds + buf * STAGE, 1, a1, b1, true, false, 0, 0);
            asm volatile("s_waitcnt vmcnt(%0) lgkmcnt(0)\n\ts_barrier" :: "n"((NS - 2) * NP) : "memory");
            half(a1, b1, lds + nb * STAGE, 0, a0, b0, true, true, kt + NS, buf);
            buf = nb;
        }
#pragma unroll
        for (int t = NS; t >= 1; --t) {
            const int nb = buf == NS - 1 ? 0 : buf + 1;
            half(a0, b0, lds + buf * STAGE, 1, a1, b1, true, false, 0, 0);
            asm volatile("s_waitcnt vmcnt(%0) lgkmcnt(0)\n\ts_barrier" :: "n"(t >= 2 ? (t - 2) * NP : 0) : "memory");
            half(a1, b1, lds + nb * STAGE, 0, a0, b0, t > 1, false, 0, 0);
            buf = nb;
        }
    }
};
DEVI v4u pk8(f32x4 a, f32x4 b) { return (v4u){pk2(a.x, a.y), pk2(a.z, a.w), pk2(b.x, b.y), pk2(b.z, b.w)}; }
DEVI void announce(GAS unsigned* w) {
    asm volatile("s_waitcnt vmcnt(0) lgkmcnt(0)\n\ts_barrier" ::: "memory");
    if (otid() == 0) __hip_atomic_fetch_add(w, 1u, RLX_AGENT);
}

template <int BM, int BN>
DEVI void kv_unit(const Params& p, LAS unsigned char* lds, int r0, int hh, int mat, unsigned fpar) {
    constexpr int WM = BM >= 128 ? 4 : 2, WN = 8 / WM, MT = BM / WM / 16, NT = BN / WN / 16;
    const int lane = otid() & 63, wave = __builtin_amdgcn_readfirstlane(otid() >> 6), wm = wave / WN, wn = wave % WN;
    const __amdgpu_buffer_rsrc_t rs = __builtin_amdgcn_make_buffer_rsrc(p.ws, 0, 0xFFFFFF00u, 0x00020000);
    const int row0 = r0 + wm * (BM / WM), hw = hh + wn * NT / 8, n0 = (wn * NT) & 7;
    if (mat == 0) {
        Gemm<BM, BN, WM, true, false> g; g.run(lds, p.x + (size_t)r0 * D_, D_, p.wk + hh * 128, 4096, 16);
#pragma unroll
        for (int m = 0; m < MT; ++m)
#pragma unroll
            for (int c = 0; c < NT / 2; ++c) {
                const int row = row0 + m * 16;
                st16(pk8(g.acc[m][2 * c], g.acc[m][2 * c + 1]), rs, lane * 16, O_KX + fb((row >> 5) * 32 + hw, fpar) + (((row >> 4) & 1) * 4 + n0 / 2 + c) * 1024);
            }
    } else {
        Gemm<BM, BN, WM, false, false> g; g.run(lds, p.x + (size_t)r0 * D_, D_, p.wv + hh * 128, 4096, 16);
#pragma unroll
        for (int m = 0; m < MT; m += 2)
#pragma unroll
            for (int n = 0; n < NT; ++n) {
                const int row = row0 + m * 16;
                st16(pk8(g.acc[m][n], g.acc[m + 1][n]), rs, lane * 16, O_VX + fb((row >> 5) * 32 + hw, fpar) + (n0 + n) * 1024);
            }
    }
    asm volatile("s_waitcnt vmcnt(0) lgkmcnt(0)\n\ts_barrier" ::: "memory");
    const int t = otid();
    if (t < (BM == 256 ? 2 : 1) * (BN / 128))
        __hip_atomic_fetch_add((GAS unsigned*)(p.ws + O_CTL) + C_KV + 8 * (((r0 >> 7) + t / (BN / 128)) * 32 + hh + t % (BN / 128)), 1u, RLX_AGENT);
}

#define FS(k)
#define QS(k)
DEVI void qa_unit(const Params& p, LAS unsigned char* lds, int panel, int hh, unsigned sidemask, bool& dead) {
    const int lane = otid() & 63, wave = __builtin_amdgcn_readfirstlane(otid() >> 6);
    const int g4 = lane >> 4, n16 = lane & 15;
    const __amdgpu_buffer_rsrc_t rs = __builtin_amdgcn_make_buffer_rsrc(p.ws, 0, 0xFFFFFF00u, 0x00020000);
    GAS unsigned* ctl = (GAS unsigned*)(p.ws + O_CTL);
    v4u qf[2][4];
    {
        Gemm<256, 128, 8, true, false> g; g.run(lds, p.x + (size_t)panel * 256 * D_, D_, p.wq + hh * 128, 4096, 16);
#pragma unroll
        for (int m = 0; m < 2; ++m)
#pragma unroll
            for (int c = 0; c < 4; ++c) qf[m][c] = pk8(g.acc[m][2 * c], g.acc[m][2 * c + 1]);
    }
    const int seg = panel * 8 + wave;
    const unsigned fpar = far_par(sidemask), kvo = fb(seg * 32 + hh, fpar);
    wait_ge(ctl + C_KV + 8 * ((seg >> 2) * 32 + hh), seg >> 2 ? 2u : 4u, ctl, dead);
    v4u kx[8], vx[8];
#pragma unroll
    for (int q = 0; q < 8; ++q) kx[q] = ld16(rs, lane * 16 + q * 1024, O_KX + kvo);
#pragma unroll
    for (int q = 0; q < 8; ++q) vx[q] = ld16(rs, lane * 16 + q * 1024, O_VX + kvo);
    f32x4 z = {0, 0, 0, 0};
    f32x4 s00 = z, s10 = z, s11 = z, sm0 = z, sm1 = z;
#pragma unroll
    for (int c = 0; c < 4; ++c) { s00 = mfma32(kx[c], qf[0][c], s00); s10 = mfma32(kx[c], qf[1][c], s10); s11 = mfma32(kx[4 + c], qf[1][c], s11); }
    LAS unsigned char* img = lds + wave * 6144;
    wait_ge(ctl + C_PROG + 32 * hh, (unsigned)(seg + 1), ctl, dead);
    QS(0);
    {
        const unsigned pg = P_HAND + (seg >> 2), sb = pg * PAGE + (seg & 3) * 524288u, sx = ((sidemask >> pg) & 1u) << 13;
        v4u t[6];
        for (unsigned spins = 0;;) {
            unsigned m = 0;
#pragma unroll
            for (int q = 0; q < 6; ++q) t[q] = ld16(rs, amap(L_QKV + hh * 6144 + q * 1024 + lane * 16) ^ sx, sb);
#pragma unroll
            for (int q = 0; q < 6; ++q) m = mx4(t[q], m);
            if (!any_sent(m) || dead) break;
            if (poll_fail(spins, ctl, 128)) dead = true;
            asm volatile("" ::: "memory");
        }
#pragma unroll
        for (int q = 0; q < 6; ++q) *(LAS v4u*)(img + q * 1024 + lane * 16) = t[q];
        asm volatile("s_waitcnt lgkmcnt(0)" ::: "memory");
    }
    QS(1);
    LAS unsigned char* kb = img + 2048 + (4 * g4 + (n16 >> 2)) * 16 + (lane & 1) * 8;
#pragma unroll
    for (int c = 0; c < 4; ++c) {
        const v2u ka = trrd(kb + c * 512), kc = trrd(kb + c * 512 + 256);
        const v4u kf = {ka.x, ka.y, kc.x, kc.y};
        sm0 = mfma32(kf, qf[0][c], sm0); sm1 = mfma32(kf, qf[1][c], sm1);
    }
    v4u b1[2]; v2u b2[2];
#pragma unroll
    for (int m = 0; m < 2; ++m) {
        float v[12];
#pragma unroll
        for (int e = 0; e < 4; ++e) {
            const bool diag = 4 * g4 + e <= n16;
            v[e] = rnd(rnd(m ? s10[e] : s00[e]) * SCALE); if (m == 0 && !diag) v[e] = -INFINITY;
            v[4 + e] = (m && diag) ? rnd(rnd(s11[e]) * SCALE) : -INFINITY;
            v[8 + e] = g4 < 2 ? rnd(rnd(m ? sm1[e] : sm0[e]) * SCALE) : -INFINITY;
        }
        float mx = v[0];
#pragma unroll
        for (int e = 1; e < 12; ++e) mx = fmaxf(mx, v[e]);
        mx = sw16max(mx); mx = sw32max(mx);
        const float ml = -mx * LOG2E; float l = 0.f;
#pragma unroll
        for (int e = 0; e < 12; ++e) { v[e] = __builtin_amdgcn_exp2f(fmaf(v[e], LOG2E, ml)); l += v[e]; }
        l = sw16sum(l, l); l = sw32sum(l, l);
        const float ri = 1.f / l;
        b1[m] = (v4u){pk2(v[0] * ri, v[1] * ri), pk2(v[2] * ri, v[3] * ri), pk2(v[4] * ri, v[5] * ri), pk2(v[6] * ri, v[7] * ri)};
        b2[m] = (v2u){pk2(v[8] * ri, v[9] * ri), pk2(v[10] * ri, v[11] * ri)};
    }
#pragma unroll
    for (int c = 0; c < 4; ++c) {
        f32x4 o[2][2];
#pragma unroll
        for (int t = 0; t < 2; ++t) {
            const v2u va = *(LAS v2u*)(img + 4096 + ((2 * c + t) * 16 + n16) * 16 + (g4 & 1) * 8);
#pragma unroll
            for (int m = 0; m < 2; ++m) { o[m][t] = mfma32(vx[2 * c + t], b1[m], z); o[m][t] = mfma16(va, b2[m], o[m][t]); }
        }
#pragma unroll
        for (int m = 0; m < 2; ++m) st16(pk8(o[m][0], o[m][1]), rs, n16 * 16384 + 16 * g4, O_OX + fb(seg * 32 + m * 16, fpar) + (hh * 128 + c * 32) * 2);
    }
    QS(2);
    asm volatile("s_waitcnt vmcnt(0)" ::: "memory");
    QS(3);
    if (lane == 0) __hip_atomic_fetch_add(ctl + C_OF + 16 * (seg * 8 + (hh >> 2)), 1u, RLX_AGENT);
}

DEVI void top2(const float* R, int& i0, int& i1) {
    const int lane = otid() & 63;
    float b0 = -INFINITY, b1 = -INFINITY; int j0 = 0x7fffffff, j1 = 0x7fffffff;
    f32x4 rv[8];
#pragma unroll
    for (int q = 0; q < 8; ++q) rv[q] = *(const f32x4*)(R + lane * 32 + q * 4);
#pragma unroll
    for (int q = 0; q < 32; ++q) {
        const float v = rv[q >> 2][q & 3]; const int k = lane * 32 + q;
        const bool f0 = v > b0 || j0 == 0x7fffffff, f1 = v > b1 || j1 == 0x7fffffff;
        b1 = f0 ? b0 : (f1 ? v : b1); j1 = f0 ? j0 : (f1 ? k : j1); b0 = f0 ? v : b0; j0 = f0 ? k : j0;
    }
    for (int m = 1; m < 64; m <<= 1) {
        const float c0 = __shfl_xor(b0, m), c1 = __shfl_xor(b1, m); const int k0 = __shfl_xor(j0, m), k1 = __shfl_xor(j1, m);
        auto gt = [](float a, int ja, float b, int jb) { return a > b || (!(b > a) && ja < jb); };
        if (gt(c0, k0, b0, j0)) { if (gt(b0, j0, c1, k1)) { b1 = b0; j1 = j0; } else { b1 = c1; j1 = k1; } b0 = c0; j0 = k0; }
        else if (gt(c0, k0, b1, j1)) { b1 = c0; j1 = k0; }
    }
    i0 = __builtin_amdgcn_readfirstlane(j0); i1 = __builtin_amdgcn_readfirstlane(j1);
}
DEVI void ffn1_unit(const Params& p, LAS unsigned char* lds, int u, int i0, int i1) {
    const int tid = otid(), co = tid & 7, ksl = tid >> 3, col = u * 64 + co * 8;
    LAS float* red = (LAS float*)lds;
    v4u wg[16], wu[16];
#pragma unroll
    for (int q = 0; q < 16; ++q) { wg[q] = *(const v4u*)(p.wg + (size_t)(ksl * 16 + q) * F_ + col); wu[q] = *(const v4u*)(p.wu + (size_t)(ksl * 16 + q) * F_ + col); }
    v4u xa[2], xb[2];
#pragma unroll
    for (int q = 0; q < 2; ++q) { xa[q] = *(const v4u*)(p.x + i0 * D_ + ksl * 16 + q * 8); xb[q] = *(const v4u*)(p.x + i1 * D_ + ksl * 16 + q * 8); }
    float a[4][8] = {};
#pragma unroll
    for (int q = 0; q < 16; ++q) {
        const unsigned ua = xa[q >> 3][(q >> 1) & 3], ub = xb[q >> 3][(q >> 1) & 3];
        const float x0 = (q & 1) ? hi16(ua) : lo16(ua), x1 = (q & 1) ? hi16(ub) : lo16(ub);
#pragma unroll
        for (int e = 0; e < 4; ++e) {
            a[0][2 * e] += x0 * lo16(wg[q][e]); a[0][2 * e + 1] += x0 * hi16(wg[q][e]); a[1][2 * e] += x1 * lo16(wg[q][e]); a[1][2 * e + 1] += x1 * hi16(wg[q][e]);
            a[2][2 * e] += x0 * lo16(wu[q][e]); a[2][2 * e + 1] += x0 * hi16(wu[q][e]); a[3][2 * e] += x1 * lo16(wu[q][e]); a[3][2 * e + 1] += x1 * hi16(wu[q][e]);
        }
    }
#pragma unroll
    for (int q = 0; q < 4; ++q)
#pragma unroll
        for (int e = 0; e < 8; ++e) red[(ksl * 4 + q) * 64 + co * 8 + e] = a[q][e];
    BAR();
    if (tid < 128) {
        const int t = tid >> 6, c = tid & 63; float g = 0.f, uu = 0.f;
        for (int k = 0; k < 64; ++k) { g += red[(k * 4 + t) * 64 + c]; uu += red[(k * 4 + 2 + t) * 64 + c]; }
        g = rnd(g); uu = rnd(uu); g = rnd(g / (1.f + __expf(-g)));
        __hip_atomic_store((GAS float*)(p.ws + O_FH) + t * F_ + u * 64 + c, rnd(g * uu), RLX_AGENT);
    }
    announce((GAS unsigned*)(p.ws + O_CTL) + C_F1);
}
DEVI void ffn2_unit(const Params& p, LAS unsigned char* lds, int u, bool& dead) {
    const int tid = otid(), co = tid & 3, ksl = tid >> 2, col = u * 32 + co * 8;
    GAS unsigned* ctl = (GAS unsigned*)(p.ws + O_CTL);
    LAS float* red = (LAS float*)lds;
    v4u w[28];
#pragma unroll
    for (int q = 0; q < 28; ++q) w[q] = *(const v4u*)(p.wd + (size_t)(ksl * 28 + q) * D_ + col);
    wait_ge(ctl + C_F1, 56u, ctl, dead);
    const __amdgpu_buffer_rsrc_t rs = __builtin_amdgcn_make_buffer_rsrc(p.ws, 0, 0xFFFFFF00u, 0x00020000);
    v4u h0[7], h1[7];
#pragma unroll
    for (int q = 0; q < 7; ++q) { h0[q] = ld16(rs, ksl * 112 + q * 16, O_FH); h1[q] = ld16(rs, ksl * 112 + q * 16, O_FH + F_ * 4); }
    float a[2][8] = {};
#pragma unroll
    for (int q = 0; q < 28; ++q) {
        const float x0 = __uint_as_float(h0[q >> 2][q & 3]), x1 = __uint_as_float(h1[q >> 2][q & 3]);
#pragma unroll
        for (int e = 0; e < 4; ++e) { a[0][2 * e] += x0 * lo16(w[q][e]); a[0][2 * e + 1] += x0 * hi16(w[q][e]); a[1][2 * e] += x1 * lo16(w[q][e]); a[1][2 * e + 1] += x1 * hi16(w[q][e]); }
    }
#pragma unroll
    for (int q = 0; q < 2; ++q)
#pragma unroll
        for (int e = 0; e < 8; ++e) red[(ksl * 2 + q) * 32 + co * 8 + e] = a[q][e];
    BAR();
    if (tid < 64) {
        const int t = tid >> 5, c = tid & 31; float y = 0.f;
        for (int k = 0; k < 128; ++k) y += red[(k * 2 + t) * 32 + c];
        __hip_atomic_store((GAS float*)(p.ws + O_FY) + t * D_ + u * 32 + c, rnd(y), RLX_AGENT);
    }
    announce(ctl + C_F2);
}

DEVI void wo_fast_unit(const Params& p, LAS unsigned char* lds, int ct, int k8, int i0, int i1, unsigned fpar, bool& dead) {
    const int tid = otid(), lane = tid & 63, wave = __builtin_amdgcn_readfirstlane(tid >> 6), fr = lane & 15, fg = lane >> 4;
    GAS unsigned* ctl = (GAS unsigned*)(p.ws + O_CTL);
    const __amdgpu_buffer_rsrc_t rs = __builtin_amdgcn_make_buffer_rsrc(p.ws, 0, 0xFFFFFF00u, 0x00020000);
    {
        const unsigned lbase = (unsigned)(uintptr_t)lds + wave * 2048;
        const u16* gb = p.wo + (size_t)k8 * 512 * D_ + ct * 128;
#pragma unroll
        for (int q = 0; q < 2; ++q) {
            const int k = wave * 8 + (lane >> 4) + 4 * q;
            const int pk = (k & 32) + 16 * ((k >> 2) & 1) + 4 * ((k >> 3) & 3) + (k & 3), f = (k & 3) | (((k >> 3) & 1) << 2);
            const unsigned vb = (pk * D_ + ((((lane & 15) >> 1) ^ f) * 16 + (lane & 1) * 8)) * 2;
#pragma unroll
            for (int kt = 0; kt < 8; ++kt) glds(vb, gb + (size_t)kt * 64 * D_, lbase + kt * 16384 + q * 1024);
        }
    }
    wait_ge(ctl + C_INIT, 128u, ctl, dead);
    wait_ge(ctl + C_F2, 32u, ctl, dead);
    asm volatile("s_waitcnt vmcnt(0)\n\ts_barrier" ::: "memory");
    const int mt = wave & 1, np = wave >> 1;
    v4u bf[16][2];
    {
        const int q4 = fr >> 2, p4 = fr & 3, fsw = q4 | ((fg & 1) << 2);
        const unsigned bpo = (8 * fg + q4) * 256 + p4 * 8 + ((fsw ^ (2 * np)) * 32);
#pragma unroll
        for (int q = 0; q < 16; ++q)
#pragma unroll
            for (int n = 0; n < 2; ++n) { LAS unsigned char* bp = lds + (bpo ^ (n * 32)) + q * 8192; const v2u x = trrd(bp), y = trrd(bp + 1024); bf[q][n] = (v4u){x.x, x.y, y.x, y.y}; }
    }
    BAR();
    const int col = ct * 128 + k8 * 16 + fg * 4;
    v4u t[8]; float gv; v2u xv;
    auto request = [&](int sg) {
        const int row = sg * 32 + mt * 16 + fr;
#pragma unroll
        for (int q = 0; q < 8; ++q) t[q] = ld16(rs, lane * 16 + q * 1024, O_S2 + fb((((sg & 7) * 8 + ct) * 2 + mt) * 8 + k8, fpar));
        gv = ((const float*)(p.ws + O_G))[row];
        xv = *(const v2u*)(p.x + row * D_ + col);
    };
    auto finish = [&](int sg) {
        const int row = sg * 32 + mt * 16 + fr;
        for (unsigned spins = 0;;) {
            v4u an = t[0], orr = t[0];
#pragma unroll
            for (int q = 1; q < 8; ++q) { an &= t[q]; orr |= t[q]; }
            const unsigned bad = (sg & 8) ? ~(an.x & an.y & an.z & an.w) : (orr.x | orr.y | orr.z | orr.w);
            if (__builtin_amdgcn_ballot_w64((bad & 1u) != 0u) == 0 || dead) break;
            if (poll_fail(spins, ctl, 64)) dead = true;
            asm volatile("" ::: "memory");
            request(sg);
        }
        float bb[4] = {lo16(xv.x), hi16(xv.x), lo16(xv.y), hi16(xv.y)};
        if (row == i0 || row == i1) {
            const float rs_ = rnd(1.f / (1.f + __expf(-((const float*)(p.ws + O_R))[row])));
#pragma unroll
            for (int e = 0; e < 4; ++e) bb[e] = rnd(bb[e] + rnd(__hip_atomic_load((GAS float*)(p.ws + O_FY) + (row == i0 ? 0 : D_) + col + e, RLX_AGENT) * rs_));
        }
        f32x4 a = __builtin_bit_cast(f32x4, t[0]);
#pragma unroll
        for (int q = 1; q < 8; ++q) a += __builtin_bit_cast(f32x4, t[q]);
        const float og = rnd(1.f - gv);
        float o[4];
#pragma unroll
        for (int e = 0; e < 4; ++e) o[e] = rnd(gv * rnd(a[e])) + rnd(og * bb[e]);
        *(v2u*)(p.out + row * D_ + col) = (v2u){pk2(o[0], o[1]), pk2(o[2], o[3])};
    };
    int ready = SF0;
    const v4u rd = {(unsigned)(uintptr_t)p.ws, (unsigned)((uintptr_t)p.ws >> 32) & 0xffffu, 0xFFFFFF00u, 0x00020000u};
    v4u ar0, ar1, ar2, ar3;
    const unsigned va0 = lane * 16, va1 = va0 + 16384, va2 = va0 + 32768, va3 = va0 + 49152;
    auto aload = [&](int sg) {
        const unsigned so = O_OX + fb(sg * 32 + wave * 4, fpar) + k8 * 1024u;
        ALD16(ar0, va0, so, 0); ALD16(ar1, va1, so, 0); ALD16(ar2, va2, so, 0); ALD16(ar3, va3, so, 0);
    };
    LAS unsigned char* const aw = lds + wave * 4096;
    const unsigned sw0 = (lane ^ ((wave * 4) & 15)) * 16;
    for (int seg = SF0; seg < NSEG; ++seg) {
        const int rnd_ = seg - 56; (void)rnd_;
        if (seg >= ready) {
            if (wave == ((2 * seg + 4) & 7)) {
                unsigned long long ok;
                for (unsigned spins = 0;;) {
                    const unsigned c = seg + lane < NSEG ? __hip_atomic_load(ctl + C_OF + 16 * ((seg + lane - SF0) * 8 + k8), RLX_AGENT) : 0u;
                    ok = __builtin_amdgcn_ballot_w64(c >= 4u);
                    if ((ok & 1ull) || dead) break;
                    if (poll_fail(spins, ctl, 64)) dead = true;
                }
                if (lane == 0) *(LAS int*)(lds + 65536) = seg + max(1, (int)__builtin_ctzll(~ok));
            }
            BAR();
            ready = __builtin_amdgcn_readfirstlane(*(LAS int*)(lds + 65536));
            aload(seg);
            asm volatile("s_waitcnt vmcnt(0)" ::: "memory");
        }
        WAITV2(2, ar0, ar1); WAITV2(2, ar2, ar3);
        FS(0);
        LAS unsigned char* img = lds + (seg & 1) * 32768;
        { LAS unsigned char* w_ = aw + (seg & 1) * 32768;
          *(LAS v4u*)(w_ + sw0) = ar0; *(LAS v4u*)(w_ + 1024 + (sw0 ^ 16)) = ar1; *(LAS v4u*)(w_ + 2048 + (sw0 ^ 32)) = ar2; *(LAS v4u*)(w_ + 3072 + (sw0 ^ 48)) = ar3; }
        asm volatile("s_waitcnt lgkmcnt(0)" ::: "memory");
        SB();
        if (seg >= 3 && np == ((seg - 3) & 3)) finish(seg - 3);
        if (seg >= 2 && np == ((seg - 2) & 3)) request(seg - 2);
        if (seg + 1 < ready) aload(seg + 1);
        BAR();
        f32x4 c0 = {0, 0, 0, 0}, c1 = c0;
        {
            v4u af[2][4];
            LAS unsigned char* ap = img + (mt * 16 + fr) * 1024;
#pragma unroll
            for (int q = 0; q < 4; ++q) af[0][q] = *(LAS v4u*)(ap + (((q * 4 + fg) ^ fr) * 16));
#pragma unroll
            for (int g = 0; g < 4; ++g) {
                if (g < 3) {
#pragma unroll
                    for (int q = 0; q < 4; ++q) af[(g + 1) & 1][q] = *(LAS v4u*)(ap + (g + 1) * 256 + (((q * 4 + fg) ^ fr) * 16));
                }
                SB();
#pragma unroll
                for (int q = 0; q < 4; ++q) { c0 = mfma32(bf[g * 4 + q][0], af[g & 1][q], c0); c1 = mfma32(bf[g * 4 + q][1], af[g & 1][q], c1); }
                SB();
            }
        }
        FS(1);
        const unsigned sb2 = O_S2 + fb((((seg & 7) * 8 + ct) * 2 + mt) * 8 + 2 * np, fpar) + k8 * 1024u;
        const unsigned tg = (seg >> 3) & 1u;
        st16((__builtin_bit_cast(v4u, c0) & ~1u) | tg, rs, lane * 16, sb2);
        st16((__builtin_bit_cast(v4u, c1) & ~1u) | tg, rs, lane * 16, sb2 + 16384u);
    }
    if (np == 1) finish(NSEG - 3);
    if (np >= 2) { const int rnd_ = 4 + np; (void)rnd_; if (np == 3) __builtin_amdgcn_s_sleep(5); request(NSEG - 4 + np); finish(NSEG - 4 + np); FS(2); }
}

constexpr int Q_K2 = 16, Q_F1 = Q_K2 + 16, Q_QA = Q_F1 + 14, Q_F2 = Q_QA + 16, Q_WF = Q_F2 + 8, Q_Q2 = Q_WF + 16, Q_END = Q_Q2 + 48;
DEVI void worker_body(const Params& p, LAS unsigned char* lds, int w) {
    const int tid = otid(), lane = tid & 63;
    GAS unsigned* ctl = (GAS unsigned*)(p.ws + O_CTL);
    const unsigned sidemask = side_mask(ctl, lane);
    bool dead = false;
    const unsigned fpar = far_par(sidemask);
    const int xq = w >> 5, hm = xq * 16 + ((w & 31) >> 1);
    kv_unit<64, 128>(p, lds, (w & 1) * 64, hm >> 1, hm & 1, fpar);
    EV(9);
    kv_unit<128, 128>(p, lds, 128 + (w & 1) * 128, hm >> 1, hm & 1, fpar);
    EV(10);
    { const int hp = (w >> 5) * 8 + ((w & 31) >> 2); kv_unit<256, 256>(p, lds, 384 + (w & 3) * 256, (hp >> 1) * 2, hp & 1, fpar); }
    EV(11);
    {
        const __amdgpu_buffer_rsrc_t rs = __builtin_amdgcn_make_buffer_rsrc(p.ws, 0, 0xFFFFFF00u, 0x00020000);
        const v4u zz = {SENT, SENT, SENT, SENT};
#pragma unroll
        for (int q = 0; q < 8; ++q) st16(zz, rs, otid() * 16, O_S2 + fb(w * 8 + q, fpar));
        announce(ctl + C_INIT);
    }
    int i0, i1; top2((const float*)(p.ws + O_R), i0, i1);
    for (;;) {
        BAR();
        if (tid == 0) *(LAS unsigned*)(lds + 147456) = __hip_atomic_fetch_add(ctl + C_QUEUE + 16 * xq, 1u, RLX_AGENT);
        BAR();
        int t = __builtin_amdgcn_readfirstlane(*(LAS unsigned*)(lds + 147456));
        if (t >= Q_END) break;
        EV(1000 + t);
        if (t < Q_K2) kv_unit<256, 256>(p, lds, 1408 + (t & 1) * 256, xq * 8 + (t >> 2) * 2, (t >> 1) & 1, fpar);
        else if (t < Q_F1) kv_unit<128, 128>(p, lds, 1920, xq * 8 + ((t - Q_K2) >> 1), t & 1, fpar);
        else if (t < Q_QA) ffn1_unit(p, lds, xq * 14 + t - Q_F1, i0, i1);
        else if (t >= Q_F2 && t < Q_WF) ffn2_unit(p, lds, xq * 8 + t - Q_F2, dead);
        else if (t >= Q_WF && t < Q_Q2) wo_fast_unit(p, lds, (t - Q_WF) >> 1, 2 * xq + ((t - Q_WF) & 1), i0, i1, fpar, dead);
        else { const int qa = t < Q_F2 ? t - Q_QA : t - Q_Q2 + 16; qa_unit(p, lds, qa >> 3, xq * 8 + (qa & 7), sidemask, dead); }
    }
}

__global__ void __launch_bounds__(512, 2) k_main(Params p, Tim tm) {
    extern __shared__ __attribute__((aligned(16))) unsigned char smem[];
    LAS unsigned char* lds = (LAS unsigned char*)smem;
    if (threadIdx.x == 0) { *(LAS unsigned*)(lds + 147460) = rt(); *(LAS int*)(lds + 147456) = claim_slot((GAS unsigned*)(p.ws + O_CTL)); }
    __syncthreads();
    const int slot = __builtin_amdgcn_readfirstlane(*(LAS int*)(lds + 147456));
    EV(1);
    if (slot < 128) chain_body(p, tm, lds, slot >> 5, slot & 31, __builtin_amdgcn_readfirstlane(*(LAS unsigned*)(lds + 147460)));
    else worker_body(p, lds, slot - 128);
    EV(99);
}

#define TIM_INIT {700, 850, 750, 120}
extern "C" void kernel_launch(void* const* d_in, const int* in_sizes, int n_in, void* d_out, int out_size, void* d_ws, size_t ws_size, hipStream_t stream) {
    Params p;
    p.x = (const u16*)d_in[0]; p.mem0 = (const u16*)d_in[1]; p.wq = (const u16*)d_in[2]; p.wk = (const u16*)d_in[3]; p.wv = (const u16*)d_in[4]; p.wo = (const u16*)d_in[5];
    p.wr = (const u16*)d_in[8]; p.wg = (const u16*)d_in[9]; p.wu = (const u16*)d_in[10]; p.wd = (const u16*)d_in[11]; p.gw = (const u16*)d_in[12];
    p.out = (u16*)d_out;
    p.ws = (unsigned char*)(((uintptr_t)d_ws + PAGE - 1) & ~(uintptr_t)(PAGE - 1));
    Tim tm = TIM_INIT;
    constexpr int LDS_M = 147456 + 64;
    (void)hipFuncSetAttribute((const void*)k_main, hipFuncAttributeMaxDynamicSharedMemorySize, LDS_M);
    hipLaunchKernelGGL(k_prep, dim3(256), dim3(320), 0, stream, p);
    hipLaunchKernelGGL(k_main, dim3(256), dim3(512), LDS_M, stream, p, tm);
}
```

```cpp
#include <hip/hip_runtime.h>
#include <hip/hip_bf16.h>
#include <stdint.h>

typedef unsigned short u16;
typedef unsigned v4u __attribute__((ext_vector_type(4)));
typedef unsigned v2u __attribute__((ext_vector_type(2)));
typedef unsigned v8u __attribute__((ext_vector_type(8)));
typedef float f32x4 __attribute__((ext_vector_type(4)));
typedef __bf16 bf16x2 __attribute__((ext_vector_type(2)));
typedef __bf16 bf16x8 __attribute__((ext_vector_type(8)));
typedef __bf16 bf16x16 __attribute__((ext_vector_type(16)));
typedef short s16x4 __attribute__((ext_vector_type(4)));
#define GAS __attribute__((address_space(1)))
#define LAS __attribute__((address_space(3)))
#define RLX_AGENT __ATOMIC_RELAXED, __HIP_MEMORY_SCOPE_AGENT
#define DEVI __device__ __forceinline__
#define BAR() asm volatile("s_waitcnt lgkmcnt(0)\n\ts_barrier" ::: "memory")

constexpr int S_ = 2048, D_ = 1024, NSEG = 64, F_ = 3584;
constexpr float SCALE = 0.08838834764831845f, LOG2E = 1.4426950408889634f;
constexpr unsigned SENT = 0xFFFFFFFFu;
constexpr unsigned PAGE = 2u << 20;
constexpr unsigned O_CTL = 0, O_R = 65536, O_G = O_R + 8192, O_FH = 131072, O_FY = O_FH + 32768;
constexpr unsigned P_HAND = 1, P_PART = 17;
constexpr unsigned O_KX = 22 * PAGE, O_VX = 38 * PAGE, O_OX = 54 * PAGE, O_S2 = 70 * PAGE  ;
constexpr unsigned L_QKV = 65536;
constexpr int C_ABORT = 0, C_ARRIVE = 64, C_T0 = 128, C_QUEUE = 192, C_XR = 256  , C_F1 = 768, C_F2 = 800, C_INIT = 832, C_SLOT = 1024  ,
              C_KV = 2048  , C_PROG = 6144  , C_OF = 7168  , C_ZEND = 15360,
              C_SIDE = 15360  ;

struct Params { const u16 *x, *mem0, *wq, *wk, *wv, *wo, *wr, *wg, *wu, *wd, *gw; u16* out; unsigned char* ws; };
constexpr int DQ = 700, D3 = 850, D1 = 750, T0_LEAD = 120;

DEVI float bf2f(u16 b) { return __uint_as_float((unsigned)b << 16); }
DEVI unsigned pk2(float lo, float hi) { bf16x2 v = {(__bf16)lo, (__bf16)hi}; return __builtin_bit_cast(unsigned, v); }
DEVI float rnd(float f) { return __uint_as_float(pk2(f, 0.f) << 16); }
DEVI float lo16(unsigned u) { return __uint_as_float(u << 16); }
DEVI float hi16(unsigned u) { return __uint_as_float(u & 0xffff0000u); }
DEVI int otid() { int t = threadIdx.x; asm volatile("" : "+v"(t)); return t; }
DEVI unsigned xcc_id() { return (unsigned)__builtin_amdgcn_s_getreg(20 | (3 << 11)) & 0xfu; }
DEVI unsigned rt() { return (unsigned)__builtin_amdgcn_s_memrealtime(); }
DEVI unsigned amap(unsigned lo) { return ((lo >> 13) << 14) + (lo & 8191); }
DEVI unsigned fb(unsigned blk, unsigned par) { return blk * 16384u + (((par ^ (blk >> 7)) & 1u) << 13); }
DEVI unsigned far_par(unsigned sidemask) { return (sidemask & 1u) ^ 1u; }
DEVI unsigned side_mask(GAS unsigned* ctl, int lane) {
    const v4u v = *(GAS v4u*)(ctl + C_SIDE + lane * 4);
    int n = 0;
#pragma unroll
    for (int e = 0; e < 4; ++e) n += __builtin_popcountll(__builtin_amdgcn_ballot_w64((v[e] & 1u) != 0u));
    return n > 128 ? 0x55555555u : 0xAAAAAAAAu;
}
DEVI v4u ld16(__amdgpu_buffer_rsrc_t r, unsigned vo, unsigned so) { return __builtin_amdgcn_raw_buffer_load_b128(r, vo, so, 16); }
DEVI void st16(v4u v, __amdgpu_buffer_rsrc_t r, unsigned vo, unsigned so) {
    __builtin_amdgcn_raw_buffer_store_b128(v, r, vo, so, 16);
    asm volatile("s_nop 1" :: "v"(v));
}
DEVI void st8(v2u v, __amdgpu_buffer_rsrc_t r, unsigned vo, unsigned so) { __builtin_amdgcn_raw_buffer_store_b64(v, r, vo, so, 16); }
DEVI void st4(unsigned v, __amdgpu_buffer_rsrc_t r, unsigned vo, unsigned so) { __builtin_amdgcn_raw_buffer_store_b32(v, r, vo, so, 16); }
DEVI unsigned umax3(unsigned a, unsigned b, unsigned c) { unsigned r; asm("v_max3_u32 %0, %1, %2, %3" : "=v"(r) : "v"(a), "v"(b), "v"(c)); return r; }
DEVI unsigned mx4(v4u v, unsigned m) { return umax3(umax3(v.x, v.y, m), v.z, v.w); }
DEVI bool any_sent(unsigned m) { return __builtin_amdgcn_ballot_w64(m == SENT) != 0; }
DEVI unsigned nosent(unsigned u) { return u < 0xFFFFFFFEu ? u : 0xFFFFFFFEu; }
DEVI f32x4 mfma32(v4u a, v4u b, f32x4 c) { return __builtin_amdgcn_mfma_f32_16x16x32_bf16(__builtin_bit_cast(bf16x8, a), __builtin_bit_cast(bf16x8, b), c, 0, 0, 0); }
DEVI f32x4 mfma16(v2u a, v2u b, f32x4 c) { return __builtin_amdgcn_mfma_f32_16x16x16bf16_1k(__builtin_bit_cast(s16x4, a), __builtin_bit_cast(s16x4, b), c, 0, 0, 0); }
DEVI f32x4 smf(v4u a, v8u b, f32x4 c, int idx) { return __builtin_amdgcn_smfmac_f32_16x16x64_bf16(__builtin_bit_cast(bf16x8, a), __builtin_bit_cast(bf16x16, b), c, idx, 0, 0); }
DEVI v2u trrd(LAS unsigned char* p) { return __builtin_bit_cast(v2u, __builtin_amdgcn_ds_read_tr16_b64_v4i16((LAS s16x4*)p)); }
DEVI float ror8(float v) { return __int_as_float(__builtin_amdgcn_update_dpp(0, __float_as_int(v), 0x128, 0xf, 0xf, false)); }
DEVI float sw32sum(float a, float b) { auto r = __builtin_amdgcn_permlane32_swap(__float_as_uint(a), __float_as_uint(b), false, false); return __uint_as_float(r[0]) + __uint_as_float(r[1]); }
DEVI float sw16sum(float a, float b) { auto r = __builtin_amdgcn_permlane16_swap(__float_as_uint(a), __float_as_uint(b), false, false); return __uint_as_float(r[0]) + __uint_as_float(r[1]); }
DEVI float sw32max(float a) { auto r = __builtin_amdgcn_permlane32_swap(__float_as_uint(a), __float_as_uint(a), false, false); return fmaxf(__uint_as_float(r[0]), __uint_as_float(r[1])); }
DEVI float sw16max(float a) { auto r = __builtin_amdgcn_permlane16_swap(__float_as_uint(a), __float_as_uint(a), false, false); return fmaxf(__uint_as_float(r[0]), __uint_as_float(r[1])); }
DEVI f32x4 fold(f32x4 a, f32x4 b) { f32x4 r; r.x = sw32sum(a.x, b.x); r.y = sw32sum(a.y, b.y); r.z = sw32sum(a.z, b.z); r.w = sw32sum(a.w, b.w); return r; }
DEVI float max5x8(float a, float b, float c, float d, float e) {
    float m, t;
    asm("v_max3_f32 %0, %2, %3, %4\n\tv_max3_f32 %0, %0, %5, %6\n\ts_nop 1\n\t"
        "v_max_f32_dpp %0, %0, %0 row_ror:8 row_mask:0xf bank_mask:0xf\n\tv_mov_b32 %1, %0\n\ts_nop 1\n\t"
        "v_permlane16_swap_b32 %0, %1\n\tv_max_f32 %0, %0, %1\n\tv_mov_b32 %1, %0\n\ts_nop 1\n\t"
        "v_permlane32_swap_b32 %0, %1\n\tv_max_f32 %0, %0, %1"
        : "=&v"(m), "=&v"(t) : "v"(a), "v"(b), "v"(c), "v"(d), "v"(e));
    return m;
}
#define ALD16(D, VO, SO, IMM) asm volatile("buffer_load_dwordx4 %0, %1, %2, %3 offen offset:" #IMM " sc1" : "=v"(D) : "v"(VO), "s"(rd), "s"(SO) : "memory")
#define ALD4(D, VO, SO, IMM) asm volatile("buffer_load_dword %0, %1, %2, %3 offen offset:" #IMM " sc1" : "=v"(D) : "v"(VO), "s"(rd), "s"(SO) : "memory")
#define WAITV1(N, A) asm volatile("s_waitcnt vmcnt(" #N ")" : "+v"(A) :: "memory")
#define WAITV2(N, A, B) asm volatile("s_waitcnt vmcnt(" #N ")" : "+v"(A), "+v"(B) :: "memory")
#define SB() __builtin_amdgcn_sched_barrier(0)
#define PIN2(a, b) asm volatile("" : "+v"(a), "+v"(b))
DEVI unsigned mt() { return (unsigned)__builtin_amdgcn_s_memtime(); }
DEVI void wait_l(unsigned t) {
    unsigned d;
    asm volatile("1:\n\ts_memrealtime vcc\n\ts_waitcnt lgkmcnt(0)\n\ts_sub_i32 %0, vcc_lo, %1\n\ts_cmp_lt_i32 %0, 0\n\ts_cbranch_scc1 1b"
                 : "=&s"(d) : "s"(__builtin_amdgcn_readfirstlane(t)) : "memory", "scc", "vcc");
}
DEVI void wait_c(unsigned t) {
    unsigned d;
    asm volatile("1:\n\ts_memtime vcc\n\ts_waitcnt lgkmcnt(0)\n\ts_sub_i32 %0, vcc_lo, %1\n\ts_cmp_lt_i32 %0, 0\n\ts_cbranch_scc1 1b"
                 : "=&s"(d) : "s"(__builtin_amdgcn_readfirstlane(t)) : "memory", "scc", "vcc");
}
constexpr unsigned SPINCAP = 40000;
DEVI bool poll_fail(unsigned& spins, GAS unsigned* ctl, int back) {
    if (back >= 128) __builtin_amdgcn_s_sleep(2); else if (back) __builtin_amdgcn_s_sleep(1);
    ++spins;
    if ((spins & 63u) == 0u) {
        if (spins > SPINCAP) { __hip_atomic_store(ctl + C_ABORT, 1u, RLX_AGENT); return true; }
        if (__builtin_amdgcn_readfirstlane(__hip_atomic_load(ctl + C_ABORT, RLX_AGENT))) return true;
    }
    return false;
}

DEVI void wait_ge(GAS unsigned* w, unsigned v, GAS unsigned* ctl, bool& dead) {
    unsigned spins = 0;
    while (!dead && (unsigned)__builtin_amdgcn_readfirstlane(__hip_atomic_load(w, RLX_AGENT)) < v) { if (poll_fail(spins, ctl, 64)) dead = true; }
    asm volatile("" ::: "memory");
}

__global__ void __launch_bounds__(320) k_prep(Params p) {
    const int tid = threadIdx.x, lane = tid & 63, wave = tid >> 6, b = blockIdx.x;
    unsigned* ctl = (unsigned*)(p.ws + O_CTL);
    if (b >= 196 && tid < 256) ctl[(b - 196) * 256 + tid] = 0u;
    if (tid == 256) {
        const unsigned page = 1 + (b >> 3) % 20;
        __amdgpu_buffer_rsrc_t rs = __builtin_amdgcn_make_buffer_rsrc(p.ws, 0, 0xFFFFFF00u, 0x00020000);
        unsigned t[2] = {0xffffffffu, 0xffffffffu};
        for (int rep = 0; rep < 3; ++rep)
            for (int side = 0; side < 2; ++side) {
                const unsigned long long c0 = __builtin_amdgcn_s_memtime();
                asm volatile("s_waitcnt lgkmcnt(0)" ::: "memory");
                st4(SENT, rs, page * PAGE + 480 * 1024 + side * 8192 + (b & 7) * 512 + (b >= 160 ? 256 : 0), 0);
                asm volatile("s_waitcnt vmcnt(0)" ::: "memory");
                const unsigned long long c1 = __builtin_amdgcn_s_memtime();
                t[side] = min(t[side], (unsigned)(c1 - c0));
            }
        ctl[C_SIDE + b] = (t[1] < t[0] ? 1u : 0u) ^ (xcc_id() >= 4 ? 1u : 0u) ^ (page & 1u);
    }
    if (wave == 4) return;
    float* R = (float*)(p.ws + O_R); float* G = (float*)(p.ws + O_G);
    v4u xv[2][2], wr[2], gw[2];
#pragma unroll
    for (int q = 0; q < 2; ++q) {
        const int k = q * 512 + lane * 8;
        xv[0][q] = *(const v4u*)(p.x + (b * 8 + wave * 2) * D_ + k); xv[1][q] = *(const v4u*)(p.x + (b * 8 + wave * 2 + 1) * D_ + k);
        wr[q] = *(const v4u*)(p.wr + k); gw[q] = *(const v4u*)(p.gw + k);
    }
    float a[2] = {0.f, 0.f}, g[2] = {0.f, 0.f};
#pragma unroll
    for (int rr = 0; rr < 2; ++rr)
#pragma unroll
        for (int q = 0; q < 2; ++q)
#pragma unroll
            for (int e = 0; e < 4; ++e) {
                const unsigned u = xv[rr][q][e];
                a[rr] = fmaf(lo16(u), lo16(wr[q][e]), a[rr]); a[rr] = fmaf(hi16(u), hi16(wr[q][e]), a[rr]);
                const unsigned pr = pk2(lo16(u) * lo16(gw[q][e]), hi16(u) * hi16(gw[q][e]));
                g[rr] += lo16(pr); g[rr] += hi16(pr);
            }
#pragma unroll
    for (int m = 32; m >= 1; m >>= 1) { a[0] += __shfl_xor(a[0], m); g[0] += __shfl_xor(g[0], m); a[1] += __shfl_xor(a[1], m); g[1] += __shfl_xor(g[1], m); }
    if (lane < 2) { const int row = b * 8 + wave * 2 + lane; R[row] = rnd(lane ? a[1] : a[0]); G[row] = rnd(1.f / (1.f + __expf(-rnd(lane ? g[1] : g[0])))); }
}

constexpr unsigned LC_PA = 0, LC_QK = 24576, LC_V = LC_QK + 4096, LC_O = LC_V + 2048, LC_MISC = 98304 + 16, LC_KV = 33792  , LC_END = LC_KV + 32768;

DEVI void chain_body(const Params& p, LAS unsigned char* lds, const int g, const int i) {
    const int tid = threadIdx.x, lane = tid & 63, wave = __builtin_amdgcn_readfirstlane(tid >> 6);
    const int h = g * 8 + (i >> 2), j = i & 3, cid = h * 4 + j;
    const int kw = (wave + i) & 7;
    GAS unsigned* ctl = (GAS unsigned*)(p.ws + O_CTL);
    const __amdgpu_buffer_rsrc_t rs = __builtin_amdgcn_make_buffer_rsrc(p.ws, 0, 0xFFFFFF00u, 0x00020000);
    const unsigned sidemask = side_mask(ctl, lane);
    v8u WA[6][2], WO[2][2];
    v4u sa[2][12], so[8];
    {
        const u16* wqk = (j < 2 ? p.wq : p.wk) + h * 128 + 64 * (j & 1) + (lane & 7) * 8;
        const u16* wvv = p.wv + h * 128 + 32 * j + (lane & 3) * 8;
#pragma unroll
        for (int ks = 0; ks < 2; ++ks) {
#pragma unroll
            for (int q = 0; q < 8; ++q) sa[ks][q] = *(const v4u*)(wqk + (size_t)(128 * kw + 64 * ks + 8 * q + (lane >> 3)) * 4096);
#pragma unroll
            for (int q = 0; q < 4; ++q) sa[ks][8 + q] = *(const v4u*)(wvv + (size_t)(128 * kw + 64 * ks + 16 * q + (lane >> 2)) * 4096);
        }
#pragma unroll
        for (int q = 0; q < 8; ++q) so[q] = *(const v4u*)(p.wo + (size_t)(h * 128 + 16 * wave + 2 * q + (lane >> 5)) * 1024 + 256 * j + (lane & 31) * 8);
    }
    SB();
    const int tau = lane >> 5, rh = (lane >> 4) & 1, n16 = lane & 15, c8 = (lane >> 3) & 1, g4 = lane >> 4;
    const unsigned o_gm = amap(g * 16384 + kw * 2048 + lane * 16);
    const unsigned qkvb = L_QKV + h * 6144;
    const int pcol = 256 * j + 32 * wave + 16 * tau + n16;
    const unsigned o_pp = amap((pcol >> 3) * 4096 + h * 128 + (rh * 8 + (pcol & 7)) * 8);
    const int sidx = c8 ? 0xEEEE : 0x4444;
    LAS unsigned char* const pw = lds + LC_PA + ((wave * 2 + rh) * 96 + 16 * tau + n16) * 16;
    LAS unsigned char* const ob = lds + LC_O + (lane & 7) * 320 + c8 * 16 + g4 * 32;
    const v4u rd = {(unsigned)(uintptr_t)p.ws, (unsigned)((uintptr_t)p.ws >> 32) & 0xffffu, 0xFFFFFF00u, 0x00020000u};
    const unsigned o_i1 = amap(qkvb + j * 1024 + lane * 16);
    const unsigned o_i2 = lane < 32 ? amap(qkvb + 4096 + j * 512 + lane * 16) : amap(((lane >> 3) & 3) * 16384 + cid * 128 + (lane & 7) * 16);
    {
        const v4u zz = {SENT, SENT, SENT, SENT};
        if (wave < 2) {
            const unsigned sb = P_HAND * PAGE + wave * 524288u, sx = ((sidemask >> P_HAND) & 1u) << 13;
            st16(zz, rs, o_i1 ^ sx, sb);
            v4u iv = zz;
            if (wave == 0 && lane >= 32) {
                iv = *(const v4u*)(p.mem0 + (lane & 7) * 1024 + cid * 8);
                iv = (v4u){nosent(iv.x), nosent(iv.y), nosent(iv.z), nosent(iv.w)};
            }
            st16(iv, rs, o_i2 ^ sx, sb);
        }
#pragma unroll
        for (int sl = 0; sl < 2; ++sl) st8((v2u){SENT, SENT}, rs, o_pp ^ (((sidemask >> (P_PART + sl)) & 1u) << 13), (P_PART + sl) * PAGE);
    }
    bool dead = false;
    const unsigned o_kv = (wave & 2 ? O_VX : O_KX) + (wave & 1) * 4096 + lane * 16, fpar = far_par(sidemask);
    v4u p0, p1, p2, p3;
    {
        LAS unsigned char* img = lds + wave * 12288;
        const int wr = lane >> 2;
        LAS unsigned char* iw = img + ((wr & 9) | ((wr >> 1) & 2) | ((wr << 1) & 4)) * 64 + (((lane & 3) ^ ((wr >> 2) & 2)) * 16);
        const int gB = lane >> 4, q4 = (lane & 15) >> 2, p4 = lane & 3;
        LAS unsigned char* ir = img + (16 * (gB >> 1) + 2 * (gB & 1) + (q4 & 1) + 8 * (q4 >> 1)) * 64 + p4 * 8;
        const unsigned hs = (q4 >> 1) * 32;
        LAS unsigned char* jw = img + (lane >> 3) * 128 + (lane & 1) * 16;
        const int jc = ((lane >> 1) & 3) ^ (lane >> 5);
        LAS unsigned char* jr = img + (16 * (gB >> 1) + 4 * (gB & 1) + (q4 & 1) + 8 * (q4 >> 1)) * 128 + p4 * 8;
        const int js = (gB & 1) | ((q4 >> 1) << 1);
        auto shuffleA = [&](int ks) {
#pragma unroll
            for (int q = 0; q < 8; ++q) *(LAS v4u*)(jw + q * 1024 + ((jc ^ ((q & 1) << 1)) * 32)) = sa[ks][q];
#pragma unroll
            for (int q = 0; q < 4; ++q) *(LAS v4u*)(iw + 8192 + q * 1024) = sa[ks][8 + q];
            asm volatile("s_waitcnt lgkmcnt(0)" ::: "memory");
#pragma unroll
            for (int t = 0; t < 6; ++t)
#pragma unroll
                for (int m = 0; m < 4; ++m) {
                    const v2u r = t < 4 ? trrd(jr + ((t ^ js) * 32) + (32 * (m >> 1) + 2 * (m & 1)) * 128) : trrd(ir + 8192 + (((t & 1) * 32) ^ hs) + (32 * (m >> 1) + 4 * (m & 1)) * 64);
                    WA[t][ks][2 * m] = r.x; WA[t][ks][2 * m + 1] = r.y;
                }
            asm volatile("s_waitcnt lgkmcnt(0)" ::: "memory");
        };
        shuffleA(0);
        SB();
        if (wave >= 4) wait_ge(ctl + C_KV + 8 * h, 4u, ctl, dead);
        asm volatile("s_waitcnt vmcnt(0)" ::: "memory");
        if (wave >= 4) { const unsigned so = fb(h, fpar); ALD16(p0, o_kv, so, 0); ALD16(p1, o_kv, so, 1024); ALD16(p2, o_kv, so, 2048); ALD16(p3, o_kv, so, 3072); }
        BAR();
        if (tid == 0) {
            if (__hip_atomic_fetch_add(ctl + C_ARRIVE, 1u, RLX_AGENT) == 127u) __hip_atomic_store(ctl + C_T0, ((rt() + T0_LEAD) & ~3u) | 0x80000000u, RLX_AGENT);
        }
        SB();
        shuffleA(1);
        BAR();
#pragma unroll
        for (int q = 0; q < 8; ++q) {
            const int f = (lane >> 5) | (((q >> 1) & 3) << 1);
            *(LAS v4u*)(lds + wave * 8192 + q * 1024 + (lane >> 5) * 512 + ((((lane >> 1) & 15) ^ f) * 32) + (lane & 1) * 16) = so[q];
        }
        BAR();
        const int fo = (q4 & 1) | (js << 1);
        LAS unsigned char* orp = lds + (16 * (gB >> 1) + 4 * (gB & 1) + (q4 & 1) + 8 * (q4 >> 1)) * 512 + p4 * 8;
#pragma unroll
        for (int t = 0; t < 2; ++t)
#pragma unroll
            for (int ks = 0; ks < 2; ++ks)
#pragma unroll
                for (int m = 0; m < 4; ++m) {
                    const v2u r = trrd(orp + (((2 * wave + t) ^ fo) * 32) + (64 * ks + 32 * (m >> 1) + 2 * (m & 1)) * 512);
                    WO[t][ks][2 * m] = r.x; WO[t][ks][2 * m + 1] = r.y;
                }
    }
#define HAND_ADDR(s) do { const unsigned pg = P_HAND + ((s) >> 2); sb = pg * PAGE + ((s) & 3) * 524288u; sx = ((sidemask >> pg) & 1u) << 13; } while (0)
#define RETRY(RELOAD, BAD)    do { unsigned spins = 0;                                                                    \
                while (!dead) { if (poll_fail(spins, ctl, 0)) { dead = true; break; } RELOAD; if (!(BAD)) break; } } while (0)
#define ZERO_A() f32x4 c0 = {0, 0, 0, 0}, c1 = c0, c2 = c0, c3 = c0, c4 = c0, c5 = c0; PIN2(c0, c1); PIN2(c2, c3); PIN2(c4, c5)
#define GATHER_MEM(EXTRA, NX) v4u a0, a1;                                                                                 \
            ALD16(a0, o_gmx, sb, 0); ALD16(a1, o_gmx, sb, 1024); EXTRA; WAITV2(NX, a0, a1);                               \
            if (__builtin_expect(any_sent(mx4(a1, mx4(a0, 0u))), 0))                                                      \
                RETRY(ALD16(a0, o_gmx, sb, 0); ALD16(a1, o_gmx, sb, 1024); WAITV2(0, a0, a1), any_sent(mx4(a1, mx4(a0, 0u))))
#define QK_MFMA() do {                                                                                                    \
            c0 = smf(a0, WA[0][0], c0, sidx); c1 = smf(a0, WA[1][0], c1, sidx); c0 = smf(a1, WA[0][1], c0, sidx); c1 = smf(a1, WA[1][1], c1, sidx); \
            SB();                                                                                                         \
            c2 = smf(a0, WA[2][0], c2, sidx); c3 = smf(a0, WA[3][0], c3, sidx); c2 = smf(a1, WA[2][1], c2, sidx); c3 = smf(a1, WA[3][1], c3, sidx); \
            SB(); *(LAS f32x4*)pw = fold(c0, c1); SB(); *(LAS f32x4*)(pw + 512) = fold(c2, c3); SB(); } while (0)
#define V_PART() do { c4 = smf(a0, WA[4][0], c4, sidx); c5 = smf(a0, WA[5][0], c5, sidx); c4 = smf(a1, WA[4][1], c4, sidx); c5 = smf(a1, WA[5][1], c5, sidx); \
            *(LAS f32x4*)(pw + 1024) = fold(c4, c5);                                                                      \
            if (lane == 0) __hip_atomic_fetch_add((LAS unsigned*)(lds + LC_MISC + 4), 1u, __ATOMIC_RELAXED, __HIP_MEMORY_SCOPE_WORKGROUP); } while (0)
#define ZERO_D() f32x4 d0 = {0, 0, 0, 0}, d1 = d0; const unsigned pp = P_PART + (s & 3), px = ((sidemask >> pp) & 1u) << 13; unsigned o_ppx = o_pp ^ px; PIN2(d0, d1); asm volatile("" : "+v"(o_ppx))
#define WO_STAGE() do {                                                                                                   \
            const v4u o0 = *(LAS v4u*)ob, o1 = *(LAS v4u*)(ob + 128); SB();                                               \
            d0 = smf(o0, WO[0][0], d0, sidx); d1 = smf(o0, WO[1][0], d1, sidx); d0 = smf(o1, WO[0][1], d0, sidx); d1 = smf(o1, WO[1][1], d1, sidx); \
            const f32x4 f = fold(d0, d1);                                                                                 \
            st8((v2u){nosent(pk2(f.x, f.y)), nosent(pk2(f.z, f.w))}, rs, o_ppx, pp * PAGE); } while (0)
#define REDUCE4(LP, OFF) do {                                                                                             \
            const f32x4 r0 = *(LAS f32x4*)(LP), r1 = *(LAS f32x4*)((LP) + 3072), r2 = *(LAS f32x4*)((LP) + 6144), r3 = *(LAS f32x4*)((LP) + 9216); \
            const f32x4 t = (r0 + r1) + (r2 + r3);                                                                        \
            st4(nosent(pk2(sw32sum(t.x, t.z), sw32sum(t.y, t.w))), rs, (OFF), sb); } while (0)
    unsigned sb, sx;
    if (wave < 4) {
        const unsigned o_gq = amap(qkvb + wave * 1024 + lane * 16);
        const unsigned o_gp = amap(cid * 4096 + lane * 16);
        const int crow = 4 * ((lane >> 2) & 1) + g4;
        v4u sel = {0u, 0u, 0u, 0u};
        sel[(lane & 7) >> 1] = (lane & 1) ? 0x3f800000u : 0x00003f80u;
        const unsigned o_pm = amap(c8 * 16384 + cid * 128 + crow * 16 + (lane & 3) * 4);
        const unsigned o_gv = amap(qkvb + 4096 + wave * 512 + n16 * 16 + (4 * (g4 & 1) + 2 * (g4 >> 1)) * 2);
        LAS unsigned char* const qb = lds + LC_QK + (4 * g4 + ((lane & 15) >> 2)) * 16 + (lane & 1) * 8;
        LAS unsigned char* const qw = lds + LC_QK + wave * 1024 + lane * 16;
        LAS unsigned char* const ow = lds + LC_O + (lane & 7) * 320 + (32 * wave + 16 * c8 + 4 * g4) * 2;
        if (tid == 0) *(LAS unsigned*)(lds + LC_MISC + 4) = 0u;
        BAR();
        if (wave == 0) {
            unsigned plan, spins = 0;
            for (;;) { plan = __builtin_amdgcn_readfirstlane(__hip_atomic_load(ctl + C_T0, RLX_AGENT)); if (plan) break; if (poll_fail(spins, ctl, 64)) { dead = true; break; } }
            { const unsigned now = rt(); plan = now + ((int)((plan - now) << 1) >> 1); }
            if (!dead) wait_l(plan);
        }
        v4u kx[8], vx[2];
        unsigned tq;
#define A_STAGE_A(S1)                                                                                                     \
            BAR();                                                                                  \
            GATHER_MEM({ LAS unsigned char* kvb = lds + LC_KV + ((S1) & 1) * 16384 + lane * 16;                           \
                _Pragma("unroll") for (int q = 0; q < 8; ++q) kx[q] = *(LAS v4u*)(kvb + q * 1024);                        \
                vx[0] = *(LAS v4u*)(kvb + 8192 + wave * 2048); vx[1] = *(LAS v4u*)(kvb + 8192 + wave * 2048 + 1024); SB(); }, 0); \
            QK_MFMA();                                                                                                    \
            BAR();                                                                                                 \
            tq = mt() + DQ;                                                                                          \
            V_PART()
        {
            HAND_ADDR(0);
            ZERO_A();
            const unsigned o_gmx = o_gm ^ sx;
            A_STAGE_A(0);
        }
        for (int s = 0; s < NSEG - 1; ++s) {
            __builtin_amdgcn_s_setprio(3);
            v4u gq;
            ZERO_D();
            unsigned o_gqx = o_gq ^ sx, o_gvx = o_gv ^ sx; PIN2(o_gqx, o_gvx);
            wait_c(tq);
            ALD16(gq, o_gqx, sb, 0); WAITV1(0, gq);
            *(LAS v4u*)qw = gq;
            SB();
            if (__builtin_expect(any_sent(mx4(gq, 0u)), 0)) { RETRY(ALD16(gq, o_gqx, sb, 0); WAITV1(0, gq), any_sent(mx4(gq, 0u))); *(LAS v4u*)qw = gq; }
            BAR();
            {
                unsigned va, vc;
                ALD4(va, o_gvx, sb, 0); ALD4(vc, o_gvx, sb, 256);
                v4u qf[4], kf[4];
#pragma unroll
                for (int c = 0; c < 4; ++c) { const v2u qa = trrd(qb + c * 512), qc = trrd(qb + c * 512 + 256); qf[c] = (v4u){qa.x, qa.y, qc.x, qc.y}; }
#pragma unroll
                for (int c = 0; c < 4; ++c) { const v2u ka = trrd(qb + c * 512 + 2048), kc = trrd(qb + c * 512 + 2304); kf[c] = (v4u){ka.x, ka.y, kc.x, kc.y}; }
                SB();
                f32x4 s0 = {0, 0, 0, 0}, s1 = s0, sm = s0;
#pragma unroll
                for (int c = 0; c < 4; ++c) { s0 = mfma32(kx[c], qf[c], s0); s1 = mfma32(kx[4 + c], qf[c], s1); sm = mfma32(kf[c], qf[c], sm); }
                SB();
                float x0 = c8 ? s1.x : s0.x, x1 = c8 ? s1.y : s0.y, x2 = c8 ? s1.z : s0.z, x3 = c8 ? s1.w : s0.w;
                float xm = tau ? (c8 ? sm.w : sm.z) : (c8 ? sm.y : sm.x);
                constexpr float CS = SCALE * LOG2E;
                const float ml = -CS * max5x8(x0, x1, x2, x3, xm);
                x0 = __builtin_amdgcn_exp2f(fmaf(x0, CS, ml)); x1 = __builtin_amdgcn_exp2f(fmaf(x1, CS, ml));
                x2 = __builtin_amdgcn_exp2f(fmaf(x2, CS, ml)); x3 = __builtin_amdgcn_exp2f(fmaf(x3, CS, ml));
                xm = __builtin_amdgcn_exp2f(fmaf(xm, CS, ml));
                const unsigned e01 = pk2(x0, x1), e23 = pk2(x2, x3), em = pk2(xm, xm);
                const unsigned f01 = __float_as_uint(ror8(__uint_as_float(e01))), f23 = __float_as_uint(ror8(__uint_as_float(e23)));
                const v4u b1 = {c8 ? f01 : e01, c8 ? f23 : e23, c8 ? e01 : f01, c8 ? e23 : f23};
                unsigned b2 = em & (c8 ? 0xffff0000u : 0x0000ffffu);
                b2 |= __float_as_uint(ror8(__uint_as_float(b2)));
                asm volatile("" : "+v"(b2));
                f32x4 o0 = {0, 0, 0, 0}, o1 = o0;
                o0 = mfma32(vx[0], b1, o0); o1 = mfma32(vx[1], b1, o1);
                SB();
                WAITV2(0, va, vc);
                if (__builtin_expect(any_sent(max(va, vc)), 0)) RETRY(ALD4(va, o_gvx, sb, 0); ALD4(vc, o_gvx, sb, 256); WAITV2(0, va, vc), any_sent(max(va, vc)));
                o0 = mfma16((v2u){va, 0u}, (v2u){b2, 0u}, o0); o1 = mfma16((v2u){vc, 0u}, (v2u){b2, 0u}, o1);
                SB();
                float l = ((x0 + x1) + (x2 + x3)) + xm;
                l += ror8(l); l = sw16sum(l, l); l = sw32sum(l, l);
                const float rinv = __builtin_amdgcn_rcpf(l);
                SB();
                float y[4];
#pragma unroll
                for (int e = 0; e < 4; ++e) y[e] = (c8 ? o1[e] : o0[e]) * rinv;
                *(LAS v2u*)ow = (v2u){pk2(y[0], y[1]), pk2(y[2], y[3])};
            }
            BAR();
            WO_STAGE();
            const unsigned t3 = mt() + D3;
            __builtin_amdgcn_s_setprio(0);
            HAND_ADDR(s + 1);
            ZERO_A();
            unsigned o_gmx = o_gm ^ sx; asm volatile("" : "+v"(o_gmx));
            if (__builtin_expect(wave == 0, 1)) {
                unsigned o_pmx = o_pm ^ sx, o_gpx = o_gp ^ px; PIN2(o_pmx, o_gpx);
                wait_c(t3);
                v4u r0, r1, r2, r3; f32x4 u; unsigned m;
#define C_LOAD() ALD16(r0, o_gpx, pp * PAGE, 0); ALD16(r1, o_gpx, pp * PAGE, 1024); ALD16(r2, o_gpx, pp * PAGE, 2048); ALD16(r3, o_gpx, pp * PAGE, 3072);  \
                WAITV1(3, r0); u = mfma32(sel, r0, (f32x4){0, 0, 0, 0}); m = mx4(r0, 0u); SB(); WAITV1(2, r1); u = mfma32(sel, r1, u); m = mx4(r1, m); SB(); \
                WAITV1(1, r2); u = mfma32(sel, r2, u); m = mx4(r2, m); SB(); WAITV1(0, r3); u = mfma32(sel, r3, u); m = mx4(r3, m)
                C_LOAD();
                if (__builtin_expect(any_sent(m), 0)) RETRY(C_LOAD(), any_sent(m));
                float z0 = tau ? u.z : u.x, z1 = tau ? u.w : u.y;
                z0 += ror8(z0); z1 += ror8(z1);
                { auto sw = __builtin_amdgcn_permlane16_swap(__float_as_uint(z0), __float_as_uint(z1), false, false); z0 = __uint_as_float(sw[0]); z1 = __uint_as_float(sw[1]); }
                const unsigned w2 = nosent(pk2(z0, z1));
                st4(w2, rs, o_pmx, sb); st4(w2, rs, o_pmx + 65536, sb);
                const unsigned plan = mt() + D1;
                wait_c(plan);
            }
            A_STAGE_A(s + 1);
        }
    } else {
        const int hw = wave - 4;
        LAS unsigned char* const l_qk = lds + LC_PA + tau * 12288 + (rh * 96 + 16 * hw + n16) * 16;
        const unsigned o_pqk = amap(qkvb + j * 1024 + (16 * hw + n16) * 16 + rh * 8 + tau * 4);
        LAS unsigned char* const l_v = lds + LC_PA + tau * 12288 + (rh * 96 + 64 + 16 * (hw & 1) + n16) * 16;
        const unsigned o_pv = amap(qkvb + 4096 + j * 512 + (16 * (hw & 1) + n16) * 16 + rh * 8 + tau * 4);
        LAS unsigned char* const l_kv = lds + LC_KV + hw * 4096 + lane * 16;
        WAITV2(0, p0, p1); WAITV2(0, p2, p3);
        BAR();
        *(LAS v4u*)l_kv = p0; *(LAS v4u*)(l_kv + 1024) = p1; *(LAS v4u*)(l_kv + 2048) = p2; *(LAS v4u*)(l_kv + 3072) = p3;
        __builtin_amdgcn_s_setprio(2);
        for (int s = -1; s < NSEG - 1; ++s) {
            if (s >= 0) {
                ZERO_D();
                BAR();
                __builtin_amdgcn_s_sleep(3);
                { const unsigned so = fb((s + 1) * 32 + h, fpar); ALD16(p0, o_kv, so, 0); ALD16(p1, o_kv, so, 1024); ALD16(p2, o_kv, so, 2048); ALD16(p3, o_kv, so, 3072); }
                {
                    const unsigned rp = P_PART + ((s + 2) & 3), rx = ((sidemask >> rp) & 1u) << 13;
                    st8((v2u){SENT, SENT}, rs, o_pp ^ rx, rp * PAGE); st8((v2u){SENT, SENT}, rs, (o_pp - 8 * 16384) ^ rx, rp * PAGE);
                    if (hw >= 2 && s + 2 < NSEG) {
                        const unsigned pg = P_HAND + ((s + 2) >> 2);
                        st16((v4u){SENT, SENT, SENT, SENT}, rs, (hw == 2 ? o_i1 : o_i2) ^ (((sidemask >> pg) & 1u) << 13), pg * PAGE + ((s + 2) & 3) * 524288u);
                    }
                }
                BAR();
                WO_STAGE();
                if (j == 0 && tid == 256) __hip_atomic_store(ctl + C_PROG + 32 * h, (unsigned)(s + 1), RLX_AGENT);
                if (((s + 2) & 3) == 0 && s + 2 < NSEG) wait_ge(ctl + C_KV + 8 * (((s + 2) >> 2) * 32 + h), 2u, ctl, dead);
                WAITV2(0, p0, p1); WAITV2(0, p2, p3);
                LAS unsigned char* kw = l_kv + ((s + 1) & 1) * 16384;
                *(LAS v4u*)kw = p0; *(LAS v4u*)(kw + 1024) = p1; *(LAS v4u*)(kw + 2048) = p2; *(LAS v4u*)(kw + 3072) = p3;
            }
            HAND_ADDR(s + 1);
            ZERO_A();
            unsigned o_pqkx = o_pqk ^ sx, o_gmx = o_gm ^ sx; PIN2(o_pqkx, o_gmx);
            BAR();
            GATHER_MEM(, 0);
            QK_MFMA();
            BAR();
            REDUCE4(l_qk, o_pqkx);
            SB();
            if (s == NSEG - 2 && j == 0 && tid == 256) __hip_atomic_store(ctl + C_PROG + 32 * h, (unsigned)NSEG, RLX_AGENT);
            V_PART();
            if (hw < 2) {
                while (__builtin_amdgcn_readfirstlane(*(volatile LAS unsigned*)(lds + LC_MISC + 4)) < 8u * (unsigned)(s + 2)) { }
                asm volatile("" ::: "memory");
                REDUCE4(l_v, o_pv ^ sx);
            }
        }
    }
}

DEVI int claim_slot(GAS unsigned* ctl) {
    const unsigned x = xcc_id() & 7u;
    const unsigned r = __hip_atomic_fetch_add(ctl + C_XR + 32 * x, 1u, RLX_AGENT);
    if (r < 32 && __hip_atomic_exchange(ctl + C_SLOT + x * 32 + r, 1u, RLX_AGENT) == 0u) return (int)(x * 32 + r);
    for (int k = 255;; k = (k + 255) & 255) if (__hip_atomic_exchange(ctl + C_SLOT + k, 1u, RLX_AGENT) == 0u) return k;
}

DEVI void glds(unsigned voff, const void* sbase, unsigned ldsaddr) {
    unsigned keep;
    asm volatile("s_mov_b32 %0, m0\n\ts_mov_b32 m0, %3\n\ts_nop 2\n\tglobal_load_lds_dwordx4 %1, %2\n\ts_mov_b32 m0, %0" : "=&s"(keep) : "v"(voff), "s"(sbase), "s"(ldsaddr) : "memory");
}
template <int BM, int BN, int WM, bool TR, bool PERMK>
struct Gemm {
    static constexpr int WN = 8 / WM, MT = BM / WM / 16, NT = BN / WN / 16, NA = BM / 64, NB = BN / 64, STAGE = (BM + BN) * 128, NS = 147456 / STAGE, LPR = BN / 8;
    f32x4 acc[MT][NT];
    DEVI void run(LAS unsigned char* lds, const u16* A, int lda, const u16* B, int ldb, int nk) {
        const int tid = otid(), lane = tid & 63, wave = __builtin_amdgcn_readfirstlane(tid >> 6);
        const int wm = wave / WN, wn = wave % WN;
        unsigned va[NA], vb[NB];
#pragma unroll
        for (int q = 0; q < NA; ++q) va[q] = ((wave * NA * 8 + q * 8 + (lane >> 3)) * lda + (((lane & 7) ^ ((lane >> 3) & 7)) * 8)) * 2;
#pragma unroll
        for (int q = 0; q < NB; ++q) {
            const int k = (wave * NB + q) * (64 / LPR) + lane / LPR, sl = lane % LPR;
            const int pk = PERMK ? ((k & 32) + 16 * ((k >> 2) & 1) + 4 * ((k >> 3) & 3) + (k & 3)) : k;
            const int f = (k & 3) | (((k >> 3) & 1) << 2);
            vb[q] = (pk * ldb + (((sl >> 1) ^ f) * 16 + (sl & 1) * 8)) * 2;
        }
        const unsigned lbase = (unsigned)(uintptr_t)lds;
        auto piece = [&](int kt, int buf, int q) {
            if (q < NA) glds(va[q], A + kt * 64, lbase + buf * STAGE + (wave * NA + q) * 1024);
            else glds(vb[q - NA], B + (size_t)kt * 64 * ldb, lbase + buf * STAGE + BM * 128 + (wave * NB + q - NA) * 1024);
        };
        const int fr = lane & 15, fg = lane >> 4;
        const unsigned ra = (wm * (BM / WM) + fr) * 128;
        const unsigned ra0 = ra + ((fg ^ (fr & 7)) * 16), ra1 = ra + (((4 + fg) ^ (fr & 7)) * 16);
        const int q4 = fr >> 2, p4 = fr & 3, fsw = q4 | ((fg & 1) << 2);
        const unsigned rb = BM * 128 + (8 * fg + q4) * (BN * 2) + p4 * 8 + (((wn * NT) ^ fsw) * 32);
        v4u a0[MT], b0[NT], a1[MT], b1[NT];
        auto frag = [&](LAS unsigned char* sp, int ks, v4u* af, v4u* bf, int r) {
            if (r < NT) { LAS unsigned char* q = sp + (rb ^ (r * 32)) + ks * (BN * 64); const v2u x = trrd(q), y = trrd(q + BN * 8); bf[r] = (v4u){x.x, x.y, y.x, y.y}; }
            else af[r - NT] = *(LAS v4u*)(sp + (ks ? ra1 : ra0) + (r - NT) * 2048);
        };
        constexpr int NM = MT * NT, NF = MT + NT, NP = NA + NB;
        auto half = [&](const v4u* af, const v4u* bf, LAS unsigned char* np, int nks, v4u* naf, v4u* nbf, bool rd, bool stg, int skt, int sbuf) {
#pragma unroll
            for (int i = 0; i < NM; ++i) {
                const int m = i / NT, n = i % NT;
                acc[m][n] = TR ? mfma32(bf[n], af[m], acc[m][n]) : mfma32(af[m], bf[n], acc[m][n]);
#pragma unroll
                for (int r = i * NF / NM; r < (i + 1) * NF / NM; ++r) if (rd) frag(np, nks, naf, nbf, r);
#pragma unroll
                for (int q = i * NP / NM; q < (i + 1) * NP / NM; ++q) if (stg) piece(skt, sbuf, q);
                SB();
            }
        };
#pragma unroll
        for (int m = 0; m < MT; ++m)
#pragma unroll
            for (int n = 0; n < NT; ++n) acc[m][n] = (f32x4){0, 0, 0, 0};
#pragma unroll
        for (int q = 0; q < NS * NP; ++q) piece(q / NP, q / NP, q % NP);
        asm volatile("s_waitcnt vmcnt(%0)\n\ts_barrier" :: "n"((NS - 1) * NP) : "memory");
#pragma unroll
        for (int r = 0; r < NF; ++r) frag(lds, 0, a0, b0, r);
        int buf = 0;
        for (int kt = 0; kt < nk - NS; ++kt) {
            const int nb = buf == NS - 1 ? 0 : buf + 1;
            half(a0, b0, lds + buf * STAGE, 1, a1, b1, true, false, 0, 0);
            asm volatile("s_waitcnt vmcnt(%0) lgkmcnt(0)\n\ts_barrier" :: "n"((NS - 2) * NP) : "memory");
            half(a1, b1, lds + nb * STAGE, 0, a0, b0, true, true, kt + NS, buf);
            buf = nb;
        }
#pragma unroll
        for (int t = NS; t >= 1; --t) {
            const int nb = buf == NS - 1 ? 0 : buf + 1;
            half(a0, b0, lds + buf * STAGE, 1, a1, b1, true, false, 0, 0);
            asm volatile("s_waitcnt vmcnt(%0) lgkmcnt(0)\n\ts_barrier" :: "n"(t >= 2 ? (t - 2) * NP : 0) : "memory");
            half(a1, b1, lds + nb * STAGE, 0, a0, b0, t > 1, false, 0, 0);
            buf = nb;
        }
    }
};
DEVI v4u pk8(f32x4 a, f32x4 b) { return (v4u){pk2(a.x, a.y), pk2(a.z, a.w), pk2(b.x, b.y), pk2(b.z, b.w)}; }
DEVI void announce(GAS unsigned* w) {
    asm volatile("s_waitcnt vmcnt(0) lgkmcnt(0)\n\ts_barrier" ::: "memory");
    if (otid() == 0) __hip_atomic_fetch_add(w, 1u, RLX_AGENT);
}

template <int BM, int BN>
DEVI void kv_unit(const Params& p, LAS unsigned char* lds, int r0, int hh, int mat, unsigned fpar) {
    constexpr int WM = BM >= 128 ? 4 : 2, WN = 8 / WM, MT = BM / WM / 16, NT = BN / WN / 16;
    const int lane = otid() & 63, wave = __builtin_amdgcn_readfirstlane(otid() >> 6), wm = wave / WN, wn = wave % WN;
    const __amdgpu_buffer_rsrc_t rs = __builtin_amdgcn_make_buffer_rsrc(p.ws, 0, 0xFFFFFF00u, 0x00020000);
    const int row0 = r0 + wm * (BM / WM), hw = hh + wn * NT / 8, n0 = (wn * NT) & 7;
    if (mat == 0) {
        Gemm<BM, BN, WM, true, false> g; g.run(lds, p.x + (size_t)r0 * D_, D_, p.wk + hh * 128, 4096, 16);
#pragma unroll
        for (int m = 0; m < MT; ++m)
#pragma unroll
            for (int c = 0; c < NT / 2; ++c) {
                const int row = row0 + m * 16;
                st16(pk8(g.acc[m][2 * c], g.acc[m][2 * c + 1]), rs, lane * 16, O_KX + fb((row >> 5) * 32 + hw, fpar) + (((row >> 4) & 1) * 4 + n0 / 2 + c) * 1024);
            }
    } else {
        Gemm<BM, BN, WM, false, false> g; g.run(lds, p.x + (size_t)r0 * D_, D_, p.wv + hh * 128, 4096, 16);
#pragma unroll
        for (int m = 0; m < MT; m += 2)
#pragma unroll
            for (int n = 0; n < NT; ++n) {
                const int row = row0 + m * 16;
                st16(pk8(g.acc[m][n], g.acc[m + 1][n]), rs, lane * 16, O_VX + fb((row >> 5) * 32 + hw, fpar) + (n0 + n) * 1024);
            }
    }
    asm volatile("s_waitcnt vmcnt(0) lgkmcnt(0)\n\ts_barrier" ::: "memory");
    const int t = otid();
    if (t < (BM == 256 ? 2 : 1) * (BN / 128))
        __hip_atomic_fetch_add((GAS unsigned*)(p.ws + O_CTL) + C_KV + 8 * (((r0 >> 7) + t / (BN / 128)) * 32 + hh + t % (BN / 128)), 1u, RLX_AGENT);
}

DEVI void qa_unit(const Params& p, LAS unsigned char* lds, int panel, int hh, unsigned sidemask, bool& dead) {
    const int lane = otid() & 63, wave = __builtin_amdgcn_readfirstlane(otid() >> 6);
    const int g4 = lane >> 4, n16 = lane & 15;
    const __amdgpu_buffer_rsrc_t rs = __builtin_amdgcn_make_buffer_rsrc(p.ws, 0, 0xFFFFFF00u, 0x00020000);
    GAS unsigned* ctl = (GAS unsigned*)(p.ws + O_CTL);
    v4u qf[2][4];
    {
        Gemm<256, 128, 8, true, false> g; g.run(lds, p.x + (size_t)panel * 256 * D_, D_, p.wq + hh * 128, 4096, 16);
#pragma unroll
        for (int m = 0; m < 2; ++m)
#pragma unroll
            for (int c = 0; c < 4; ++c) qf[m][c] = pk8(g.acc[m][2 * c], g.acc[m][2 * c + 1]);
    }
    const int seg = panel * 8 + wave;
    const unsigned fpar = far_par(sidemask), kvo = fb(seg * 32 + hh, fpar);
    wait_ge(ctl + C_KV + 8 * ((seg >> 2) * 32 + hh), seg >> 2 ? 2u : 4u, ctl, dead);
    v4u kx[8], vx[8];
#pragma unroll
    for (int q = 0; q < 8; ++q) kx[q] = ld16(rs, lane * 16 + q * 1024, O_KX + kvo);
#pragma unroll
    for (int q = 0; q < 8; ++q) vx[q] = ld16(rs, lane * 16 + q * 1024, O_VX + kvo);
    f32x4 z = {0, 0, 0, 0};
    f32x4 s00 = z, s10 = z, s11 = z, sm0 = z, sm1 = z;
#pragma unroll
    for (int c = 0; c < 4; ++c) { s00 = mfma32(kx[c], qf[0][c], s00); s10 = mfma32(kx[c], qf[1][c], s10); s11 = mfma32(kx[4 + c], qf[1][c], s11); }
    LAS unsigned char* img = lds + wave * 6144;
    wait_ge(ctl + C_PROG + 32 * hh, (unsigned)(seg + 1), ctl, dead);
    {
        const unsigned pg = P_HAND + (seg >> 2), sb = pg * PAGE + (seg & 3) * 524288u, sx = ((sidemask >> pg) & 1u) << 13;
        v4u t[6];
        for (unsigned spins = 0;;) {
            unsigned m = 0;
#pragma unroll
            for (int q = 0; q < 6; ++q) t[q] = ld16(rs, amap(L_QKV + hh * 6144 + q * 1024 + lane * 16) ^ sx, sb);
#pragma unroll
            for (int q = 0; q < 6; ++q) m = mx4(t[q], m);
            if (!any_sent(m) || dead) break;
            if (poll_fail(spins, ctl, 128)) dead = true;
            asm volatile("" ::: "memory");
        }
#pragma unroll
        for (int q = 0; q < 6; ++q) *(LAS v4u*)(img + q * 1024 + lane * 16) = t[q];
        asm volatile("s_waitcnt lgkmcnt(0)" ::: "memory");
    }
    LAS unsigned char* kb = img + 2048 + (4 * g4 + (n16 >> 2)) * 16 + (lane & 1) * 8;
#pragma unroll
    for (int c = 0; c < 4; ++c) {
        const v2u ka = trrd(kb + c * 512), kc = trrd(kb + c * 512 + 256);
        const v4u kf = {ka.x, ka.y, kc.x, kc.y};
        sm0 = mfma32(kf, qf[0][c], sm0); sm1 = mfma32(kf, qf[1][c], sm1);
    }
    v4u b1[2]; v2u b2[2];
#pragma unroll
    for (int m = 0; m < 2; ++m) {
        float v[12];
#pragma unroll
        for (int e = 0; e < 4; ++e) {
            const bool diag = 4 * g4 + e <= n16;
            v[e] = rnd(rnd(m ? s10[e] : s00[e]) * SCALE); if (m == 0 && !diag) v[e] = -INFINITY;
            v[4 + e] = (m && diag) ? rnd(rnd(s11[e]) * SCALE) : -INFINITY;
            v[8 + e] = g4 < 2 ? rnd(rnd(m ? sm1[e] : sm0[e]) * SCALE) : -INFINITY;
        }
        float mx = v[0];
#pragma unroll
        for (int e = 1; e < 12; ++e) mx = fmaxf(mx, v[e]);
        mx = sw16max(mx); mx = sw32max(mx);
        const float ml = -mx * LOG2E; float l = 0.f;
#pragma unroll
        for (int e = 0; e < 12; ++e) { v[e] = __builtin_amdgcn_exp2f(fmaf(v[e], LOG2E, ml)); l += v[e]; }
        l = sw16sum(l, l); l = sw32sum(l, l);
        const float ri = 1.f / l;
        b1[m] = (v4u){pk2(v[0] * ri, v[1] * ri), pk2(v[2] * ri, v[3] * ri), pk2(v[4] * ri, v[5] * ri), pk2(v[6] * ri, v[7] * ri)};
        b2[m] = (v2u){pk2(v[8] * ri, v[9] * ri), pk2(v[10] * ri, v[11] * ri)};
    }
#pragma unroll
    for (int c = 0; c < 4; ++c) {
        f32x4 o[2][2];
#pragma unroll
        for (int t = 0; t < 2; ++t) {
            const v2u va = *(LAS v2u*)(img + 4096 + ((2 * c + t) * 16 + n16) * 16 + (g4 & 1) * 8);
#pragma unroll
            for (int m = 0; m < 2; ++m) { o[m][t] = mfma32(vx[2 * c + t], b1[m], z); o[m][t] = mfma16(va, b2[m], o[m][t]); }
        }
#pragma unroll
        for (int m = 0; m < 2; ++m) st16(pk8(o[m][0], o[m][1]), rs, n16 * 16384 + 16 * g4, O_OX + fb(seg * 32 + m * 16, fpar) + (hh * 128 + c * 32) * 2);
    }
    asm volatile("s_waitcnt vmcnt(0)" ::: "memory");
    if (lane == 0) __hip_atomic_fetch_add(ctl + C_OF + 16 * (seg * 8 + (hh >> 2)), 1u, RLX_AGENT);
}

DEVI void top2(const float* R, int& i0, int& i1) {
    const int lane = otid() & 63;
    float b0 = -INFINITY, b1 = -INFINITY; int j0 = 0x7fffffff, j1 = 0x7fffffff;
    f32x4 rv[8];
#pragma unroll
    for (int q = 0; q < 8; ++q) rv[q] = *(const f32x4*)(R + lane * 32 + q * 4);
#pragma unroll
    for (int q = 0; q < 32; ++q) {
        const float v = rv[q >> 2][q & 3]; const int k = lane * 32 + q;
        const bool f0 = v > b0 || j0 == 0x7fffffff, f1 = v > b1 || j1 == 0x7fffffff;
        b1 = f0 ? b0 : (f1 ? v : b1); j1 = f0 ? j0 : (f1 ? k : j1); b0 = f0 ? v : b0; j0 = f0 ? k : j0;
    }
    for (int m = 1; m < 64; m <<= 1) {
        const float c0 = __shfl_xor(b0, m), c1 = __shfl_xor(b1, m); const int k0 = __shfl_xor(j0, m), k1 = __shfl_xor(j1, m);
        auto gt = [](float a, int ja, float b, int jb) { return a > b || (!(b > a) && ja < jb); };
        if (gt(c0, k0, b0, j0)) { if (gt(b0, j0, c1, k1)) { b1 = b0; j1 = j0; } else { b1 = c1; j1 = k1; } b0 = c0; j0 = k0; }
        else if (gt(c0, k0, b1, j1)) { b1 = c0; j1 = k0; }
    }
    i0 = __builtin_amdgcn_readfirstlane(j0); i1 = __builtin_amdgcn_readfirstlane(j1);
}
DEVI void ffn1_unit(const Params& p, LAS unsigned char* lds, int u, int i0, int i1) {
    const int tid = otid(), co = tid & 7, ksl = tid >> 3, col = u * 64 + co * 8;
    LAS float* red = (LAS float*)lds;
    v4u xa[2], xb[2];
#pragma unroll
    for (int q = 0; q < 2; ++q) { xa[q] = *(const v4u*)(p.x + i0 * D_ + ksl * 16 + q * 8); xb[q] = *(const v4u*)(p.x + i1 * D_ + ksl * 16 + q * 8); }
    float a[4][8] = {};
#pragma unroll
    for (int bq = 0; bq < 16; bq += 16) {
        v4u wg[16], wu[16];
#pragma unroll
        for (int q = 0; q < 16; ++q) { wg[q] = *(const v4u*)(p.wg + (size_t)(ksl * 16 + bq + q) * F_ + col); wu[q] = *(const v4u*)(p.wu + (size_t)(ksl * 16 + bq + q) * F_ + col); }
#pragma unroll
        for (int q = 0; q < 16; ++q) {
            const unsigned ua = xa[(bq + q) >> 3][((bq + q) >> 1) & 3], ub = xb[(bq + q) >> 3][((bq + q) >> 1) & 3];
            const float x0 = ((bq + q) & 1) ? hi16(ua) : lo16(ua), x1 = ((bq + q) & 1) ? hi16(ub) : lo16(ub);
#pragma unroll
            for (int e = 0; e < 4; ++e) {
                a[0][2 * e] += x0 * lo16(wg[q][e]); a[0][2 * e + 1] += x0 * hi16(wg[q][e]); a[1][2 * e] += x1 * lo16(wg[q][e]); a[1][2 * e + 1] += x1 * hi16(wg[q][e]);
                a[2][2 * e] += x0 * lo16(wu[q][e]); a[2][2 * e + 1] += x0 * hi16(wu[q][e]); a[3][2 * e] += x1 * lo16(wu[q][e]); a[3][2 * e + 1] += x1 * hi16(wu[q][e]);
            }
        }
        asm volatile("" ::: "memory"); SB();
    }
#pragma unroll
    for (int q = 0; q < 4; ++q)
#pragma unroll
        for (int e = 0; e < 8; ++e) red[(ksl * 4 + q) * 64 + co * 8 + e] = a[q][e];
    BAR();
    if (tid < 128) {
        const int t = tid >> 6, c = tid & 63; float g = 0.f, uu = 0.f;
        for (int k = 0; k < 64; ++k) { g += red[(k * 4 + t) * 64 + c]; uu += red[(k * 4 + 2 + t) * 64 + c]; }
        g = rnd(g); uu = rnd(uu); g = rnd(g / (1.f + __expf(-g)));
        __hip_atomic_store((GAS float*)(p.ws + O_FH) + t * F_ + u * 64 + c, rnd(g * uu), RLX_AGENT);
    }
    announce((GAS unsigned*)(p.ws + O_CTL) + C_F1);
}
DEVI void ffn2_unit(const Params& p, LAS unsigned char* lds, int u, bool& dead) {
    const int tid = otid(), co = tid & 3, ksl = tid >> 2, col = u * 32 + co * 8;
    GAS unsigned* ctl = (GAS unsigned*)(p.ws + O_CTL);
    LAS float* red = (LAS float*)lds;
    v4u w[28];
#pragma unroll
    for (int q = 0; q < 28; ++q) w[q] = *(const v4u*)(p.wd + (size_t)(ksl * 28 + q) * D_ + col);
    wait_ge(ctl + C_F1, 56u, ctl, dead);
    const __amdgpu_buffer_rsrc_t rs = __builtin_amdgcn_make_buffer_rsrc(p.ws, 0, 0xFFFFFF00u, 0x00020000);
    v4u h0[7], h1[7];
#pragma unroll
    for (int q = 0; q < 7; ++q) { h0[q] = ld16(rs, ksl * 112 + q * 16, O_FH); h1[q] = ld16(rs, ksl * 112 + q * 16, O_FH + F_ * 4); }
    float a[2][8] = {};
#pragma unroll
    for (int q = 0; q < 28; ++q) {
        const float x0 = __uint_as_float(h0[q >> 2][q & 3]), x1 = __uint_as_float(h1[q >> 2][q & 3]);
#pragma unroll
        for (int e = 0; e < 4; ++e) { a[0][2 * e] += x0 * lo16(w[q][e]); a[0][2 * e + 1] += x0 * hi16(w[q][e]); a[1][2 * e] += x1 * lo16(w[q][e]); a[1][2 * e + 1] += x1 * hi16(w[q][e]); }
    }
#pragma unroll
    for (int q = 0; q < 2; ++q)
#pragma unroll
        for (int e = 0; e < 8; ++e) red[(ksl * 2 + q) * 32 + co * 8 + e] = a[q][e];
    BAR();
    if (tid < 64) {
        const int t = tid >> 5, c = tid & 31; float y = 0.f;
        for (int k = 0; k < 128; ++k) y += red[(k * 2 + t) * 32 + c];
        __hip_atomic_store((GAS float*)(p.ws + O_FY) + t * D_ + u * 32 + c, rnd(y), RLX_AGENT);
    }
    announce(ctl + C_F2);
}

DEVI void wo_fast_unit(const Params& p, LAS unsigned char* lds, int ct, int k8, int i0, int i1, unsigned fpar, bool& dead) {
    const int tid = otid(), lane = tid & 63, wave = __builtin_amdgcn_readfirstlane(tid >> 6), fr = lane & 15, fg = lane >> 4;
    GAS unsigned* ctl = (GAS unsigned*)(p.ws + O_CTL);
    const __amdgpu_buffer_rsrc_t rs = __builtin_amdgcn_make_buffer_rsrc(p.ws, 0, 0xFFFFFF00u, 0x00020000);
    {
        const unsigned lbase = (unsigned)(uintptr_t)lds + wave * 2048;
        const u16* gb = p.wo + (size_t)k8 * 512 * D_ + ct * 128;
#pragma unroll
        for (int q = 0; q < 2; ++q) {
            const int k = wave * 8 + (lane >> 4) + 4 * q;
            const int pk = (k & 32) + 16 * ((k >> 2) & 1) + 4 * ((k >> 3) & 3) + (k & 3), f = (k & 3) | (((k >> 3) & 1) << 2);
            const unsigned vb = (pk * D_ + ((((lane & 15) >> 1) ^ f) * 16 + (lane & 1) * 8)) * 2;
#pragma unroll
            for (int kt = 0; kt < 8; ++kt) glds(vb, gb + (size_t)kt * 64 * D_, lbase + kt * 16384 + q * 1024);
        }
    }
    wait_ge(ctl + C_INIT, 128u, ctl, dead);
    asm volatile("s_waitcnt vmcnt(0)\n\ts_barrier" ::: "memory");
    const int mt = wave & 1, np = wave >> 1;
    v4u bf[16][2];
    {
        const int q4 = fr >> 2, p4 = fr & 3, fsw = q4 | ((fg & 1) << 2);
        const unsigned bpo = (8 * fg + q4) * 256 + p4 * 8 + ((fsw ^ (2 * np)) * 32);
#pragma unroll
        for (int q = 0; q < 16; ++q)
#pragma unroll
            for (int n = 0; n < 2; ++n) { LAS unsigned char* bp = lds + (bpo ^ (n * 32)) + q * 8192; const v2u x = trrd(bp), y = trrd(bp + 1024); bf[q][n] = (v4u){x.x, x.y, y.x, y.y}; }
    }
    BAR();
    const int col = ct * 128 + k8 * 16 + fg * 4;
    v4u t[8]; float gv; v2u xv;
    auto request = [&](int sg) {
        const int row = sg * 32 + mt * 16 + fr;
#pragma unroll
        for (int q = 0; q < 8; ++q) t[q] = ld16(rs, lane * 16 + q * 1024, O_S2 + fb((((sg & 7) * 8 + ct) * 2 + mt) * 8 + k8, fpar));
        gv = ((const float*)(p.ws + O_G))[row];
        xv = *(const v2u*)(p.x + row * D_ + col);
    };
    auto finish = [&](int sg) {
        const int row = sg * 32 + mt * 16 + fr;
        for (unsigned spins = 0;;) {
            v4u an = t[0], orr = t[0];
#pragma unroll
            for (int q = 1; q < 8; ++q) { an &= t[q]; orr |= t[q]; }
            const unsigned bad = (sg & 8) ? ~(an.x & an.y & an.z & an.w) : (orr.x | orr.y | orr.z | orr.w);
            if (__builtin_amdgcn_ballot_w64((bad & 1u) != 0u) == 0 || dead) break;
            if (poll_fail(spins, ctl, 64)) dead = true;
            asm volatile("" ::: "memory");
            request(sg);
        }
        float bb[4] = {lo16(xv.x), hi16(xv.x), lo16(xv.y), hi16(xv.y)};
        if ((i0 >> 5) == sg || (i1 >> 5) == sg) wait_ge(ctl + C_F2, 32u, ctl, dead);
        if (row == i0 || row == i1) {
            const float rs_ = rnd(1.f / (1.f + __expf(-((const float*)(p.ws + O_R))[row])));
#pragma unroll
            for (int e = 0; e < 4; ++e) bb[e] = rnd(bb[e] + rnd(__hip_atomic_load((GAS float*)(p.ws + O_FY) + (row == i0 ? 0 : D_) + col + e, RLX_AGENT) * rs_));
        }
        f32x4 a = __builtin_bit_cast(f32x4, t[0]);
#pragma unroll
        for (int q = 1; q < 8; ++q) a += __builtin_bit_cast(f32x4, t[q]);
        const float og = rnd(1.f - gv);
        float o[4];
#pragma unroll
        for (int e = 0; e < 4; ++e) o[e] = rnd(gv * rnd(a[e])) + rnd(og * bb[e]);
        *(v2u*)(p.out + row * D_ + col) = (v2u){pk2(o[0], o[1]), pk2(o[2], o[3])};
    };
    int ready = 0;
    const v4u rd = {(unsigned)(uintptr_t)p.ws, (unsigned)((uintptr_t)p.ws >> 32) & 0xffffu, 0xFFFFFF00u, 0x00020000u};
    v4u ar0, ar1, ar2, ar3;
    const unsigned va0 = lane * 16, va1 = va0 + 16384, va2 = va0 + 32768, va3 = va0 + 49152;
    auto aload = [&](int sg) {
        const unsigned so = O_OX + fb(sg * 32 + wave * 4, fpar) + k8 * 1024u;
        ALD16(ar0, va0, so, 0); ALD16(ar1, va1, so, 0); ALD16(ar2, va2, so, 0); ALD16(ar3, va3, so, 0);
    };
    LAS unsigned char* const aw = lds + wave * 4096;
    const unsigned sw0 = (lane ^ ((wave * 4) & 15)) * 16;
    for (int seg = 0; seg < NSEG; ++seg) {
        if (seg >= ready) {
            if (wave == ((2 * seg + 4) & 7)) {
                unsigned long long ok;
                for (unsigned spins = 0;;) {
                    const unsigned c = seg + lane < NSEG ? __hip_atomic_load(ctl + C_OF + 16 * ((seg + lane) * 8 + k8), RLX_AGENT) : 0u;
                    ok = __builtin_amdgcn_ballot_w64(c >= 4u);
                    if ((ok & 1ull) || dead) break;
                    if (poll_fail(spins, ctl, 64)) dead = true;
                }
                if (lane == 0) *(LAS int*)(lds + 65536) = seg + max(1, (int)__builtin_ctzll(~ok));
            }
            BAR();
            ready = __builtin_amdgcn_readfirstlane(*(LAS int*)(lds + 65536));
            aload(seg);
            asm volatile("s_waitcnt vmcnt(0)" ::: "memory");
        }
        WAITV2(2, ar0, ar1); WAITV2(2, ar2, ar3);
        LAS unsigned char* img = lds + (seg & 1) * 32768;
        { LAS unsigned char* w_ = aw + (seg & 1) * 32768;
          *(LAS v4u*)(w_ + sw0) = ar0; *(LAS v4u*)(w_ + 1024 + (sw0 ^ 16)) = ar1; *(LAS v4u*)(w_ + 2048 + (sw0 ^ 32)) = ar2; *(LAS v4u*)(w_ + 3072 + (sw0 ^ 48)) = ar3; }
        asm volatile("s_waitcnt lgkmcnt(0)" ::: "memory");
        SB();
        if (seg >= 3 && np == ((seg - 3) & 3)) finish(seg - 3);
        if (seg >= 2 && np == ((seg - 2) & 3)) request(seg - 2);
        if (seg + 1 < ready) aload(seg + 1);
        BAR();
        f32x4 c0 = {0, 0, 0, 0}, c1 = c0;
        {
            v4u af[2][4];
            LAS unsigned char* ap = img + (mt * 16 + fr) * 1024;
#pragma unroll
            for (int q = 0; q < 4; ++q) af[0][q] = *(LAS v4u*)(ap + (((q * 4 + fg) ^ fr) * 16));
#pragma unroll
            for (int g = 0; g < 4; ++g) {
                if (g < 3) {
#pragma unroll
                    for (int q = 0; q < 4; ++q) af[(g + 1) & 1][q] = *(LAS v4u*)(ap + (g + 1) * 256 + (((q * 4 + fg) ^ fr) * 16));
                }
                SB();
#pragma unroll
                for (int q = 0; q < 4; ++q) { c0 = mfma32(bf[g * 4 + q][0], af[g & 1][q], c0); c1 = mfma32(bf[g * 4 + q][1], af[g & 1][q], c1); }
                SB();
            }
        }
        const unsigned sb2 = O_S2 + fb((((seg & 7) * 8 + ct) * 2 + mt) * 8 + 2 * np, fpar) + k8 * 1024u;
        const unsigned tg = (seg >> 3) & 1u;
        st16((__builtin_bit_cast(v4u, c0) & ~1u) | tg, rs, lane * 16, sb2);
        st16((__builtin_bit_cast(v4u, c1) & ~1u) | tg, rs, lane * 16, sb2 + 16384u);
    }
    if (np == 1) finish(NSEG - 3);
    if (np >= 2) { if (np == 3) __builtin_amdgcn_s_sleep(5); request(NSEG - 4 + np); finish(NSEG - 4 + np); }
}

constexpr int Q_F1 = 0, Q_F2 = Q_F1 + 14, Q_QA = Q_F2 + 8, Q_K2 = Q_QA + 16, Q_WF = Q_K2 + 16, Q_Q2 = Q_WF + 16, Q_END = Q_Q2 + 48;
DEVI void worker_body(const Params& p, LAS unsigned char* lds, int w) {
    const int tid = otid(), lane = tid & 63;
    GAS unsigned* ctl = (GAS unsigned*)(p.ws + O_CTL);
    const unsigned sidemask = side_mask(ctl, lane);
    bool dead = false;
    const unsigned fpar = far_par(sidemask);
    const int xq = w >> 5, hm = xq * 16 + ((w & 31) >> 1);
    kv_unit<64, 128>(p, lds, (w & 1) * 64, hm >> 1, hm & 1, fpar);
    kv_unit<128, 128>(p, lds, 128 + (w & 1) * 128, hm >> 1, hm & 1, fpar);
    kv_unit<256, 128>(p, lds, 384 + (w & 1) * 256, hm >> 1, hm & 1, fpar);
    { const int hp = xq * 8 + ((w & 31) >> 2); kv_unit<256, 256>(p, lds, 896 + (w & 3) * 256, (hp >> 1) * 2, hp & 1, fpar); }
    {
        const __amdgpu_buffer_rsrc_t rs = __builtin_amdgcn_make_buffer_rsrc(p.ws, 0, 0xFFFFFF00u, 0x00020000);
        const v4u zz = {SENT, SENT, SENT, SENT};
#pragma unroll
        for (int q = 0; q < 8; ++q) st16(zz, rs, otid() * 16, O_S2 + fb(w * 8 + q, fpar));
        announce(ctl + C_INIT);
    }
    int i0, i1; top2((const float*)(p.ws + O_R), i0, i1);
    for (;;) {
        BAR();
        if (tid == 0) *(LAS unsigned*)(lds + 147456) = __hip_atomic_fetch_add(ctl + C_QUEUE + 16 * xq, 1u, RLX_AGENT);
        BAR();
        int t = __builtin_amdgcn_readfirstlane(*(LAS unsigned*)(lds + 147456));
        if (t >= Q_END) break;
        if (t < Q_F2) ffn1_unit(p, lds, xq * 14 + t - Q_F1, i0, i1);
        else if (t < Q_QA) ffn2_unit(p, lds, xq * 8 + t - Q_F2, dead);
        else if (t >= Q_K2 && t < Q_WF) kv_unit<128, 128>(p, lds, 1920, xq * 8 + ((t - Q_K2) >> 1), t & 1, fpar);
        else if (t >= Q_WF && t < Q_Q2) wo_fast_unit(p, lds, (t - Q_WF) >> 1, 2 * xq + ((t - Q_WF) & 1), i0, i1, fpar, dead);
        else { const int qa = t < Q_K2 ? t - Q_QA : t - Q_Q2 + 16; qa_unit(p, lds, qa >> 3, xq * 8 + (qa & 7), sidemask, dead); }
    }
}

__global__ void __launch_bounds__(512, 2) k_main(Params p) {
    extern __shared__ __attribute__((aligned(16))) unsigned char smem[];
    LAS unsigned char* lds = (LAS unsigned char*)smem;
    if (threadIdx.x == 0) *(LAS int*)(lds + 147456) = claim_slot((GAS unsigned*)(p.ws + O_CTL));
    __syncthreads();
    const int slot = __builtin_amdgcn_readfirstlane(*(LAS int*)(lds + 147456));
    if (slot < 128) chain_body(p, lds, slot >> 5, slot & 31);
    else worker_body(p, lds, slot - 128);
}

extern "C" void kernel_launch(void* const* d_in, const int* in_sizes, int n_in, void* d_out, int out_size, void* d_ws, size_t ws_size, hipStream_t stream) {
    Params p;
    p.x = (const u16*)d_in[0]; p.mem0 = (const u16*)d_in[1]; p.wq = (const u16*)d_in[2]; p.wk = (const u16*)d_in[3]; p.wv = (const u16*)d_in[4]; p.wo = (const u16*)d_in[5];
    p.wr = (const u16*)d_in[8]; p.wg = (const u16*)d_in[9]; p.wu = (const u16*)d_in[10]; p.wd = (const u16*)d_in[11]; p.gw = (const u16*)d_in[12];
    p.out = (u16*)d_out;
    p.ws = (unsigned char*)(((uintptr_t)d_ws + PAGE - 1) & ~(uintptr_t)(PAGE - 1));
    constexpr int LDS_M = 147456 + 64;
    (void)hipFuncSetAttribute((const void*)k_main, hipFuncAttributeMaxDynamicSharedMemorySize, LDS_M);
    hipLaunchKernelGGL(k_prep, dim3(256), dim3(320), 0, stream, p);
    hipLaunchKernelGGL(k_main, dim3(256), dim3(512), LDS_M, stream, p);
}
```

```cpp
#include <hip/hip_runtime.h>
#include <hip/hip_bf16.h>
#include <stdint.h>

typedef unsigned short u16;
typedef unsigned v4u __attribute__((ext_vector_type(4)));
typedef unsigned v2u __attribute__((ext_vector_type(2)));
typedef unsigned v8u __attribute__((ext_vector_type(8)));
typedef float f32x4 __attribute__((ext_vector_type(4)));
typedef __bf16 bf16x2 __attribute__((ext_vector_type(2)));
typedef __bf16 bf16x8 __attribute__((ext_vector_type(8)));
typedef __bf16 bf16x16 __attribute__((ext_vector_type(16)));
typedef short s16x4 __attribute__((ext_vector_type(4)));
#define GAS __attribute__((address_space(1)))
#define LAS __attribute__((address_space(3)))
#define RLX_AGENT __ATOMIC_RELAXED, __HIP_MEMORY_SCOPE_AGENT
#define DEVI __device__ __forceinline__
#define BAR() asm volatile("s_waitcnt lgkmcnt(0)\n\ts_barrier" ::: "memory")

constexpr int S_ = 2048, D_ = 1024, NSEG = 64, F_ = 3584;
constexpr float SCALE = 0.08838834764831845f, LOG2E = 1.4426950408889634f;
constexpr unsigned SENT = 0xFFFFFFFFu;
constexpr unsigned PAGE = 2u << 20;
constexpr unsigned O_CTL = 0, O_R = 65536, O_G = O_R + 8192, O_FH = 131072, O_FY = O_FH + 32768;
constexpr unsigned P_HAND = 1, P_PART = 17;
constexpr unsigned O_KX = 22 * PAGE, O_VX = 38 * PAGE, O_OX = 54 * PAGE, O_S2 = 70 * PAGE  ;
constexpr unsigned L_QKV = 65536;
constexpr int C_ABORT = 0, C_ARRIVE = 64, C_T0 = 128, C_QUEUE = 192, C_XR = 256  , C_F1 = 768, C_F2 = 800, C_INIT = 832, C_SLOT = 1024  ,
              C_KV = 2048  , C_PROG = 6144  , C_OF = 7168  , C_ZEND = 15360,
              C_SIDE = 15360  ;

struct Params { const u16 *x, *mem0, *wq, *wk, *wv, *wo, *wr, *wg, *wu, *wd, *gw; u16* out; unsigned char* ws; };
constexpr int DQ = 700, D3 = 850, D1 = 750, T0_LEAD = 120;

DEVI float bf2f(u16 b) { return __uint_as_float((unsigned)b << 16); }
DEVI unsigned pk2(float lo, float hi) { bf16x2 v = {(__bf16)lo, (__bf16)hi}; return __builtin_bit_cast(unsigned, v); }
DEVI float rnd(float f) { return __uint_as_float(pk2(f, 0.f) << 16); }
DEVI float lo16(unsigned u) { return __uint_as_float(u << 16); }
DEVI float hi16(unsigned u) { return __uint_as_float(u & 0xffff0000u); }
DEVI int otid() { int t = threadIdx.x; asm volatile("" : "+v"(t)); return t; }
DEVI unsigned xcc_id() { return (unsigned)__builtin_amdgcn_s_getreg(20 | (3 << 11)) & 0xfu; }
DEVI unsigned rt() { return (unsigned)__builtin_amdgcn_s_memrealtime(); }
DEVI unsigned amap(unsigned lo) { return ((lo >> 13) << 14) + (lo & 8191); }
DEVI unsigned fb(unsigned blk, unsigned par) { return blk * 16384u + (((par ^ (blk >> 7)) & 1u) << 13); }
DEVI unsigned far_par(unsigned sidemask) { return (sidemask & 1u) ^ 1u; }
DEVI unsigned side_mask(GAS unsigned* ctl, int lane) {
    const v4u v = *(GAS v4u*)(ctl + C_SIDE + lane * 4);
    int n = 0;
#pragma unroll
    for (int e = 0; e < 4; ++e) n += __builtin_popcountll(__builtin_amdgcn_ballot_w64((v[e] & 1u) != 0u));
    return n > 128 ? 0x55555555u : 0xAAAAAAAAu;
}
DEVI v4u ld16(__amdgpu_buffer_rsrc_t r, unsigned vo, unsigned so) { return __builtin_amdgcn_raw_buffer_load_b128(r, vo, so, 16); }
DEVI void st16(v4u v, __amdgpu_buffer_rsrc_t r, unsigned vo, unsigned so) {
    __builtin_amdgcn_raw_buffer_store_b128(v, r, vo, so, 16);
    asm volatile("s_nop 1" :: "v"(v));
}
DEVI void st8(v2u v, __amdgpu_buffer_rsrc_t r, unsigned vo, unsigned so) { __builtin_amdgcn_raw_buffer_store_b64(v, r, vo, so, 16); }
DEVI void st4(unsigned v, __amdgpu_buffer_rsrc_t r, unsigned vo, unsigned so) { __builtin_amdgcn_raw_buffer_store_b32(v, r, vo, so, 16); }
DEVI unsigned umax3(unsigned a, unsigned b, unsigned c) { unsigned r; asm("v_max3_u32 %0, %1, %2, %3" : "=v"(r) : "v"(a), "v"(b), "v"(c)); return r; }
DEVI unsigned mx4(v4u v, unsigned m) { return umax3(umax3(v.x, v.y, m), v.z, v.w); }
DEVI bool any_sent(unsigned m) { return __builtin_amdgcn_ballot_w64(m == SENT) != 0; }
DEVI unsigned nosent(unsigned u) { return u < 0xFFFFFFFEu ? u : 0xFFFFFFFEu; }
DEVI f32x4 mfma32(v4u a, v4u b, f32x4 c) { return __builtin_amdgcn_mfma_f32_16x16x32_bf16(__builtin_bit_cast(bf16x8, a), __builtin_bit_cast(bf16x8, b), c, 0, 0, 0); }
DEVI f32x4 mfma16(v2u a, v2u b, f32x4 c) { return __builtin_amdgcn_mfma_f32_16x16x16bf16_1k(__builtin_bit_cast(s16x4, a), __builtin_bit_cast(s16x4, b), c, 0, 0, 0); }
DEVI f32x4 smf(v4u a, v8u b, f32x4 c, int idx) { return __builtin_amdgcn_smfmac_f32_16x16x64_bf16(__builtin_bit_cast(bf16x8, a), __builtin_bit_cast(bf16x16, b), c, idx, 0, 0); }
DEVI v2u trrd(LAS unsigned char* p) { return __builtin_bit_cast(v2u, __builtin_amdgcn_ds_read_tr16_b64_v4i16((LAS s16x4*)p)); }
DEVI float ror8(float v) { return __int_as_float(__builtin_amdgcn_update_dpp(0, __float_as_int(v), 0x128, 0xf, 0xf, false)); }
DEVI float sw32sum(float a, float b) { auto r = __builtin_amdgcn_permlane32_swap(__float_as_uint(a), __float_as_uint(b), false, false); return __uint_as_float(r[0]) + __uint_as_float(r[1]); }
DEVI float sw16sum(float a, float b) { auto r = __builtin_amdgcn_permlane16_swap(__float_as_uint(a), __float_as_uint(b), false, false); return __uint_as_float(r[0]) + __uint_as_float(r[1]); }
DEVI float sw32max(float a) { auto r = __builtin_amdgcn_permlane32_swap(__float_as_uint(a), __float_as_uint(a), false, false); return fmaxf(__uint_as_float(r[0]), __uint_as_float(r[1])); }
DEVI float sw16max(float a) { auto r = __builtin_amdgcn_permlane16_swap(__float_as_uint(a), __float_as_uint(a), false, false); return fmaxf(__uint_as_float(r[0]), __uint_as_float(r[1])); }
DEVI f32x4 fold(f32x4 a, f32x4 b) { f32x4 r; r.x = sw32sum(a.x, b.x); r.y = sw32sum(a.y, b.y); r.z = sw32sum(a.z, b.z); r.w = sw32sum(a.w, b.w); return r; }
DEVI float max5x8(float a, float b, float c, float d, float e) {
    float m, t;
    asm("v_max3_f32 %0, %2, %3, %4\n\tv_max3_f32 %0, %0, %5, %6\n\ts_nop 1\n\t"
        "v_max_f32_dpp %0, %0, %0 row_ror:8 row_mask:0xf bank_mask:0xf\n\tv_mov_b32 %1, %0\n\ts_nop 1\n\t"
        "v_permlane16_swap_b32 %0, %1\n\tv_max_f32 %0, %0, %1\n\tv_mov_b32 %1, %0\n\ts_nop 1\n\t"
        "v_permlane32_swap_b32 %0, %1\n\tv_max_f32 %0, %0, %1"
        : "=&v"(m), "=&v"(t) : "v"(a), "v"(b), "v"(c), "v"(d), "v"(e));
    return m;
}
#define ALD16(D, VO, SO, IMM) asm volatile("buffer_load_dwordx4 %0, %1, %2, %3 offen offset:" #IMM " sc1" : "=v"(D) : "v"(VO), "s"(rd), "s"(SO) : "memory")
#define ALD4(D, VO, SO, IMM) asm volatile("buffer_load_dword %0, %1, %2, %3 offen offset:" #IMM " sc1" : "=v"(D) : "v"(VO), "s"(rd), "s"(SO) : "memory")
#define WAITV1(N, A) asm volatile("s_waitcnt vmcnt(" #N ")" : "+v"(A) :: "memory")
#define WAITV2(N, A, B) asm volatile("s_waitcnt vmcnt(" #N ")" : "+v"(A), "+v"(B) :: "memory")
#define SB() __builtin_amdgcn_sched_barrier(0)
#define PIN2(a, b) asm volatile("" : "+v"(a), "+v"(b))
DEVI unsigned mt() { return (unsigned)__builtin_amdgcn_s_memtime(); }
DEVI void wait_l(unsigned t) {
    unsigned d;
    asm volatile("1:\n\ts_memrealtime vcc\n\ts_waitcnt lgkmcnt(0)\n\ts_sub_i32 %0, vcc_lo, %1\n\ts_cmp_lt_i32 %0, 0\n\ts_cbranch_scc1 1b"
                 : "=&s"(d) : "s"(__builtin_amdgcn_readfirstlane(t)) : "memory", "scc", "vcc");
}
DEVI void wait_c(unsigned t) {
    unsigned d;
    asm volatile("1:\n\ts_memtime vcc\n\ts_waitcnt lgkmcnt(0)\n\ts_sub_i32 %0, vcc_lo, %1\n\ts_cmp_lt_i32 %0, 0\n\ts_cbranch_scc1 1b"
                 : "=&s"(d) : "s"(__builtin_amdgcn_readfirstlane(t)) : "memory", "scc", "vcc");
}
constexpr unsigned SPINCAP = 40000;
DEVI bool poll_fail(unsigned& spins, GAS unsigned* ctl, int back) {
    if (back >= 128) __builtin_amdgcn_s_sleep(2); else if (back) __builtin_amdgcn_s_sleep(1);
    ++spins;
    if ((spins & 63u) == 0u) {
        if (spins > SPINCAP) { __hip_atomic_store(ctl + C_ABORT, 1u, RLX_AGENT); return true; }
        if (__builtin_amdgcn_readfirstlane(__hip_atomic_load(ctl + C_ABORT, RLX_AGENT))) return true;
    }
    return false;
}

DEVI void wait_ge(GAS unsigned* w, unsigned v, GAS unsigned* ctl, bool& dead) {
    unsigned spins = 0;
    while (!dead && (unsigned)__builtin_amdgcn_readfirstlane(__hip_atomic_load(w, RLX_AGENT)) < v) { if (poll_fail(spins, ctl, 64)) dead = true; }
    asm volatile("" ::: "memory");
}

__global__ void __launch_bounds__(320) k_prep(Params p) {
    const int tid = threadIdx.x, lane = tid & 63, wave = tid >> 6, b = blockIdx.x;
    unsigned* ctl = (unsigned*)(p.ws + O_CTL);
    if (b >= 196 && tid < 256) ctl[(b - 196) * 256 + tid] = 0u;
    if (tid == 256) {
        const unsigned page = 1 + (b >> 3) % 20;
        __amdgpu_buffer_rsrc_t rs = __builtin_amdgcn_make_buffer_rsrc(p.ws, 0, 0xFFFFFF00u, 0x00020000);
        unsigned t[2] = {0xffffffffu, 0xffffffffu};
        for (int rep = 0; rep < 2; ++rep)
            for (int side = 0; side < 2; ++side) {
                const unsigned long long c0 = __builtin_amdgcn_s_memtime();
                asm volatile("s_waitcnt lgkmcnt(0)" ::: "memory");
                st4(SENT, rs, page * PAGE + 480 * 1024 + side * 8192 + (b & 7) * 512 + (b >= 160 ? 256 : 0), 0);
                asm volatile("s_waitcnt vmcnt(0)" ::: "memory");
                const unsigned long long c1 = __builtin_amdgcn_s_memtime();
                t[side] = min(t[side], (unsigned)(c1 - c0));
            }
        ctl[C_SIDE + b] = (t[1] < t[0] ? 1u : 0u) ^ (xcc_id() >= 4 ? 1u : 0u) ^ (page & 1u);
    }
    if (wave == 4) return;
    float* R = (float*)(p.ws + O_R); float* G = (float*)(p.ws + O_G);
    v4u xv[2][2], wr[2], gw[2];
#pragma unroll
    for (int q = 0; q < 2; ++q) {
        const int k = q * 512 + lane * 8;
        xv[0][q] = *(const v4u*)(p.x + (b * 8 + wave * 2) * D_ + k); xv[1][q] = *(const v4u*)(p.x + (b * 8 + wave * 2 + 1) * D_ + k);
        wr[q] = *(const v4u*)(p.wr + k); gw[q] = *(const v4u*)(p.gw + k);
    }
    float a[2] = {0.f, 0.f}, g[2] = {0.f, 0.f};
#pragma unroll
    for (int rr = 0; rr < 2; ++rr)
#pragma unroll
        for (int q = 0; q < 2; ++q)
#pragma unroll
            for (int e = 0; e < 4; ++e) {
                const unsigned u = xv[rr][q][e];
                a[rr] = fmaf(lo16(u), lo16(wr[q][e]), a[rr]); a[rr] = fmaf(hi16(u), hi16(wr[q][e]), a[rr]);
                const unsigned pr = pk2(lo16(u) * lo16(gw[q][e]), hi16(u) * hi16(gw[q][e]));
                g[rr] += lo16(pr); g[rr] += hi16(pr);
            }
#pragma unroll
    for (int m = 32; m >= 1; m >>= 1) { a[0] += __shfl_xor(a[0], m); g[0] += __shfl_xor(g[0], m); a[1] += __shfl_xor(a[1], m); g[1] += __shfl_xor(g[1], m); }
    if (lane < 2) { const int row = b * 8 + wave * 2 + lane; R[row] = rnd(lane ? a[1] : a[0]); G[row] = rnd(1.f / (1.f + __expf(-rnd(lane ? g[1] : g[0])))); }
}

constexpr unsigned LC_PA = 0, LC_QK = 24576, LC_V = LC_QK + 4096, LC_O = LC_V + 2048, LC_MISC = 98304 + 16, LC_KV = 33792  , LC_END = LC_KV + 32768;

DEVI void chain_body(const Params& p, LAS unsigned char* lds, const int g, const int i) {
    const int tid = threadIdx.x, lane = tid & 63, wave = __builtin_amdgcn_readfirstlane(tid >> 6);
    const int h = g * 8 + (i >> 2), j = i & 3, cid = h * 4 + j;
    const int kw = (wave + i) & 7;
    GAS unsigned* ctl = (GAS unsigned*)(p.ws + O_CTL);
    const __amdgpu_buffer_rsrc_t rs = __builtin_amdgcn_make_buffer_rsrc(p.ws, 0, 0xFFFFFF00u, 0x00020000);
    const unsigned sidemask = side_mask(ctl, lane);
    v8u WA[6][2], WO[2][2];
    v4u sa[2][12], so[8];
    {
        const u16* wqk = (j < 2 ? p.wq : p.wk) + h * 128 + 64 * (j & 1) + (lane & 7) * 8;
        const u16* wvv = p.wv + h * 128 + 32 * j + (lane & 3) * 8;
#pragma unroll
        for (int ks = 0; ks < 2; ++ks) {
#pragma unroll
            for (int q = 0; q < 8; ++q) sa[ks][q] = *(const v4u*)(wqk + (size_t)(128 * kw + 64 * ks + 8 * q + (lane >> 3)) * 4096);
#pragma unroll
            for (int q = 0; q < 4; ++q) sa[ks][8 + q] = *(const v4u*)(wvv + (size_t)(128 * kw + 64 * ks + 16 * q + (lane >> 2)) * 4096);
        }
#pragma unroll
        for (int q = 0; q < 8; ++q) so[q] = *(const v4u*)(p.wo + (size_t)(h * 128 + 16 * wave + 2 * q + (lane >> 5)) * 1024 + 256 * j + (lane & 31) * 8);
    }
    SB();
    const int tau = lane >> 5, rh = (lane >> 4) & 1, n16 = lane & 15, c8 = (lane >> 3) & 1, g4 = lane >> 4;
    const unsigned o_gm = amap(g * 16384 + kw * 2048 + lane * 16);
    const unsigned qkvb = L_QKV + h * 6144;
    const int pcol = 256 * j + 32 * wave + 16 * tau + n16;
    const unsigned o_pp = amap((pcol >> 3) * 4096 + h * 128 + (rh * 8 + (pcol & 7)) * 8);
    const int sidx = c8 ? 0xEEEE : 0x4444;
    LAS unsigned char* const pw = lds + LC_PA + ((wave * 2 + rh) * 96 + 16 * tau + n16) * 16;
    LAS unsigned char* const ob = lds + LC_O + (lane & 7) * 320 + c8 * 16 + g4 * 32;
    const v4u rd = {(unsigned)(uintptr_t)p.ws, (unsigned)((uintptr_t)p.ws >> 32) & 0xffffu, 0xFFFFFF00u, 0x00020000u};
    const unsigned o_i1 = amap(qkvb + j * 1024 + lane * 16);
    const unsigned o_i2 = lane < 32 ? amap(qkvb + 4096 + j * 512 + lane * 16) : amap(((lane >> 3) & 3) * 16384 + cid * 128 + (lane & 7) * 16);
    {
        const v4u zz = {SENT, SENT, SENT, SENT};
        if (wave < 2) {
            const unsigned sb = P_HAND * PAGE + wave * 524288u, sx = ((sidemask >> P_HAND) & 1u) << 13;
            st16(zz, rs, o_i1 ^ sx, sb);
            v4u iv = zz;
            if (wave == 0 && lane >= 32) {
                iv = *(const v4u*)(p.mem0 + (lane & 7) * 1024 + cid * 8);
                iv = (v4u){nosent(iv.x), nosent(iv.y), nosent(iv.z), nosent(iv.w)};
            }
            st16(iv, rs, o_i2 ^ sx, sb);
        }
#pragma unroll
        for (int sl = 0; sl < 2; ++sl) st8((v2u){SENT, SENT}, rs, o_pp ^ (((sidemask >> (P_PART + sl)) & 1u) << 13), (P_PART + sl) * PAGE);
    }
    bool dead = false;
    const unsigned o_kv = (wave & 2 ? O_VX : O_KX) + (wave & 1) * 4096 + lane * 16, fpar = far_par(sidemask);
    v4u p0, p1, p2, p3;
    {
        LAS unsigned char* img = lds + wave * 12288;
        const int wr = lane >> 2;
        LAS unsigned char* iw = img + ((wr & 9) | ((wr >> 1) & 2) | ((wr << 1) & 4)) * 64 + (((lane & 3) ^ ((wr >> 2) & 2)) * 16);
        const int gB = lane >> 4, q4 = (lane & 15) >> 2, p4 = lane & 3;
        LAS unsigned char* ir = img + (16 * (gB >> 1) + 2 * (gB & 1) + (q4 & 1) + 8 * (q4 >> 1)) * 64 + p4 * 8;
        const unsigned hs = (q4 >> 1) * 32;
        LAS unsigned char* jw = img + (lane >> 3) * 128 + (lane & 1) * 16;
        const int jc = ((lane >> 1) & 3) ^ (lane >> 5);
        LAS unsigned char* jr = img + (16 * (gB >> 1) + 4 * (gB & 1) + (q4 & 1) + 8 * (q4 >> 1)) * 128 + p4 * 8;
        const int js = (gB & 1) | ((q4 >> 1) << 1);
        auto shuffleA = [&](int ks) {
#pragma unroll
            for (int q = 0; q < 8; ++q) *(LAS v4u*)(jw + q * 1024 + ((jc ^ ((q & 1) << 1)) * 32)) = sa[ks][q];
#pragma unroll
            for (int q = 0; q < 4; ++q) *(LAS v4u*)(iw + 8192 + q * 1024) = sa[ks][8 + q];
            asm volatile("s_waitcnt lgkmcnt(0)" ::: "memory");
#pragma unroll
            for (int t = 0; t < 6; ++t)
#pragma unroll
                for (int m = 0; m < 4; ++m) {
                    const v2u r = t < 4 ? trrd(jr + ((t ^ js) * 32) + (32 * (m >> 1) + 2 * (m & 1)) * 128) : trrd(ir + 8192 + (((t & 1) * 32) ^ hs) + (32 * (m >> 1) + 4 * (m & 1)) * 64);
                    WA[t][ks][2 * m] = r.x; WA[t][ks][2 * m + 1] = r.y;
                }
            asm volatile("s_waitcnt lgkmcnt(0)" ::: "memory");
        };
        shuffleA(0);
        SB();
        asm volatile("s_waitcnt vmcnt(0)" ::: "memory");
        BAR();
        if (tid == 0) {
            if (__hip_atomic_fetch_add(ctl + C_ARRIVE, 1u, RLX_AGENT) == 127u) __hip_atomic_store(ctl + C_T0, ((rt() + T0_LEAD) & ~3u) | 0x80000000u, RLX_AGENT);
        }
        SB();
        shuffleA(1);
        SB();
        if (wave >= 4) {
            wait_ge(ctl + C_KV + 8 * h, 4u, ctl, dead);
            const unsigned so = fb(h, fpar); ALD16(p0, o_kv, so, 0); ALD16(p1, o_kv, so, 1024); ALD16(p2, o_kv, so, 2048); ALD16(p3, o_kv, so, 3072);
        }
        BAR();
#pragma unroll
        for (int q = 0; q < 8; ++q) {
            const int f = (lane >> 5) | (((q >> 1) & 3) << 1);
            *(LAS v4u*)(lds + wave * 8192 + q * 1024 + (lane >> 5) * 512 + ((((lane >> 1) & 15) ^ f) * 32) + (lane & 1) * 16) = so[q];
        }
        BAR();
        const int fo = (q4 & 1) | (js << 1);
        LAS unsigned char* orp = lds + (16 * (gB >> 1) + 4 * (gB & 1) + (q4 & 1) + 8 * (q4 >> 1)) * 512 + p4 * 8;
#pragma unroll
        for (int t = 0; t < 2; ++t)
#pragma unroll
            for (int ks = 0; ks < 2; ++ks)
#pragma unroll
                for (int m = 0; m < 4; ++m) {
                    const v2u r = trrd(orp + (((2 * wave + t) ^ fo) * 32) + (64 * ks + 32 * (m >> 1) + 2 * (m & 1)) * 512);
                    WO[t][ks][2 * m] = r.x; WO[t][ks][2 * m + 1] = r.y;
                }
    }
#define HAND_ADDR(s) do { const unsigned pg = P_HAND + ((s) >> 2); sb = pg * PAGE + ((s) & 3) * 524288u; sx = ((sidemask >> pg) & 1u) << 13; } while (0)
#define RETRY(RELOAD, BAD)    do { unsigned spins = 0;                                                                    \
                while (!dead) { if (poll_fail(spins, ctl, 0)) { dead = true; break; } RELOAD; if (!(BAD)) break; } } while (0)
#define ZERO_A() f32x4 c0 = {0, 0, 0, 0}, c1 = c0, c2 = c0, c3 = c0, c4 = c0, c5 = c0; PIN2(c0, c1); PIN2(c2, c3); PIN2(c4, c5)
#define GATHER_MEM(EXTRA, NX) v4u a0, a1;                                                                                 \
            ALD16(a0, o_gmx, sb, 0); ALD16(a1, o_gmx, sb, 1024); EXTRA; WAITV2(NX, a0, a1);                               \
            if (__builtin_expect(any_sent(mx4(a1, mx4(a0, 0u))), 0))                                                      \
                RETRY(ALD16(a0, o_gmx, sb, 0); ALD16(a1, o_gmx, sb, 1024); WAITV2(0, a0, a1), any_sent(mx4(a1, mx4(a0, 0u))))
#define QK_MFMA() do {                                                                                                    \
            c0 = smf(a0, WA[0][0], c0, sidx); c1 = smf(a0, WA[1][0], c1, sidx); c0 = smf(a1, WA[0][1], c0, sidx); c1 = smf(a1, WA[1][1], c1, sidx); \
            SB();                                                                                                         \
            c2 = smf(a0, WA[2][0], c2, sidx); c3 = smf(a0, WA[3][0], c3, sidx); c2 = smf(a1, WA[2][1], c2, sidx); c3 = smf(a1, WA[3][1], c3, sidx); \
            SB(); *(LAS f32x4*)pw = fold(c0, c1); SB(); *(LAS f32x4*)(pw + 512) = fold(c2, c3); SB(); } while (0)
#define V_PART() do { c4 = smf(a0, WA[4][0], c4, sidx); c5 = smf(a0, WA[5][0], c5, sidx); c4 = smf(a1, WA[4][1], c4, sidx); c5 = smf(a1, WA[5][1], c5, sidx); \
            *(LAS f32x4*)(pw + 1024) = fold(c4, c5);                                                                      \
            if (lane == 0) __hip_atomic_fetch_add((LAS unsigned*)(lds + LC_MISC + 4), 1u, __ATOMIC_RELAXED, __HIP_MEMORY_SCOPE_WORKGROUP); } while (0)
#define ZERO_D() f32x4 d0 = {0, 0, 0, 0}, d1 = d0; const unsigned pp = P_PART + (s & 3), px = ((sidemask >> pp) & 1u) << 13; unsigned o_ppx = o_pp ^ px; PIN2(d0, d1); asm volatile("" : "+v"(o_ppx))
#define WO_STAGE() do {                                                                                                   \
            const v4u o0 = *(LAS v4u*)ob, o1 = *(LAS v4u*)(ob + 128); SB();                                               \
            d0 = smf(o0, WO[0][0], d0, sidx); d1 = smf(o0, WO[1][0], d1, sidx); d0 = smf(o1, WO[0][1], d0, sidx); d1 = smf(o1, WO[1][1], d1, sidx); \
            const f32x4 f = fold(d0, d1);                                                                                 \
            st8((v2u){nosent(pk2(f.x, f.y)), nosent(pk2(f.z, f.w))}, rs, o_ppx, pp * PAGE); } while (0)
#define REDUCE4(LP, OFF) do {                                                                                             \
            const f32x4 r0 = *(LAS f32x4*)(LP), r1 = *(LAS f32x4*)((LP) + 3072), r2 = *(LAS f32x4*)((LP) + 6144), r3 = *(LAS f32x4*)((LP) + 9216); \
            const f32x4 t = (r0 + r1) + (r2 + r3);                                                                        \
            st4(nosent(pk2(sw32sum(t.x, t.z), sw32sum(t.y, t.w))), rs, (OFF), sb); } while (0)
    unsigned sb, sx;
    if (wave < 4) {
        const unsigned o_gq = amap(qkvb + wave * 1024 + lane * 16);
        const unsigned o_gp = amap(cid * 4096 + lane * 16);
        const int crow = 4 * ((lane >> 2) & 1) + g4;
        v4u sel = {0u, 0u, 0u, 0u};
        sel[(lane & 7) >> 1] = (lane & 1) ? 0x3f800000u : 0x00003f80u;
        const unsigned o_pm = amap(c8 * 16384 + cid * 128 + crow * 16 + (lane & 3) * 4);
        const unsigned o_gv = amap(qkvb + 4096 + wave * 512 + n16 * 16 + (4 * (g4 & 1) + 2 * (g4 >> 1)) * 2);
        LAS unsigned char* const qb = lds + LC_QK + (4 * g4 + ((lane & 15) >> 2)) * 16 + (lane & 1) * 8;
        LAS unsigned char* const qw = lds + LC_QK + wave * 1024 + lane * 16;
        LAS unsigned char* const ow = lds + LC_O + (lane & 7) * 320 + (32 * wave + 16 * c8 + 4 * g4) * 2;
        if (tid == 0) *(LAS unsigned*)(lds + LC_MISC + 4) = 0u;
        BAR();
        if (wave == 0) {
            unsigned plan, spins = 0;
            for (;;) { plan = __builtin_amdgcn_readfirstlane(__hip_atomic_load(ctl + C_T0, RLX_AGENT)); if (plan) break; if (poll_fail(spins, ctl, 64)) { dead = true; break; } }
            { const unsigned now = rt(); plan = now + ((int)((plan - now) << 1) >> 1); }
            if (!dead) wait_l(plan);
        }
        v4u kx[8], vx[2];
        unsigned tq;
#define A_STAGE_A(S1)                                                                                                     \
            BAR();                                                                                  \
            GATHER_MEM({ LAS unsigned char* kvb = lds + LC_KV + ((S1) & 1) * 16384 + lane * 16;                           \
                _Pragma("unroll") for (int q = 0; q < 8; ++q) kx[q] = *(LAS v4u*)(kvb + q * 1024);                        \
                vx[0] = *(LAS v4u*)(kvb + 8192 + wave * 2048); vx[1] = *(LAS v4u*)(kvb + 8192 + wave * 2048 + 1024); SB(); }, 0); \
            QK_MFMA();                                                                                                    \
            BAR();                                                                                                 \
            tq = mt() + DQ;                                                                                          \
            V_PART()
        {
            HAND_ADDR(0);
            ZERO_A();
            const unsigned o_gmx = o_gm ^ sx;
            A_STAGE_A(0);
        }
        for (int s = 0; s < NSEG - 1; ++s) {
            __builtin_amdgcn_s_setprio(3);
            v4u gq;
            ZERO_D();
            unsigned o_gqx = o_gq ^ sx, o_gvx = o_gv ^ sx; PIN2(o_gqx, o_gvx);
            wait_c(tq);
            ALD16(gq, o_gqx, sb, 0); WAITV1(0, gq);
            *(LAS v4u*)qw = gq;
            SB();
            if (__builtin_expect(any_sent(mx4(gq, 0u)), 0)) { RETRY(ALD16(gq, o_gqx, sb, 0); WAITV1(0, gq), any_sent(mx4(gq, 0u))); *(LAS v4u*)qw = gq; }
            BAR();
            {
                unsigned va, vc;
                ALD4(va, o_gvx, sb, 0); ALD4(vc, o_gvx, sb, 256);
                v4u qf[4], kf[4];
#pragma unroll
                for (int c = 0; c < 4; ++c) { const v2u qa = trrd(qb + c * 512), qc = trrd(qb + c * 512 + 256); qf[c] = (v4u){qa.x, qa.y, qc.x, qc.y}; }
#pragma unroll
                for (int c = 0; c < 4; ++c) { const v2u ka = trrd(qb + c * 512 + 2048), kc = trrd(qb + c * 512 + 2304); kf[c] = (v4u){ka.x, ka.y, kc.x, kc.y}; }
                SB();
                f32x4 s0 = {0, 0, 0, 0}, s1 = s0, sm = s0;
#pragma unroll
                for (int c = 0; c < 4; ++c) { s0 = mfma32(kx[c], qf[c], s0); s1 = mfma32(kx[4 + c], qf[c], s1); sm = mfma32(kf[c], qf[c], sm); }
                SB();
                float x0 = c8 ? s1.x : s0.x, x1 = c8 ? s1.y : s0.y, x2 = c8 ? s1.z : s0.z, x3 = c8 ? s1.w : s0.w;
                float xm = tau ? (c8 ? sm.w : sm.z) : (c8 ? sm.y : sm.x);
                constexpr float CS = SCALE * LOG2E;
                const float ml = -CS * max5x8(x0, x1, x2, x3, xm);
                x0 = __builtin_amdgcn_exp2f(fmaf(x0, CS, ml)); x1 = __builtin_amdgcn_exp2f(fmaf(x1, CS, ml));
                x2 = __builtin_amdgcn_exp2f(fmaf(x2, CS, ml)); x3 = __builtin_amdgcn_exp2f(fmaf(x3, CS, ml));
                xm = __builtin_amdgcn_exp2f(fmaf(xm, CS, ml));
                const unsigned e01 = pk2(x0, x1), e23 = pk2(x2, x3), em = pk2(xm, xm);
                const unsigned f01 = __float_as_uint(ror8(__uint_as_float(e01))), f23 = __float_as_uint(ror8(__uint_as_float(e23)));
                const v4u b1 = {c8 ? f01 : e01, c8 ? f23 : e23, c8 ? e01 : f01, c8 ? e23 : f23};
                unsigned b2 = em & (c8 ? 0xffff0000u : 0x0000ffffu);
                b2 |= __float_as_uint(ror8(__uint_as_float(b2)));
                asm volatile("" : "+v"(b2));
                f32x4 o0 = {0, 0, 0, 0}, o1 = o0;
                o0 = mfma32(vx[0], b1, o0); o1 = mfma32(vx[1], b1, o1);
                SB();
                WAITV2(0, va, vc);
                if (__builtin_expect(any_sent(max(va, vc)), 0)) RETRY(ALD4(va, o_gvx, sb, 0); ALD4(vc, o_gvx, sb, 256); WAITV2(0, va, vc), any_sent(max(va, vc)));
                o0 = mfma16((v2u){va, 0u}, (v2u){b2, 0u}, o0); o1 = mfma16((v2u){vc, 0u}, (v2u){b2, 0u}, o1);
                SB();
                float l = ((x0 + x1) + (x2 + x3)) + xm;
                l += ror8(l); l = sw16sum(l, l); l = sw32sum(l, l);
                const float rinv = __builtin_amdgcn_rcpf(l);
                SB();
                float y[4];
#pragma unroll
                for (int e = 0; e < 4; ++e) y[e] = (c8 ? o1[e] : o0[e]) * rinv;
                *(LAS v2u*)ow = (v2u){pk2(y[0], y[1]), pk2(y[2], y[3])};
            }
            BAR();
            WO_STAGE();
            const unsigned t3 = mt() + D3;
            __builtin_amdgcn_s_setprio(0);
            HAND_ADDR(s + 1);
            ZERO_A();
            unsigned o_gmx = o_gm ^ sx; asm volatile("" : "+v"(o_gmx));
            if (__builtin_expect(wave == 0, 1)) {
                unsigned o_pmx = o_pm ^ sx, o_gpx = o_gp ^ px; PIN2(o_pmx, o_gpx);
                wait_c(t3);
                v4u r0, r1, r2, r3; f32x4 u; unsigned m;
#define C_LOAD() ALD16(r0, o_gpx, pp * PAGE, 0); ALD16(r1, o_gpx, pp * PAGE, 1024); ALD16(r2, o_gpx, pp * PAGE, 2048); ALD16(r3, o_gpx, pp * PAGE, 3072);  \
                WAITV1(3, r0); u = mfma32(sel, r0, (f32x4){0, 0, 0, 0}); m = mx4(r0, 0u); SB(); WAITV1(2, r1); u = mfma32(sel, r1, u); m = mx4(r1, m); SB(); \
                WAITV1(1, r2); u = mfma32(sel, r2, u); m = mx4(r2, m); SB(); WAITV1(0, r3); u = mfma32(sel, r3, u); m = mx4(r3, m)
                C_LOAD();
                if (__builtin_expect(any_sent(m), 0)) RETRY(C_LOAD(), any_sent(m));
                float z0 = tau ? u.z : u.x, z1 = tau ? u.w : u.y;
                z0 += ror8(z0); z1 += ror8(z1);
                { auto sw = __builtin_amdgcn_permlane16_swap(__float_as_uint(z0), __float_as_uint(z1), false, false); z0 = __uint_as_float(sw[0]); z1 = __uint_as_float(sw[1]); }
                const unsigned w2 = nosent(pk2(z0, z1));
                st4(w2, rs, o_pmx, sb); st4(w2, rs, o_pmx + 65536, sb);
                const unsigned plan = mt() + D1;
                wait_c(plan);
            }
            A_STAGE_A(s + 1);
        }
    } else {
        const int hw = wave - 4;
        LAS unsigned char* const l_qk = lds + LC_PA + tau * 12288 + (rh * 96 + 16 * hw + n16) * 16;
        const unsigned o_pqk = amap(qkvb + j * 1024 + (16 * hw + n16) * 16 + rh * 8 + tau * 4);
        LAS unsigned char* const l_v = lds + LC_PA + tau * 12288 + (rh * 96 + 64 + 16 * (hw & 1) + n16) * 16;
        const unsigned o_pv = amap(qkvb + 4096 + j * 512 + (16 * (hw & 1) + n16) * 16 + rh * 8 + tau * 4);
        LAS unsigned char* const l_kv = lds + LC_KV + hw * 4096 + lane * 16;
        WAITV2(0, p0, p1); WAITV2(0, p2, p3);
        BAR();
        *(LAS v4u*)l_kv = p0; *(LAS v4u*)(l_kv + 1024) = p1; *(LAS v4u*)(l_kv + 2048) = p2; *(LAS v4u*)(l_kv + 3072) = p3;
        __builtin_amdgcn_s_setprio(2);
        for (int s = -1; s < NSEG - 1; ++s) {
            if (s >= 0) {
                ZERO_D();
                BAR();
                __builtin_amdgcn_s_sleep(3);
                { const unsigned so = fb((s + 1) * 32 + h, fpar); ALD16(p0, o_kv, so, 0); ALD16(p1, o_kv, so, 1024); ALD16(p2, o_kv, so, 2048); ALD16(p3, o_kv, so, 3072); }
                {
                    const unsigned rp = P_PART + ((s + 2) & 3), rx = ((sidemask >> rp) & 1u) << 13;
                    st8((v2u){SENT, SENT}, rs, o_pp ^ rx, rp * PAGE); st8((v2u){SENT, SENT}, rs, (o_pp - 8 * 16384) ^ rx, rp * PAGE);
                    if (hw >= 2 && s + 2 < NSEG) {
                        const unsigned pg = P_HAND + ((s + 2) >> 2);
                        st16((v4u){SENT, SENT, SENT, SENT}, rs, (hw == 2 ? o_i1 : o_i2) ^ (((sidemask >> pg) & 1u) << 13), pg * PAGE + ((s + 2) & 3) * 524288u);
                    }
                }
                BAR();
                WO_STAGE();
                if (j == 0 && tid == 256) __hip_atomic_store(ctl + C_PROG + 32 * h, (unsigned)(s + 1), RLX_AGENT);
                if (((s + 2) & 3) == 0 && s + 2 < NSEG) wait_ge(ctl + C_KV + 8 * (((s + 2) >> 2) * 32 + h), 2u, ctl, dead);
                WAITV2(0, p0, p1); WAITV2(0, p2, p3);
                LAS unsigned char* kw = l_kv + ((s + 1) & 1) * 16384;
                *(LAS v4u*)kw = p0; *(LAS v4u*)(kw + 1024) = p1; *(LAS v4u*)(kw + 2048) = p2; *(LAS v4u*)(kw + 3072) = p3;
            }
            HAND_ADDR(s + 1);
            ZERO_A();
            unsigned o_pqkx = o_pqk ^ sx, o_gmx = o_gm ^ sx; PIN2(o_pqkx, o_gmx);
            BAR();
            GATHER_MEM(, 0);
            QK_MFMA();
            BAR();
            REDUCE4(l_qk, o_pqkx);
            SB();
            if (s == NSEG - 2 && j == 0 && tid == 256) __hip_atomic_store(ctl + C_PROG + 32 * h, (unsigned)NSEG, RLX_AGENT);
            V_PART();
            if (hw < 2) {
                while (__builtin_amdgcn_readfirstlane(*(volatile LAS unsigned*)(lds + LC_MISC + 4)) < 8u * (unsigned)(s + 2)) { }
                asm volatile("" ::: "memory");
                REDUCE4(l_v, o_pv ^ sx);
            }
        }
    }
}

DEVI int claim_slot(GAS unsigned* ctl) {
    const unsigned x = xcc_id() & 7u;
    const unsigned r = __hip_atomic_fetch_add(ctl + C_XR + 32 * x, 1u, RLX_AGENT);
    if (r < 32 && __hip_atomic_exchange(ctl + C_SLOT + x * 32 + r, 1u, RLX_AGENT) == 0u) return (int)(x * 32 + r);
    for (int k = 255;; k = (k + 255) & 255) if (__hip_atomic_exchange(ctl + C_SLOT + k, 1u, RLX_AGENT) == 0u) return k;
}

DEVI void glds(unsigned voff, const void* sbase, unsigned ldsaddr) {
    unsigned keep;
    asm volatile("s_mov_b32 %0, m0\n\ts_mov_b32 m0, %3\n\ts_nop 2\n\tglobal_load_lds_dwordx4 %1, %2\n\ts_mov_b32 m0, %0" : "=&s"(keep) : "v"(voff), "s"(sbase), "s"(ldsaddr) : "memory");
}
template <int BM, int BN, int WM, bool TR, bool PERMK>
struct Gemm {
    static constexpr int WN = 8 / WM, MT = BM / WM / 16, NT = BN / WN / 16, NA = BM / 64, NB = BN / 64, STAGE = (BM + BN) * 128, NS = 147456 / STAGE, LPR = BN / 8;
    f32x4 acc[MT][NT];
    DEVI void run(LAS unsigned char* lds, const u16* A, int lda, const u16* B, int ldb, int nk) {
        const int tid = otid(), lane = tid & 63, wave = __builtin_amdgcn_readfirstlane(tid >> 6);
        const int wm = wave / WN, wn = wave % WN;
        unsigned va[NA], vb[NB];
#pragma unroll
        for (int q = 0; q < NA; ++q) va[q] = ((wave * NA * 8 + q * 8 + (lane >> 3)) * lda + (((lane & 7) ^ ((lane >> 3) & 7)) * 8)) * 2;
#pragma unroll
        for (int q = 0; q < NB; ++q) {
            const int k = (wave * NB + q) * (64 / LPR) + lane / LPR, sl = lane % LPR;
            const int pk = PERMK ? ((k & 32) + 16 * ((k >> 2) & 1) + 4 * ((k >> 3) & 3) + (k & 3)) : k;
            const int f = (k & 3) | (((k >> 3) & 1) << 2);
            vb[q] = (pk * ldb + (((sl >> 1) ^ f) * 16 + (sl & 1) * 8)) * 2;
        }
        const unsigned lbase = (unsigned)(uintptr_t)lds;
        auto piece = [&](int kt, int buf, int q) {
            if (q < NA) glds(va[q], A + kt * 64, lbase + buf * STAGE + (wave * NA + q) * 1024);
            else glds(vb[q - NA], B + (size_t)kt * 64 * ldb, lbase + buf * STAGE + BM * 128 + (wave * NB + q - NA) * 1024);
        };
        const int fr = lane & 15, fg = lane >> 4;
        const unsigned ra = (wm * (BM / WM) + fr) * 128;
        const unsigned ra0 = ra + ((fg ^ (fr & 7)) * 16), ra1 = ra + (((4 + fg) ^ (fr & 7)) * 16);
        const int q4 = fr >> 2, p4 = fr & 3, fsw = q4 | ((fg & 1) << 2);
        const unsigned rb = BM * 128 + (8 * fg + q4) * (BN * 2) + p4 * 8 + (((wn * NT) ^ fsw) * 32);
        v4u a0[MT], b0[NT], a1[MT], b1[NT];
        auto frag = [&](LAS unsigned char* sp, int ks, v4u* af, v4u* bf, int r) {
            if (r < NT) { LAS unsigned char* q = sp + (rb ^ (r * 32)) + ks * (BN * 64); const v2u x = trrd(q), y = trrd(q + BN * 8); bf[r] = (v4u){x.x, x.y, y.x, y.y}; }
            else af[r - NT] = *(LAS v4u*)(sp + (ks ? ra1 : ra0) + (r - NT) * 2048);
        };
        constexpr int NM = MT * NT, NF = MT + NT, NP = NA + NB;
        auto half = [&](const v4u* af, const v4u* bf, LAS unsigned char* np, int nks, v4u* naf, v4u* nbf, bool rd, bool stg, int skt, int sbuf) {
#pragma unroll
            for (int i = 0; i < NM; ++i) {
                const int m = i / NT, n = i % NT;
                acc[m][n] = TR ? mfma32(bf[n], af[m], acc[m][n]) : mfma32(af[m], bf[n], acc[m][n]);
#pragma unroll
                for (int r = i * NF / NM; r < (i + 1) * NF / NM; ++r) if (rd) frag(np, nks, naf, nbf, r);
#pragma unroll
                for (int q = i * NP / NM; q < (i + 1) * NP / NM; ++q) if (stg) piece(skt, sbuf, q);
                SB();
            }
        };
#pragma unroll
        for (int m = 0; m < MT; ++m)
#pragma unroll
            for (int n = 0; n < NT; ++n) acc[m][n] = (f32x4){0, 0, 0, 0};
#pragma unroll
        for (int q = 0; q < NS * NP; ++q) piece(q / NP, q / NP, q % NP);
        asm volatile("s_waitcnt vmcnt(%0)\n\ts_barrier" :: "n"((NS - 1) * NP) : "memory");
#pragma unroll
        for (int r = 0; r < NF; ++r) frag(lds, 0, a0, b0, r);
        int buf = 0;
        for (int kt = 0; kt < nk - NS; ++kt) {
            const int nb = buf == NS - 1 ? 0 : buf + 1;
            half(a0, b0, lds + buf * STAGE, 1, a1, b1, true, false, 0, 0);
            asm volatile("s_waitcnt vmcnt(%0) lgkmcnt(0)\n\ts_barrier" :: "n"((NS - 2) * NP) : "memory");
            half(a1, b1, lds + nb * STAGE, 0, a0, b0, true, true, kt + NS, buf);
            buf = nb;
        }
#pragma unroll
        for (int t = NS; t >= 1; --t) {
            const int nb = buf == NS - 1 ? 0 : buf + 1;
            half(a0, b0, lds + buf * STAGE, 1, a1, b1, true, false, 0, 0);
            asm volatile("s_waitcnt vmcnt(%0) lgkmcnt(0)\n\ts_barrier" :: "n"(t >= 2 ? (t - 2) * NP : 0) : "memory");
            half(a1, b1, lds + nb * STAGE, 0, a0, b0, t > 1, false, 0, 0);
            buf = nb;
        }
    }
};
DEVI v4u pk8(f32x4 a, f32x4 b) { return (v4u){pk2(a.x, a.y), pk2(a.z, a.w), pk2(b.x, b.y), pk2(b.z, b.w)}; }
DEVI void announce(GAS unsigned* w) {
    asm volatile("s_waitcnt vmcnt(0) lgkmcnt(0)\n\ts_barrier" ::: "memory");
    if (otid() == 0) __hip_atomic_fetch_add(w, 1u, RLX_AGENT);
}

template <int BM, int BN>
DEVI void kv_unit(const Params& p, LAS unsigned char* lds, int r0, int hh, int mat, unsigned fpar) {
    constexpr int WM = BM >= 128 ? 4 : 2, WN = 8 / WM, MT = BM / WM / 16, NT = BN / WN / 16;
    const int lane = otid() & 63, wave = __builtin_amdgcn_readfirstlane(otid() >> 6), wm = wave / WN, wn = wave % WN;
    const __amdgpu_buffer_rsrc_t rs = __builtin_amdgcn_make_buffer_rsrc(p.ws, 0, 0xFFFFFF00u, 0x00020000);
    const int row0 = r0 + wm * (BM / WM), hw = hh + wn * NT / 8, n0 = (wn * NT) & 7;
    if (mat == 0) {
        Gemm<BM, BN, WM, true, false> g; g.run(lds, p.x + (size_t)r0 * D_, D_, p.wk + hh * 128, 4096, 16);
#pragma unroll
        for (int m = 0; m < MT; ++m)
#pragma unroll
            for (int c = 0; c < NT / 2; ++c) {
                const int row = row0 + m * 16;
                st16(pk8(g.acc[m][2 * c], g.acc[m][2 * c + 1]), rs, lane * 16, O_KX + fb((row >> 5) * 32 + hw, fpar) + (((row >> 4) & 1) * 4 + n0 / 2 + c) * 1024);
            }
    } else {
        Gemm<BM, BN, WM, false, false> g; g.run(lds, p.x + (size_t)r0 * D_, D_, p.wv + hh * 128, 4096, 16);
#pragma unroll
        for (int m = 0; m < MT; m += 2)
#pragma unroll
            for (int n = 0; n < NT; ++n) {
                const int row = row0 + m * 16;
                st16(pk8(g.acc[m][n], g.acc[m + 1][n]), rs, lane * 16, O_VX + fb((row >> 5) * 32 + hw, fpar) + (n0 + n) * 1024);
            }
    }
    asm volatile("s_waitcnt vmcnt(0) lgkmcnt(0)\n\ts_barrier" ::: "memory");
    const int t = otid();
    if (t < (BM == 256 ? 2 : 1) * (BN / 128))
        __hip_atomic_fetch_add((GAS unsigned*)(p.ws + O_CTL) + C_KV + 8 * (((r0 >> 7) + t / (BN / 128)) * 32 + hh + t % (BN / 128)), 1u, RLX_AGENT);
}

DEVI void qa_unit(const Params& p, LAS unsigned char* lds, int panel, int hh, unsigned sidemask, bool& dead) {
    const int lane = otid() & 63, wave = __builtin_amdgcn_readfirstlane(otid() >> 6);
    const int g4 = lane >> 4, n16 = lane & 15;
    const __amdgpu_buffer_rsrc_t rs = __builtin_amdgcn_make_buffer_rsrc(p.ws, 0, 0xFFFFFF00u, 0x00020000);
    GAS unsigned* ctl = (GAS unsigned*)(p.ws + O_CTL);
    v4u qf[2][4];
    {
        Gemm<256, 128, 8, true, false> g; g.run(lds, p.x + (size_t)panel * 256 * D_, D_, p.wq + hh * 128, 4096, 16);
#pragma unroll
        for (int m = 0; m < 2; ++m)
#pragma unroll
            for (int c = 0; c < 4; ++c) qf[m][c] = pk8(g.acc[m][2 * c], g.acc[m][2 * c + 1]);
    }
    const int seg = panel * 8 + wave;
    const unsigned fpar = far_par(sidemask), kvo = fb(seg * 32 + hh, fpar);
    wait_ge(ctl + C_KV + 8 * ((seg >> 2) * 32 + hh), seg >> 2 ? 2u : 4u, ctl, dead);
    v4u kx[8], vx[8];
#pragma unroll
    for (int q = 0; q < 8; ++q) kx[q] = ld16(rs, lane * 16 + q * 1024, O_KX + kvo);
#pragma unroll
    for (int q = 0; q < 8; ++q) vx[q] = ld16(rs, lane * 16 + q * 1024, O_VX + kvo);
    f32x4 z = {0, 0, 0, 0};
    f32x4 s00 = z, s10 = z, s11 = z, sm0 = z, sm1 = z;
#pragma unroll
    for (int c = 0; c < 4; ++c) { s00 = mfma32(kx[c], qf[0][c], s00); s10 = mfma32(kx[c], qf[1][c], s10); s11 = mfma32(kx[4 + c], qf[1][c], s11); }
    LAS unsigned char* img = lds + wave * 6144;
    wait_ge(ctl + C_PROG + 32 * hh, (unsigned)(seg + 1), ctl, dead);
    {
        const unsigned pg = P_HAND + (seg >> 2), sb = pg * PAGE + (seg & 3) * 524288u, sx = ((sidemask >> pg) & 1u) << 13;
        v4u t[6];
        for (unsigned spins = 0;;) {
            unsigned m = 0;
#pragma unroll
            for (int q = 0; q < 6; ++q) t[q] = ld16(rs, amap(L_QKV + hh * 6144 + q * 1024 + lane * 16) ^ sx, sb);
#pragma unroll
            for (int q = 0; q < 6; ++q) m = mx4(t[q], m);
            if (!any_sent(m) || dead) break;
            if (poll_fail(spins, ctl, 128)) dead = true;
            asm volatile("" ::: "memory");
        }
#pragma unroll
        for (int q = 0; q < 6; ++q) *(LAS v4u*)(img + q * 1024 + lane * 16) = t[q];
        asm volatile("s_waitcnt lgkmcnt(0)" ::: "memory");
    }
    LAS unsigned char* kb = img + 2048 + (4 * g4 + (n16 >> 2)) * 16 + (lane & 1) * 8;
#pragma unroll
    for (int c = 0; c < 4; ++c) {
        const v2u ka = trrd(kb + c * 512), kc = trrd(kb + c * 512 + 256);
        const v4u kf = {ka.x, ka.y, kc.x, kc.y};
        sm0 = mfma32(kf, qf[0][c], sm0); sm1 = mfma32(kf, qf[1][c], sm1);
    }
    v4u b1[2]; v2u b2[2];
#pragma unroll
    for (int m = 0; m < 2; ++m) {
        float v[12];
#pragma unroll
        for (int e = 0; e < 4; ++e) {
            const bool diag = 4 * g4 + e <= n16;
            v[e] = rnd(rnd(m ? s10[e] : s00[e]) * SCALE); if (m == 0 && !diag) v[e] = -INFINITY;
            v[4 + e] = (m && diag) ? rnd(rnd(s11[e]) * SCALE) : -INFINITY;
            v[8 + e] = g4 < 2 ? rnd(rnd(m ? sm1[e] : sm0[e]) * SCALE) : -INFINITY;
        }
        float mx = v[0];
#pragma unroll
        for (int e = 1; e < 12; ++e) mx = fmaxf(mx, v[e]);
        mx = sw16max(mx); mx = sw32max(mx);
        const float ml = -mx * LOG2E; float l = 0.f;
#pragma unroll
        for (int e = 0; e < 12; ++e) { v[e] = __builtin_amdgcn_exp2f(fmaf(v[e], LOG2E, ml)); l += v[e]; }
        l = sw16sum(l, l); l = sw32sum(l, l);
        const float ri = 1.f / l;
        b1[m] = (v4u){pk2(v[0] * ri, v[1] * ri), pk2(v[2] * ri, v[3] * ri), pk2(v[4] * ri, v[5] * ri), pk2(v[6] * ri, v[7] * ri)};
        b2[m] = (v2u){pk2(v[8] * ri, v[9] * ri), pk2(v[10] * ri, v[11] * ri)};
    }
#pragma unroll
    for (int c = 0; c < 4; ++c) {
        f32x4 o[2][2];
#pragma unroll
        for (int t = 0; t < 2; ++t) {
            const v2u va = *(LAS v2u*)(img + 4096 + ((2 * c + t) * 16 + n16) * 16 + (g4 & 1) * 8);
#pragma unroll
            for (int m = 0; m < 2; ++m) { o[m][t] = mfma32(vx[2 * c + t], b1[m], z); o[m][t] = mfma16(va, b2[m], o[m][t]); }
        }
#pragma unroll
        for (int m = 0; m < 2; ++m) st16(pk8(o[m][0], o[m][1]), rs, n16 * 16384 + 16 * g4, O_OX + fb(seg * 32 + m * 16, fpar) + (hh * 128 + c * 32) * 2);
    }
    asm volatile("s_waitcnt vmcnt(0)" ::: "memory");
    if (lane == 0) __hip_atomic_fetch_add(ctl + C_OF + 16 * (seg * 8 + (hh >> 2)), 1u, RLX_AGENT);
}

DEVI void top2(const float* R, int& i0, int& i1) {
    const int lane = otid() & 63;
    float b0 = -INFINITY, b1 = -INFINITY; int j0 = 0x7fffffff, j1 = 0x7fffffff;
    f32x4 rv[8];
#pragma unroll
    for (int q = 0; q < 8; ++q) rv[q] = *(const f32x4*)(R + lane * 32 + q * 4);
#pragma unroll
    for (int q = 0; q < 32; ++q) {
        const float v = rv[q >> 2][q & 3]; const int k = lane * 32 + q;
        const bool f0 = v > b0 || j0 == 0x7fffffff, f1 = v > b1 || j1 == 0x7fffffff;
        b1 = f0 ? b0 : (f1 ? v : b1); j1 = f0 ? j0 : (f1 ? k : j1); b0 = f0 ? v : b0; j0 = f0 ? k : j0;
    }
    for (int m = 1; m < 64; m <<= 1) {
        const float c0 = __shfl_xor(b0, m), c1 = __shfl_xor(b1, m); const int k0 = __shfl_xor(j0, m), k1 = __shfl_xor(j1, m);
        auto gt = [](float a, int ja, float b, int jb) { return a > b || (!(b > a) && ja < jb); };
        if (gt(c0, k0, b0, j0)) { if (gt(b0, j0, c1, k1)) { b1 = b0; j1 = j0; } else { b1 = c1; j1 = k1; } b0 = c0; j0 = k0; }
        else if (gt(c0, k0, b1, j1)) { b1 = c0; j1 = k0; }
    }
    i0 = __builtin_amdgcn_readfirstlane(j0); i1 = __builtin_amdgcn_readfirstlane(j1);
}
DEVI void ffn1_unit(const Params& p, LAS unsigned char* lds, int u, int i0, int i1) {
    const int tid = otid(), co = tid & 7, ksl = tid >> 3, col = u * 64 + co * 8;
    LAS float* red = (LAS float*)lds;
    v4u xa[2], xb[2];
#pragma unroll
    for (int q = 0; q < 2; ++q) { xa[q] = *(const v4u*)(p.x + i0 * D_ + ksl * 16 + q * 8); xb[q] = *(const v4u*)(p.x + i1 * D_ + ksl * 16 + q * 8); }
    float a[4][8] = {};
#pragma unroll
    for (int bq = 0; bq < 16; bq += 16) {
        v4u wg[16], wu[16];
#pragma unroll
        for (int q = 0; q < 16; ++q) { wg[q] = *(const v4u*)(p.wg + (size_t)(ksl * 16 + bq + q) * F_ + col); wu[q] = *(const v4u*)(p.wu + (size_t)(ksl * 16 + bq + q) * F_ + col); }
#pragma unroll
        for (int q = 0; q < 16; ++q) {
            const unsigned ua = xa[(bq + q) >> 3][((bq + q) >> 1) & 3], ub = xb[(bq + q) >> 3][((bq + q) >> 1) & 3];
            const float x0 = ((bq + q) & 1) ? hi16(ua) : lo16(ua), x1 = ((bq + q) & 1) ? hi16(ub) : lo16(ub);
#pragma unroll
            for (int e = 0; e < 4; ++e) {
                a[0][2 * e] += x0 * lo16(wg[q][e]); a[0][2 * e + 1] += x0 * hi16(wg[q][e]); a[1][2 * e] += x1 * lo16(wg[q][e]); a[1][2 * e + 1] += x1 * hi16(wg[q][e]);
                a[2][2 * e] += x0 * lo16(wu[q][e]); a[2][2 * e + 1] += x0 * hi16(wu[q][e]); a[3][2 * e] += x1 * lo16(wu[q][e]); a[3][2 * e + 1] += x1 * hi16(wu[q][e]);
            }
        }
        asm volatile("" ::: "memory"); SB();
    }
#pragma unroll
    for (int q = 0; q < 4; ++q)
#pragma unroll
        for (int e = 0; e < 8; ++e) red[(ksl * 4 + q) * 64 + co * 8 + e] = a[q][e];
    BAR();
    if (tid < 128) {
        const int t = tid >> 6, c = tid & 63; float g = 0.f, uu = 0.f;
        for (int k = 0; k < 64; ++k) { g += red[(k * 4 + t) * 64 + c]; uu += red[(k * 4 + 2 + t) * 64 + c]; }
        g = rnd(g); uu = rnd(uu); g = rnd(g / (1.f + __expf(-g)));
        __hip_atomic_store((GAS float*)(p.ws + O_FH) + t * F_ + u * 64 + c, rnd(g * uu), RLX_AGENT);
    }
    announce((GAS unsigned*)(p.ws + O_CTL) + C_F1);
}
DEVI void ffn2_unit(const Params& p, LAS unsigned char* lds, int u, bool& dead) {
    const int tid = otid(), co = tid & 3, ksl = tid >> 2, col = u * 32 + co * 8;
    GAS unsigned* ctl = (GAS unsigned*)(p.ws + O_CTL);
    LAS float* red = (LAS float*)lds;
    v4u w[28];
#pragma unroll
    for (int q = 0; q < 28; ++q) w[q] = *(const v4u*)(p.wd + (size_t)(ksl * 28 + q) * D_ + col);
    wait_ge(ctl + C_F1, 56u, ctl, dead);
    const __amdgpu_buffer_rsrc_t rs = __builtin_amdgcn_make_buffer_rsrc(p.ws, 0, 0xFFFFFF00u, 0x00020000);
    v4u h0[7], h1[7];
#pragma unroll
    for (int q = 0; q < 7; ++q) { h0[q] = ld16(rs, ksl * 112 + q * 16, O_FH); h1[q] = ld16(rs, ksl * 112 + q * 16, O_FH + F_ * 4); }
    float a[2][8] = {};
#pragma unroll
    for (int q = 0; q < 28; ++q) {
        const float x0 = __uint_as_float(h0[q >> 2][q & 3]), x1 = __uint_as_float(h1[q >> 2][q & 3]);
#pragma unroll
        for (int e = 0; e < 4; ++e) { a[0][2 * e] += x0 * lo16(w[q][e]); a[0][2 * e + 1] += x0 * hi16(w[q][e]); a[1][2 * e] += x1 * lo16(w[q][e]); a[1][2 * e + 1] += x1 * hi16(w[q][e]); }
    }
#pragma unroll
    for (int q = 0; q < 2; ++q)
#pragma unroll
        for (int e = 0; e < 8; ++e) red[(ksl * 2 + q) * 32 + co * 8 + e] = a[q][e];
    BAR();
    if (tid < 64) {
        const int t = tid >> 5, c = tid & 31; float y = 0.f;
        for (int k = 0; k < 128; ++k) y += red[(k * 2 + t) * 32 + c];
        __hip_atomic_store((GAS float*)(p.ws + O_FY) + t * D_ + u * 32 + c, rnd(y), RLX_AGENT);
    }
    announce(ctl + C_F2);
}

DEVI void wo_fast_unit(const Params& p, LAS unsigned char* lds, int ct, int k8, int i0, int i1, unsigned fpar, bool& dead) {
    const int tid = otid(), lane = tid & 63, wave = __builtin_amdgcn_readfirstlane(tid >> 6), fr = lane & 15, fg = lane >> 4;
    GAS unsigned* ctl = (GAS unsigned*)(p.ws + O_CTL);
    const __amdgpu_buffer_rsrc_t rs = __builtin_amdgcn_make_buffer_rsrc(p.ws, 0, 0xFFFFFF00u, 0x00020000);
    {
        const unsigned lbase = (unsigned)(uintptr_t)lds + wave * 2048;
        const u16* gb = p.wo + (size_t)k8 * 512 * D_ + ct * 128;
#pragma unroll
        for (int q = 0; q < 2; ++q) {
            const int k = wave * 8 + (lane >> 4) + 4 * q;
            const int pk = (k & 32) + 16 * ((k >> 2) & 1) + 4 * ((k >> 3) & 3) + (k & 3), f = (k & 3) | (((k >> 3) & 1) << 2);
            const unsigned vb = (pk * D_ + ((((lane & 15) >> 1) ^ f) * 16 + (lane & 1) * 8)) * 2;
#pragma unroll
            for (int kt = 0; kt < 8; ++kt) glds(vb, gb + (size_t)kt * 64 * D_, lbase + kt * 16384 + q * 1024);
        }
    }
    wait_ge(ctl + C_INIT, 128u, ctl, dead);
    asm volatile("s_waitcnt vmcnt(0)\n\ts_barrier" ::: "memory");
    const int mt = wave & 1, np = wave >> 1;
    v4u bf[16][2];
    {
        const int q4 = fr >> 2, p4 = fr & 3, fsw = q4 | ((fg & 1) << 2);
        const unsigned bpo = (8 * fg + q4) * 256 + p4 * 8 + ((fsw ^ (2 * np)) * 32);
#pragma unroll
        for (int q = 0; q < 16; ++q)
#pragma unroll
            for (int n = 0; n < 2; ++n) { LAS unsigned char* bp = lds + (bpo ^ (n * 32)) + q * 8192; const v2u x = trrd(bp), y = trrd(bp + 1024); bf[q][n] = (v4u){x.x, x.y, y.x, y.y}; }
    }
    BAR();
    const int col = ct * 128 + k8 * 16 + fg * 4;
    v4u t[8]; float gv; v2u xv;
    auto request = [&](int sg) {
        const int row = sg * 32 + mt * 16 + fr;
#pragma unroll
        for (int q = 0; q < 8; ++q) t[q] = ld16(rs, lane * 16 + q * 1024, O_S2 + fb((((sg & 7) * 8 + ct) * 2 + mt) * 8 + k8, fpar));
        gv = ((const float*)(p.ws + O_G))[row];
        xv = *(const v2u*)(p.x + row * D_ + col);
    };
    auto finish = [&](int sg) {
        const int row = sg * 32 + mt * 16 + fr;
        for (unsigned spins = 0;;) {
            v4u an = t[0], orr = t[0];
#pragma unroll
            for (int q = 1; q < 8; ++q) { an &= t[q]; orr |= t[q]; }
            const unsigned bad = (sg & 8) ? ~(an.x & an.y & an.z & an.w) : (orr.x | orr.y | orr.z | orr.w);
            if (__builtin_amdgcn_ballot_w64((bad & 1u) != 0u) == 0 || dead) break;
            if (poll_fail(spins, ctl, 64)) dead = true;
            asm volatile("" ::: "memory");
            request(sg);
        }
        float bb[4] = {lo16(xv.x), hi16(xv.x), lo16(xv.y), hi16(xv.y)};
        if ((i0 >> 5) == sg || (i1 >> 5) == sg) wait_ge(ctl + C_F2, 32u, ctl, dead);
        if (row == i0 || row == i1) {
            const float rs_ = rnd(1.f / (1.f + __expf(-((const float*)(p.ws + O_R))[row])));
#pragma unroll
            for (int e = 0; e < 4; ++e) bb[e] = rnd(bb[e] + rnd(__hip_atomic_load((GAS float*)(p.ws + O_FY) + (row == i0 ? 0 : D_) + col + e, RLX_AGENT) * rs_));
        }
        f32x4 a = __builtin_bit_cast(f32x4, t[0]);
#pragma unroll
        for (int q = 1; q < 8; ++q) a += __builtin_bit_cast(f32x4, t[q]);
        const float og = rnd(1.f - gv);
        float o[4];
#pragma unroll
        for (int e = 0; e < 4; ++e) o[e] = rnd(gv * rnd(a[e])) + rnd(og * bb[e]);
        *(v2u*)(p.out + row * D_ + col) = (v2u){pk2(o[0], o[1]), pk2(o[2], o[3])};
    };
    int ready = 0;
    const v4u rd = {(unsigned)(uintptr_t)p.ws, (unsigned)((uintptr_t)p.ws >> 32) & 0xffffu, 0xFFFFFF00u, 0x00020000u};
    v4u ar0, ar1, ar2, ar3;
    const unsigned va0 = lane * 16, va1 = va0 + 16384, va2 = va0 + 32768, va3 = va0 + 49152;
    auto aload = [&](int sg) {
        const unsigned so = O_OX + fb(sg * 32 + wave * 4, fpar) + k8 * 1024u;
        ALD16(ar0, va0, so, 0); ALD16(ar1, va1, so, 0); ALD16(ar2, va2, so, 0); ALD16(ar3, va3, so, 0);
    };
    LAS unsigned char* const aw = lds + wave * 4096;
    const unsigned sw0 = (lane ^ ((wave * 4) & 15)) * 16;
    for (int seg = 0; seg < NSEG; ++seg) {
        if (seg >= ready) {
            if (wave == ((2 * seg + 4) & 7)) {
                unsigned long long ok;
                for (unsigned spins = 0;;) {
                    const unsigned c = seg + lane < NSEG ? __hip_atomic_load(ctl + C_OF + 16 * ((seg + lane) * 8 + k8), RLX_AGENT) : 0u;
                    ok = __builtin_amdgcn_ballot_w64(c >= 4u);
                    if ((ok & 1ull) || dead) break;
                    if (poll_fail(spins, ctl, 64)) dead = true;
                }
                if (lane == 0) *(LAS int*)(lds + 65536) = seg + max(1, (int)__builtin_ctzll(~ok));
            }
            BAR();
            ready = __builtin_amdgcn_readfirstlane(*(LAS int*)(lds + 65536));
            aload(seg);
            asm volatile("s_waitcnt vmcnt(0)" ::: "memory");
        }
        WAITV2(2, ar0, ar1); WAITV2(2, ar2, ar3);
        LAS unsigned char* img = lds + (seg & 1) * 32768;
        { LAS unsigned char* w_ = aw + (seg & 1) * 32768;
          *(LAS v4u*)(w_ + sw0) = ar0; *(LAS v4u*)(w_ + 1024 + (sw0 ^ 16)) = ar1; *(LAS v4u*)(w_ + 2048 + (sw0 ^ 32)) = ar2; *(LAS v4u*)(w_ + 3072 + (sw0 ^ 48)) = ar3; }
        asm volatile("s_waitcnt lgkmcnt(0)" ::: "memory");
        SB();
        if (seg >= 3 && np == ((seg - 3) & 3)) finish(seg - 3);
        if (seg >= 2 && np == ((seg - 2) & 3)) request(seg - 2);
        if (seg + 1 < ready) aload(seg + 1);
        BAR();
        f32x4 c0 = {0, 0, 0, 0}, c1 = c0;
        {
            v4u af[2][4];
            LAS unsigned char* ap = img + (mt * 16 + fr) * 1024;
#pragma unroll
            for (int q = 0; q < 4; ++q) af[0][q] = *(LAS v4u*)(ap + (((q * 4 + fg) ^ fr) * 16));
#pragma unroll
            for (int g = 0; g < 4; ++g) {
                if (g < 3) {
#pragma unroll
                    for (int q = 0; q < 4; ++q) af[(g + 1) & 1][q] = *(LAS v4u*)(ap + (g + 1) * 256 + (((q * 4 + fg) ^ fr) * 16));
                }
                SB();
#pragma unroll
                for (int q = 0; q < 4; ++q) { c0 = mfma32(bf[g * 4 + q][0], af[g & 1][q], c0); c1 = mfma32(bf[g * 4 + q][1], af[g & 1][q], c1); }
                SB();
            }
        }
        const unsigned sb2 = O_S2 + fb((((seg & 7) * 8 + ct) * 2 + mt) * 8 + 2 * np, fpar) + k8 * 1024u;
        const unsigned tg = (seg >> 3) & 1u;
        st16((__builtin_bit_cast(v4u, c0) & ~1u) | tg, rs, lane * 16, sb2);
        st16((__builtin_bit_cast(v4u, c1) & ~1u) | tg, rs, lane * 16, sb2 + 16384u);
    }
    if (np == 1) finish(NSEG - 3);
    if (np >= 2) { if (np == 3) __builtin_amdgcn_s_sleep(5); request(NSEG - 4 + np); finish(NSEG - 4 + np); }
}

constexpr int Q_F1 = 0, Q_F2 = Q_F1 + 14, Q_QA = Q_F2 + 8, Q_K2 = Q_QA + 16, Q_WF = Q_K2 + 16, Q_Q2 = Q_WF + 16, Q_END = Q_Q2 + 48;
DEVI void worker_body(const Params& p, LAS unsigned char* lds, int w) {
    const int tid = otid(), lane = tid & 63;
    GAS unsigned* ctl = (GAS unsigned*)(p.ws + O_CTL);
    const unsigned sidemask = side_mask(ctl, lane);
    bool dead = false;
    const unsigned fpar = far_par(sidemask);
    const int xq = w >> 5, hm = xq * 16 + ((w & 31) >> 1);
    kv_unit<64, 128>(p, lds, (w & 1) * 64, hm >> 1, hm & 1, fpar);
    kv_unit<128, 128>(p, lds, 128 + (w & 1) * 128, hm >> 1, hm & 1, fpar);
    kv_unit<256, 128>(p, lds, 384 + (w & 1) * 256, hm >> 1, hm & 1, fpar);
    { const int hp = xq * 8 + ((w & 31) >> 2); kv_unit<256, 256>(p, lds, 896 + (w & 3) * 256, (hp >> 1) * 2, hp & 1, fpar); }
    {
        const __amdgpu_buffer_rsrc_t rs = __builtin_amdgcn_make_buffer_rsrc(p.ws, 0, 0xFFFFFF00u, 0x00020000);
        const v4u zz = {SENT, SENT, SENT, SENT};
#pragma unroll
        for (int q = 0; q < 8; ++q) st16(zz, rs, otid() * 16, O_S2 + fb(w * 8 + q, fpar));
        announce(ctl + C_INIT);
    }
    int i0, i1; top2((const float*)(p.ws + O_R), i0, i1);
    for (;;) {
        BAR();
        if (tid == 0) *(LAS unsigned*)(lds + 147456) = __hip_atomic_fetch_add(ctl + C_QUEUE + 16 * xq, 1u, RLX_AGENT);
        BAR();
        int t = __builtin_amdgcn_readfirstlane(*(LAS unsigned*)(lds + 147456));
        if (t >= Q_END) break;
        if (t < Q_F2) ffn1_unit(p, lds, xq * 14 + t - Q_F1, i0, i1);
        else if (t < Q_QA) ffn2_unit(p, lds, xq * 8 + t - Q_F2, dead);
        else if (t >= Q_K2 && t < Q_WF) kv_unit<128, 128>(p, lds, 1920, xq * 8 + ((t - Q_K2) >> 1), t & 1, fpar);
        else if (t >= Q_WF && t < Q_Q2) wo_fast_unit(p, lds, (t - Q_WF) >> 1, 2 * xq + ((t - Q_WF) & 1), i0, i1, fpar, dead);
        else { const int qa = t < Q_K2 ? t - Q_QA : t - Q_Q2 + 16; qa_unit(p, lds, qa >> 3, xq * 8 + (qa & 7), sidemask, dead); }
    }
}

__global__ void __launch_bounds__(512, 2) k_main(Params p) {
    extern __shared__ __attribute__((aligned(16))) unsigned char smem[];
    LAS unsigned char* lds = (LAS unsigned char*)smem;
    if (threadIdx.x == 0) *(LAS int*)(lds + 147456) = claim_slot((GAS unsigned*)(p.ws + O_CTL));
    __syncthreads();
    const int slot = __builtin_amdgcn_readfirstlane(*(LAS int*)(lds + 147456));
    if (slot < 128) chain_body(p, lds, slot >> 5, slot & 31);
    else worker_body(p, lds, slot - 128);
}

extern "C" void kernel_launch(void* const* d_in, const int* in_sizes, int n_in, void* d_out, int out_size, void* d_ws, size_t ws_size, hipStream_t stream) {
    Params p;
    p.x = (const u16*)d_in[0]; p.mem0 = (const u16*)d_in[1]; p.wq = (const u16*)d_in[2]; p.wk = (const u16*)d_in[3]; p.wv = (const u16*)d_in[4]; p.wo = (const u16*)d_in[5];
    p.wr = (const u16*)d_in[8]; p.wg = (const u16*)d_in[9]; p.wu = (const u16*)d_in[10]; p.wd = (const u16*)d_in[11]; p.gw = (const u16*)d_in[12];
    p.out = (u16*)d_out;
    p.ws = (unsigned char*)(((uintptr_t)d_ws + PAGE - 1) & ~(uintptr_t)(PAGE - 1));
    constexpr int LDS_M = 147456 + 64;
    (void)hipFuncSetAttribute((const void*)k_main, hipFuncAttributeMaxDynamicSharedMemorySize, LDS_M);
    hipLaunchKernelGGL(k_prep, dim3(256), dim3(320), 0, stream, p);
    hipLaunchKernelGGL(k_main, dim3(256), dim3(512), LDS_M, stream, p);
}
```

```cpp
#include <hip/hip_runtime.h>
#include <hip/hip_bf16.h>
#include <stdint.h>

typedef unsigned short u16;
typedef unsigned v4u __attribute__((ext_vector_type(4)));
typedef unsigned v2u __attribute__((ext_vector_type(2)));
typedef unsigned v8u __attribute__((ext_vector_type(8)));
typedef float f32x4 __attribute__((ext_vector_type(4)));
typedef __bf16 bf16x2 __attribute__((ext_vector_type(2)));
typedef __bf16 bf16x8 __attribute__((ext_vector_type(8)));
typedef __bf16 bf16x16 __attribute__((ext_vector_type(16)));
typedef short s16x4 __attribute__((ext_vector_type(4)));
#define GAS __attribute__((address_space(1)))
#define LAS __attribute__((address_space(3)))
#define RLX_AGENT __ATOMIC_RELAXED, __HIP_MEMORY_SCOPE_AGENT
#define DEVI __device__ __forceinline__
#define BAR() asm volatile("s_waitcnt lgkmcnt(0)\n\ts_barrier" ::: "memory")

constexpr int D_ = 1024, NSEG = 64, F_ = 3584;
constexpr float SCALE = 0.08838834764831845f, LOG2E = 1.4426950408889634f;
constexpr unsigned SENT = 0xFFFFFFFFu;
constexpr unsigned PAGE = 2u << 20;
constexpr unsigned O_CTL = 0, O_R = 65536, O_G = O_R + 8192, O_FH = 131072, O_FY = O_FH + 32768;
constexpr unsigned P_HAND = 1, P_PART = 17;
constexpr unsigned O_KX = 22 * PAGE, O_VX = 38 * PAGE, O_OX = 54 * PAGE, O_S2 = 70 * PAGE  ;
constexpr unsigned L_QKV = 65536;
constexpr int C_ABORT = 0, C_ARRIVE = 64, C_T0 = 128, C_QUEUE = 192, C_XR = 256  , C_F1 = 768, C_F2 = 800, C_INIT = 832, C_SLOT = 1024  ,
              C_KV = 2048  , C_PROG = 6144  , C_OF = 7168  , C_ZEND = 15360,
              C_SIDE = 15360  ;

struct Params { const u16 *x, *mem0, *wq, *wk, *wv, *wo, *wr, *wg, *wu, *wd, *gw; u16* out; unsigned char* ws; };
constexpr int DQ = 650, D3 = 800, D1 = 700, T0_LEAD = 120;

DEVI float bf2f(u16 b) { return __uint_as_float((unsigned)b << 16); }
DEVI unsigned pk2(float lo, float hi) { bf16x2 v = {(__bf16)lo, (__bf16)hi}; return __builtin_bit_cast(unsigned, v); }
DEVI float rnd(float f) { return __uint_as_float(pk2(f, 0.f) << 16); }
DEVI float lo16(unsigned u) { return __uint_as_float(u << 16); }
DEVI float hi16(unsigned u) { return __uint_as_float(u & 0xffff0000u); }
DEVI int otid() { int t = threadIdx.x; asm volatile("" : "+v"(t)); return t; }
DEVI unsigned xcc_id() { return (unsigned)__builtin_amdgcn_s_getreg(20 | (3 << 11)) & 0xfu; }
DEVI unsigned rt() { return (unsigned)__builtin_amdgcn_s_memrealtime(); }
DEVI unsigned amap(unsigned lo) { return ((lo >> 13) << 14) + (lo & 8191); }
DEVI unsigned fb(unsigned blk, unsigned par) { return blk * 16384u + (((par ^ (blk >> 7)) & 1u) << 13); }
DEVI unsigned far_par(unsigned sidemask) { return (sidemask & 1u) ^ 1u; }
DEVI unsigned side_mask(GAS unsigned* ctl, int lane) {
    const v4u v = *(GAS v4u*)(ctl + C_SIDE + lane * 4);
    int n = 0;
#pragma unroll
    for (int e = 0; e < 4; ++e) n += __builtin_popcountll(__builtin_amdgcn_ballot_w64((v[e] & 1u) != 0u));
    return n > 128 ? 0x55555555u : 0xAAAAAAAAu;
}
DEVI v4u ld16(__amdgpu_buffer_rsrc_t r, unsigned vo, unsigned so) { return __builtin_amdgcn_raw_buffer_load_b128(r, vo, so, 16); }
DEVI void st16(v4u v, __amdgpu_buffer_rsrc_t r, unsigned vo, unsigned so) {
    __builtin_amdgcn_raw_buffer_store_b128(v, r, vo, so, 16);
    asm volatile("s_nop 1" :: "v"(v));
}
DEVI void st8(v2u v, __amdgpu_buffer_rsrc_t r, unsigned vo, unsigned so) { __builtin_amdgcn_raw_buffer_store_b64(v, r, vo, so, 16); }
DEVI void st4(unsigned v, __amdgpu_buffer_rsrc_t r, unsigned vo, unsigned so) { __builtin_amdgcn_raw_buffer_store_b32(v, r, vo, so, 16); }
DEVI unsigned umax3(unsigned a, unsigned b, unsigned c) { unsigned r; asm("v_max3_u32 %0, %1, %2, %3" : "=v"(r) : "v"(a), "v"(b), "v"(c)); return r; }
DEVI unsigned mx4(v4u v, unsigned m) { return umax3(umax3(v.x, v.y, m), v.z, v.w); }
DEVI bool any_sent(unsigned m) { return __builtin_amdgcn_ballot_w64(m == SENT) != 0; }
DEVI unsigned nosent(unsigned u) { return u < 0xFFFFFFFEu ? u : 0xFFFFFFFEu; }
DEVI f32x4 mfma32(v4u a, v4u b, f32x4 c) { return __builtin_amdgcn_mfma_f32_16x16x32_bf16(__builtin_bit_cast(bf16x8, a), __builtin_bit_cast(bf16x8, b), c, 0, 0, 0); }
DEVI f32x4 mfma16(v2u a, v2u b, f32x4 c) { return __builtin_amdgcn_mfma_f32_16x16x16bf16_1k(__builtin_bit_cast(s16x4, a), __builtin_bit_cast(s16x4, b), c, 0, 0, 0); }
DEVI f32x4 smf(v4u a, v8u b, f32x4 c, int idx) { return __builtin_amdgcn_smfmac_f32_16x16x64_bf16(__builtin_bit_cast(bf16x8, a), __builtin_bit_cast(bf16x16, b), c, idx, 0, 0); }
DEVI v2u trrd(LAS unsigned char* p) { return __builtin_bit_cast(v2u, __builtin_amdgcn_ds_read_tr16_b64_v4i16((LAS s16x4*)p)); }
DEVI float ror8(float v) { return __int_as_float(__builtin_amdgcn_update_dpp(0, __float_as_int(v), 0x128, 0xf, 0xf, false)); }
DEVI float sw32sum(float a, float b) { auto r = __builtin_amdgcn_permlane32_swap(__float_as_uint(a), __float_as_uint(b), false, false); return __uint_as_float(r[0]) + __uint_as_float(r[1]); }
DEVI float sw16sum(float a, float b) { auto r = __builtin_amdgcn_permlane16_swap(__float_as_uint(a), __float_as_uint(b), false, false); return __uint_as_float(r[0]) + __uint_as_float(r[1]); }
DEVI float sw32max(float a) { auto r = __builtin_amdgcn_permlane32_swap(__float_as_uint(a), __float_as_uint(a), false, false); return fmaxf(__uint_as_float(r[0]), __uint_as_float(r[1])); }
DEVI float sw16max(float a) { auto r = __builtin_amdgcn_permlane16_swap(__float_as_uint(a), __float_as_uint(a), false, false); return fmaxf(__uint_as_float(r[0]), __uint_as_float(r[1])); }
DEVI f32x4 fold(f32x4 a, f32x4 b) { f32x4 r; r.x = sw32sum(a.x, b.x); r.y = sw32sum(a.y, b.y); r.z = sw32sum(a.z, b.z); r.w = sw32sum(a.w, b.w); return r; }
DEVI float max5x8(float a, float b, float c, float d, float e) {
    float m, t;
    asm("v_max3_f32 %0, %2, %3, %4\n\tv_max3_f32 %0, %0, %5, %6\n\ts_nop 1\n\t"
        "v_max_f32_dpp %0, %0, %0 row_ror:8 row_mask:0xf bank_mask:0xf\n\tv_mov_b32 %1, %0\n\ts_nop 1\n\t"
        "v_permlane16_swap_b32 %0, %1\n\tv_max_f32 %0, %0, %1\n\tv_mov_b32 %1, %0\n\ts_nop 1\n\t"
        "v_permlane32_swap_b32 %0, %1\n\tv_max_f32 %0, %0, %1"
        : "=&v"(m), "=&v"(t) : "v"(a), "v"(b), "v"(c), "v"(d), "v"(e));
    return m;
}
#define ALD16(D, VO, SO, IMM) asm volatile("buffer_load_dwordx4 %0, %1, %2, %3 offen offset:" #IMM " sc1" : "=v"(D) : "v"(VO), "s"(rd), "s"(SO) : "memory")
#define ALD4(D, VO, SO, IMM) asm volatile("buffer_load_dword %0, %1, %2, %3 offen offset:" #IMM " sc1" : "=v"(D) : "v"(VO), "s"(rd), "s"(SO) : "memory")
#define WAITV1(N, A) asm volatile("s_waitcnt vmcnt(" #N ")" : "+v"(A) :: "memory")
#define WAITV2(N, A, B) asm volatile("s_waitcnt vmcnt(" #N ")" : "+v"(A), "+v"(B) :: "memory")
#define SB() __builtin_amdgcn_sched_barrier(0)
#define PIN2(a, b) asm volatile("" : "+v"(a), "+v"(b))
DEVI unsigned mt() { return (unsigned)__builtin_amdgcn_s_memtime(); }
DEVI void wait_l(unsigned t) {
    unsigned d;
    asm volatile("1:\n\ts_memrealtime vcc\n\ts_waitcnt lgkmcnt(0)\n\ts_sub_i32 %0, vcc_lo, %1\n\ts_cmp_lt_i32 %0, 0\n\ts_cbranch_scc1 1b"
                 : "=&s"(d) : "s"(__builtin_amdgcn_readfirstlane(t)) : "memory", "scc", "vcc");
}
DEVI void wait_c(unsigned t) {
    unsigned d;
    asm volatile("1:\n\ts_memtime vcc\n\ts_waitcnt lgkmcnt(0)\n\ts_sub_i32 %0, vcc_lo, %1\n\ts_cmp_lt_i32 %0, 0\n\ts_cbranch_scc1 1b"
                 : "=&s"(d) : "s"(__builtin_amdgcn_readfirstlane(t)) : "memory", "scc", "vcc");
}
constexpr unsigned SPINCAP = 40000;
DEVI bool poll_fail(unsigned& spins, GAS unsigned* ctl, int back) {
    if (back >= 128) __builtin_amdgcn_s_sleep(2); else if (back) __builtin_amdgcn_s_sleep(1);
    ++spins;
    if ((spins & 63u) == 0u) {
        if (spins > SPINCAP) { __hip_atomic_store(ctl + C_ABORT, 1u, RLX_AGENT); return true; }
        if (__builtin_amdgcn_readfirstlane(__hip_atomic_load(ctl + C_ABORT, RLX_AGENT))) return true;
    }
    return false;
}

DEVI void wait_ge(GAS unsigned* w, unsigned v, GAS unsigned* ctl, bool& dead) {
    unsigned spins = 0;
    while (!dead && (unsigned)__builtin_amdgcn_readfirstlane(__hip_atomic_load(w, RLX_AGENT)) < v) { if (poll_fail(spins, ctl, 64)) dead = true; }
    asm volatile("" ::: "memory");
}

__global__ void __launch_bounds__(320) k_prep(Params p) {
    const int tid = threadIdx.x, lane = tid & 63, wave = tid >> 6, b = blockIdx.x;
    unsigned* ctl = (unsigned*)(p.ws + O_CTL);
    if (b >= 196 && tid < 256) ctl[(b - 196) * 256 + tid] = 0u;
    if (tid == 256) {
        const unsigned page = 1 + (b >> 3) % 20;
        __amdgpu_buffer_rsrc_t rs = __builtin_amdgcn_make_buffer_rsrc(p.ws, 0, 0xFFFFFF00u, 0x00020000);
        unsigned t[2] = {0xffffffffu, 0xffffffffu};
        for (int rep = 0; rep < 2; ++rep)
            for (int side = 0; side < 2; ++side) {
                const unsigned long long c0 = __builtin_amdgcn_s_memtime();
                asm volatile("s_waitcnt lgkmcnt(0)" ::: "memory");
                st4(SENT, rs, page * PAGE + 480 * 1024 + side * 8192 + (b & 7) * 512 + (b >= 160 ? 256 : 0), 0);
                asm volatile("s_waitcnt vmcnt(0)" ::: "memory");
                const unsigned long long c1 = __builtin_amdgcn_s_memtime();
                t[side] = min(t[side], (unsigned)(c1 - c0));
            }
        ctl[C_SIDE + b] = (t[1] < t[0] ? 1u : 0u) ^ (xcc_id() >= 4 ? 1u : 0u) ^ (page & 1u);
    }
    if (wave == 4) return;
    float* R = (float*)(p.ws + O_R); float* G = (float*)(p.ws + O_G);
    v4u xv[2][2], wr[2], gw[2];
#pragma unroll
    for (int q = 0; q < 2; ++q) {
        const int k = q * 512 + lane * 8;
        xv[0][q] = *(const v4u*)(p.x + (b * 8 + wave * 2) * D_ + k); xv[1][q] = *(const v4u*)(p.x + (b * 8 + wave * 2 + 1) * D_ + k);
        wr[q] = *(const v4u*)(p.wr + k); gw[q] = *(const v4u*)(p.gw + k);
    }
    float a[2] = {0.f, 0.f}, g[2] = {0.f, 0.f};
#pragma unroll
    for (int rr = 0; rr < 2; ++rr)
#pragma unroll
        for (int q = 0; q < 2; ++q)
#pragma unroll
            for (int e = 0; e < 4; ++e) {
                const unsigned u = xv[rr][q][e];
                a[rr] = fmaf(lo16(u), lo16(wr[q][e]), a[rr]); a[rr] = fmaf(hi16(u), hi16(wr[q][e]), a[rr]);
                const unsigned pr = pk2(lo16(u) * lo16(gw[q][e]), hi16(u) * hi16(gw[q][e]));
                g[rr] += lo16(pr); g[rr] += hi16(pr);
            }
#pragma unroll
    for (int m = 32; m >= 1; m >>= 1) { a[0] += __shfl_xor(a[0], m); g[0] += __shfl_xor(g[0], m); a[1] += __shfl_xor(a[1], m); g[1] += __shfl_xor(g[1], m); }
    if (lane < 2) { const int row = b * 8 + wave * 2 + lane; R[row] = rnd(lane ? a[1] : a[0]); G[row] = rnd(1.f / (1.f + __expf(-rnd(lane ? g[1] : g[0])))); }
}

constexpr unsigned LC_PA = 0, LC_QK = 24576, LC_O = LC_QK + 6144, LC_MISC = 98304 + 16, LC_KV = 33792  ;

DEVI void chain_body(const Params& p, LAS unsigned char* lds, const int g, const int i) {
    const int tid = threadIdx.x, lane = tid & 63, wave = __builtin_amdgcn_readfirstlane(tid >> 6);
    const int h = g * 8 + (i >> 2), j = i & 3, cid = h * 4 + j;
    const int kw = (wave + i) & 7;
    GAS unsigned* ctl = (GAS unsigned*)(p.ws + O_CTL);
    const __amdgpu_buffer_rsrc_t rs = __builtin_amdgcn_make_buffer_rsrc(p.ws, 0, 0xFFFFFF00u, 0x00020000);
    const unsigned sidemask = side_mask(ctl, lane);
    v8u WA[6][2], WO[2][2];
    v4u sa[2][12], so[8];
    {
        const u16* wqk = (j < 2 ? p.wq : p.wk) + h * 128 + 64 * (j & 1) + (lane & 7) * 8;
        const u16* wvv = p.wv + h * 128 + 32 * j + (lane & 3) * 8;
#pragma unroll
        for (int ks = 0; ks < 2; ++ks) {
#pragma unroll
            for (int q = 0; q < 8; ++q) sa[ks][q] = *(const v4u*)(wqk + (size_t)(128 * kw + 64 * ks + 8 * q + (lane >> 3)) * 4096);
#pragma unroll
            for (int q = 0; q < 4; ++q) sa[ks][8 + q] = *(const v4u*)(wvv + (size_t)(128 * kw + 64 * ks + 16 * q + (lane >> 2)) * 4096);
        }
#pragma unroll
        for (int q = 0; q < 8; ++q) so[q] = *(const v4u*)(p.wo + (size_t)(h * 128 + 16 * wave + 2 * q + (lane >> 5)) * 1024 + 256 * j + (lane & 31) * 8);
    }
    SB();
    const int tau = lane >> 5, rh = (lane >> 4) & 1, n16 = lane & 15, c8 = (lane >> 3) & 1, g4 = lane >> 4;
    const unsigned o_gm = amap(g * 16384 + kw * 2048 + lane * 16);
    const unsigned qkvb = L_QKV + h * 6144;
    const int pcol = 256 * j + 32 * wave + 16 * tau + n16;
    const unsigned o_pp = amap((pcol >> 3) * 4096 + h * 128 + (rh * 8 + (pcol & 7)) * 8);
    const int sidx = c8 ? 0xEEEE : 0x4444;
    LAS unsigned char* const pw = lds + LC_PA + ((wave * 2 + rh) * 96 + 16 * tau + n16) * 16;
    LAS unsigned char* const ob = lds + LC_O + (lane & 7) * 320 + c8 * 16 + g4 * 32;
    const v4u rd = {(unsigned)(uintptr_t)p.ws, (unsigned)((uintptr_t)p.ws >> 32) & 0xffffu, 0xFFFFFF00u, 0x00020000u};
    const unsigned o_i1 = amap(qkvb + j * 1024 + lane * 16);
    const unsigned o_i2 = lane < 32 ? amap(qkvb + 4096 + j * 512 + lane * 16) : amap(((lane >> 3) & 3) * 16384 + cid * 128 + (lane & 7) * 16);
    {
        const v4u zz = {SENT, SENT, SENT, SENT};
        if (wave < 2) {
            const unsigned sb = P_HAND * PAGE + wave * 524288u, sx = ((sidemask >> P_HAND) & 1u) << 13;
            st16(zz, rs, o_i1 ^ sx, sb);
            v4u iv = zz;
            if (wave == 0 && lane >= 32) {
                iv = *(const v4u*)(p.mem0 + (lane & 7) * 1024 + cid * 8);
                iv = (v4u){nosent(iv.x), nosent(iv.y), nosent(iv.z), nosent(iv.w)};
            }
            st16(iv, rs, o_i2 ^ sx, sb);
        }
#pragma unroll
        for (int sl = 0; sl < 2; ++sl) st8((v2u){SENT, SENT}, rs, o_pp ^ (((sidemask >> (P_PART + sl)) & 1u) << 13), (P_PART + sl) * PAGE);
    }
    bool dead = false;
    const unsigned o_kv = (wave & 2 ? O_VX : O_KX) + (wave & 1) * 4096 + lane * 16, fpar = far_par(sidemask);
    v4u p0, p1, p2, p3;
    {
        LAS unsigned char* img = lds + wave * 12288;
        const int wr = lane >> 2;
        LAS unsigned char* iw = img + ((wr & 9) | ((wr >> 1) & 2) | ((wr << 1) & 4)) * 64 + (((lane & 3) ^ ((wr >> 2) & 2)) * 16);
        const int gB = lane >> 4, q4 = (lane & 15) >> 2, p4 = lane & 3;
        LAS unsigned char* ir = img + (16 * (gB >> 1) + 2 * (gB & 1) + (q4 & 1) + 8 * (q4 >> 1)) * 64 + p4 * 8;
        const unsigned hs = (q4 >> 1) * 32;
        LAS unsigned char* jw = img + (lane >> 3) * 128 + (lane & 1) * 16;
        const int jc = ((lane >> 1) & 3) ^ (lane >> 5);
        LAS unsigned char* jr = img + (16 * (gB >> 1) + 4 * (gB & 1) + (q4 & 1) + 8 * (q4 >> 1)) * 128 + p4 * 8;
        const int js = (gB & 1) | ((q4 >> 1) << 1);
        auto shuffleA = [&](int ks) {
#pragma unroll
            for (int q = 0; q < 8; ++q) *(LAS v4u*)(jw + q * 1024 + ((jc ^ ((q & 1) << 1)) * 32)) = sa[ks][q];
#pragma unroll
            for (int q = 0; q < 4; ++q) *(LAS v4u*)(iw + 8192 + q * 1024) = sa[ks][8 + q];
            asm volatile("s_waitcnt lgkmcnt(0)" ::: "memory");
#pragma unroll
            for (int t = 0; t < 6; ++t)
#pragma unroll
                for (int m = 0; m < 4; ++m) {
                    const v2u r = t < 4 ? trrd(jr + ((t ^ js) * 32) + (32 * (m >> 1) + 2 * (m & 1)) * 128) : trrd(ir + 8192 + (((t & 1) * 32) ^ hs) + (32 * (m >> 1) + 4 * (m & 1)) * 64);
                    WA[t][ks][2 * m] = r.x; WA[t][ks][2 * m + 1] = r.y;
                }
            asm volatile("s_waitcnt lgkmcnt(0)" ::: "memory");
        };
        shuffleA(0);
        SB();
        asm volatile("s_waitcnt vmcnt(0)" ::: "memory");
        BAR();
        if (tid == 0) {
            if (__hip_atomic_fetch_add(ctl + C_ARRIVE, 1u, RLX_AGENT) == 127u) __hip_atomic_store(ctl + C_T0, ((rt() + T0_LEAD) & ~3u) | 0x80000000u, RLX_AGENT);
        }
        SB();
        shuffleA(1);
        SB();
        if (wave >= 4) {
            wait_ge(ctl + C_KV + 8 * h, 4u, ctl, dead);
            const unsigned so = fb(h, fpar); ALD16(p0, o_kv, so, 0); ALD16(p1, o_kv, so, 1024); ALD16(p2, o_kv, so, 2048); ALD16(p3, o_kv, so, 3072);
        }
        BAR();
#pragma unroll
        for (int q = 0; q < 8; ++q) {
            const int f = (lane >> 5) | (((q >> 1) & 3) << 1);
            *(LAS v4u*)(lds + wave * 8192 + q * 1024 + (lane >> 5) * 512 + ((((lane >> 1) & 15) ^ f) * 32) + (lane & 1) * 16) = so[q];
        }
        BAR();
        const int fo = (q4 & 1) | (js << 1);
        LAS unsigned char* orp = lds + (16 * (gB >> 1) + 4 * (gB & 1) + (q4 & 1) + 8 * (q4 >> 1)) * 512 + p4 * 8;
#pragma unroll
        for (int t = 0; t < 2; ++t)
#pragma unroll
            for (int ks = 0; ks < 2; ++ks)
#pragma unroll
                for (int m = 0; m < 4; ++m) {
                    const v2u r = trrd(orp + (((2 * wave + t) ^ fo) * 32) + (64 * ks + 32 * (m >> 1) + 2 * (m & 1)) * 512);
                    WO[t][ks][2 * m] = r.x; WO[t][ks][2 * m + 1] = r.y;
                }
    }
#define HAND_ADDR(s) do { const unsigned pg = P_HAND + ((s) >> 2); sb = pg * PAGE + ((s) & 3) * 524288u; sx = ((sidemask >> pg) & 1u) << 13; } while (0)
#define RETRY(RELOAD, BAD)    do { unsigned spins = 0;                                                                    \
                while (!dead) { if (poll_fail(spins, ctl, 0)) { dead = true; break; } RELOAD; if (!(BAD)) break; } } while (0)
#define ZERO_A() f32x4 c0 = {0, 0, 0, 0}, c1 = c0, c2 = c0, c3 = c0, c4 = c0, c5 = c0; PIN2(c0, c1); PIN2(c2, c3); PIN2(c4, c5)
#define GATHER_MEM(EXTRA, NX) v4u a0, a1;                                                                                 \
            ALD16(a0, o_gmx, sb, 0); ALD16(a1, o_gmx, sb, 1024); EXTRA; WAITV2(NX, a0, a1);                               \
            if (__builtin_expect(any_sent(mx4(a1, mx4(a0, 0u))), 0))                                                      \
                RETRY(ALD16(a0, o_gmx, sb, 0); ALD16(a1, o_gmx, sb, 1024); WAITV2(0, a0, a1), any_sent(mx4(a1, mx4(a0, 0u))))
#define QK_MFMA() do {                                                                                                    \
            c0 = smf(a0, WA[0][0], c0, sidx); c1 = smf(a0, WA[1][0], c1, sidx); c0 = smf(a1, WA[0][1], c0, sidx); c1 = smf(a1, WA[1][1], c1, sidx); \
            SB();                                                                                                         \
            c2 = smf(a0, WA[2][0], c2, sidx); c3 = smf(a0, WA[3][0], c3, sidx); c2 = smf(a1, WA[2][1], c2, sidx); c3 = smf(a1, WA[3][1], c3, sidx); \
            SB(); *(LAS f32x4*)pw = fold(c0, c1); SB(); *(LAS f32x4*)(pw + 512) = fold(c2, c3); SB(); } while (0)
#define V_MFMA() do { c4 = smf(a0, WA[4][0], c4, sidx); c5 = smf(a0, WA[5][0], c5, sidx); c4 = smf(a1, WA[4][1], c4, sidx); c5 = smf(a1, WA[5][1], c5, sidx); PIN2(c4, c5); } while (0)
#define V_PUT() do { *(LAS f32x4*)(pw + 1024) = fold(c4, c5);                                                             \
            if (lane == 0) __hip_atomic_fetch_add((LAS unsigned*)(lds + LC_MISC + 4), 1u, __ATOMIC_RELAXED, __HIP_MEMORY_SCOPE_WORKGROUP); } while (0)
#define V_PART() do { V_MFMA(); V_PUT(); } while (0)
#define ZERO_D() f32x4 d0 = {0, 0, 0, 0}, d1 = d0; const unsigned pp = P_PART + (s & 3), px = ((sidemask >> pp) & 1u) << 13; unsigned o_ppx = o_pp ^ px; PIN2(d0, d1); asm volatile("" : "+v"(o_ppx))
#define WO_STAGE() do {                                                                                                   \
            const v4u o0 = *(LAS v4u*)ob, o1 = *(LAS v4u*)(ob + 128); SB();                                               \
            d0 = smf(o0, WO[0][0], d0, sidx); d1 = smf(o0, WO[1][0], d1, sidx); d0 = smf(o1, WO[0][1], d0, sidx); d1 = smf(o1, WO[1][1], d1, sidx); \
            const f32x4 f = fold(d0, d1);                                                                                 \
            st8((v2u){nosent(pk2(f.x, f.y)), nosent(pk2(f.z, f.w))}, rs, o_ppx, pp * PAGE); } while (0)
#define REDUCE4(LP, OFF) do {                                                                                             \
            const f32x4 r0 = *(LAS f32x4*)(LP), r1 = *(LAS f32x4*)((LP) + 3072), r2 = *(LAS f32x4*)((LP) + 6144), r3 = *(LAS f32x4*)((LP) + 9216); \
            const f32x4 t = (r0 + r1) + (r2 + r3);                                                                        \
            st4(nosent(pk2(sw32sum(t.x, t.z), sw32sum(t.y, t.w))), rs, (OFF), sb); } while (0)
    unsigned sb, sx;
    if (wave < 4) {
        const unsigned o_gq = amap(qkvb + wave * 1024 + lane * 16);
        const unsigned o_gp = amap(cid * 4096 + lane * 16);
        const int crow = 4 * ((lane >> 2) & 1) + g4;
        v4u sel = {0u, 0u, 0u, 0u};
        sel[(lane & 7) >> 1] = (lane & 1) ? 0x3f800000u : 0x00003f80u;
        const unsigned o_pm = amap(c8 * 16384 + cid * 128 + crow * 16 + (lane & 3) * 4);
        const unsigned o_gv = amap(qkvb + 4096 + wave * 512 + n16 * 16 + (4 * (g4 & 1) + 2 * (g4 >> 1)) * 2);
        LAS unsigned char* const qb = lds + LC_QK + (4 * g4 + ((lane & 15) >> 2)) * 16 + (lane & 1) * 8;
        LAS unsigned char* const qw = lds + LC_QK + wave * 1024 + lane * 16;
        LAS unsigned char* const ow = lds + LC_O + (lane & 7) * 320 + (32 * wave + 16 * c8 + 4 * g4) * 2;
        if (tid == 0) *(LAS unsigned*)(lds + LC_MISC + 4) = 0u;
        BAR();
        if (wave == 0) {
            unsigned plan, spins = 0;
            for (;;) { plan = __builtin_amdgcn_readfirstlane(__hip_atomic_load(ctl + C_T0, RLX_AGENT)); if (plan) break; if (poll_fail(spins, ctl, 64)) { dead = true; break; } }
            { const unsigned now = rt(); plan = now + ((int)((plan - now) << 1) >> 1); }
            if (!dead) wait_l(plan);
        }
        v4u kx[8], vx[2];
        unsigned tq;
#define A_STAGE_A(S1)                                                                                                     \
            BAR();                                                                                  \
            GATHER_MEM({ LAS unsigned char* kvb = lds + LC_KV + ((S1) & 1) * 16384 + lane * 16;                           \
                _Pragma("unroll") for (int q = 0; q < 8; ++q) kx[q] = *(LAS v4u*)(kvb + q * 1024);                        \
                vx[0] = *(LAS v4u*)(kvb + 8192 + wave * 2048); vx[1] = *(LAS v4u*)(kvb + 8192 + wave * 2048 + 1024); SB(); }, 0); \
            QK_MFMA();                                                                                                    \
            BAR();                                                                                                 \
            tq = mt() + DQ;                                                                                          \
            V_PART()
        {
            HAND_ADDR(0);
            ZERO_A();
            const unsigned o_gmx = o_gm ^ sx;
            A_STAGE_A(0);
        }
        for (int s = 0; s < NSEG - 1; ++s) {
            __builtin_amdgcn_s_setprio(3);
            v4u gq;
            ZERO_D();
            unsigned o_gqx = o_gq ^ sx, o_gvx = o_gv ^ sx; PIN2(o_gqx, o_gvx);
            wait_c(tq);
            ALD16(gq, o_gqx, sb, 0); WAITV1(0, gq);
            *(LAS v4u*)qw = gq;
            SB();
            if (__builtin_expect(any_sent(mx4(gq, 0u)), 0)) { RETRY(ALD16(gq, o_gqx, sb, 0); WAITV1(0, gq), any_sent(mx4(gq, 0u))); *(LAS v4u*)qw = gq; }
            BAR();
            {
                unsigned va, vc;
                ALD4(va, o_gvx, sb, 0); ALD4(vc, o_gvx, sb, 256);
                v4u qf[4], kf[4];
#pragma unroll
                for (int c = 0; c < 4; ++c) { const v2u qa = trrd(qb + c * 512), qc = trrd(qb + c * 512 + 256); qf[c] = (v4u){qa.x, qa.y, qc.x, qc.y}; }
#pragma unroll
                for (int c = 0; c < 4; ++c) { const v2u ka = trrd(qb + c * 512 + 2048), kc = trrd(qb + c * 512 + 2304); kf[c] = (v4u){ka.x, ka.y, kc.x, kc.y}; }
                SB();
                f32x4 s0 = {0, 0, 0, 0}, s1 = s0, sm = s0;
#pragma unroll
                for (int c = 0; c < 4; ++c) { s0 = mfma32(kx[c], qf[c], s0); s1 = mfma32(kx[4 + c], qf[c], s1); sm = mfma32(kf[c], qf[c], sm); }
                SB();
                float x0 = c8 ? s1.x : s0.x, x1 = c8 ? s1.y : s0.y, x2 = c8 ? s1.z : s0.z, x3 = c8 ? s1.w : s0.w;
                float xm = tau ? (c8 ? sm.w : sm.z) : (c8 ? sm.y : sm.x);
                constexpr float CS = SCALE * LOG2E;
                const float ml = -CS * max5x8(x0, x1, x2, x3, xm);
                x0 = __builtin_amdgcn_exp2f(fmaf(x0, CS, ml)); x1 = __builtin_amdgcn_exp2f(fmaf(x1, CS, ml));
                x2 = __builtin_amdgcn_exp2f(fmaf(x2, CS, ml)); x3 = __builtin_amdgcn_exp2f(fmaf(x3, CS, ml));
                xm = __builtin_amdgcn_exp2f(fmaf(xm, CS, ml));
                const unsigned e01 = pk2(x0, x1), e23 = pk2(x2, x3), em = pk2(xm, xm);
                const unsigned f01 = __float_as_uint(ror8(__uint_as_float(e01))), f23 = __float_as_uint(ror8(__uint_as_float(e23)));
                const v4u b1 = {c8 ? f01 : e01, c8 ? f23 : e23, c8 ? e01 : f01, c8 ? e23 : f23};
                unsigned b2 = em & (c8 ? 0xffff0000u : 0x0000ffffu);
                b2 |= __float_as_uint(ror8(__uint_as_float(b2)));
                asm volatile("" : "+v"(b2));
                f32x4 o0 = {0, 0, 0, 0}, o1 = o0;
                o0 = mfma32(vx[0], b1, o0); o1 = mfma32(vx[1], b1, o1);
                SB();
                WAITV2(0, va, vc);
                if (__builtin_expect(any_sent(max(va, vc)), 0)) RETRY(ALD4(va, o_gvx, sb, 0); ALD4(vc, o_gvx, sb, 256); WAITV2(0, va, vc), any_sent(max(va, vc)));
                o0 = mfma16((v2u){va, 0u}, (v2u){b2, 0u}, o0); o1 = mfma16((v2u){vc, 0u}, (v2u){b2, 0u}, o1);
                SB();
                float l = ((x0 + x1) + (x2 + x3)) + xm;
                l += ror8(l); l = sw16sum(l, l); l = sw32sum(l, l);
                const float rinv = __builtin_amdgcn_rcpf(l);
                SB();
                float y[4];
#pragma unroll
                for (int e = 0; e < 4; ++e) y[e] = (c8 ? o1[e] : o0[e]) * rinv;
                *(LAS v2u*)ow = (v2u){pk2(y[0], y[1]), pk2(y[2], y[3])};
            }
            BAR();
            WO_STAGE();
            const unsigned t3 = mt() + D3;
            __builtin_amdgcn_s_setprio(0);
            HAND_ADDR(s + 1);
            ZERO_A();
            unsigned o_gmx = o_gm ^ sx; asm volatile("" : "+v"(o_gmx));
            if (__builtin_expect(wave == 0, 1)) {
                unsigned o_pmx = o_pm ^ sx, o_gpx = o_gp ^ px; PIN2(o_pmx, o_gpx);
                wait_c(t3);
                v4u r0, r1, r2, r3; f32x4 u; unsigned m;
#define C_LOAD() ALD16(r0, o_gpx, pp * PAGE, 0); ALD16(r1, o_gpx, pp * PAGE, 1024); ALD16(r2, o_gpx, pp * PAGE, 2048); ALD16(r3, o_gpx, pp * PAGE, 3072);  \
                WAITV1(3, r0); u = mfma32(sel, r0, (f32x4){0, 0, 0, 0}); m = mx4(r0, 0u); SB(); WAITV1(2, r1); u = mfma32(sel, r1, u); m = mx4(r1, m); SB(); \
                WAITV1(1, r2); u = mfma32(sel, r2, u); m = mx4(r2, m); SB(); WAITV1(0, r3); u = mfma32(sel, r3, u); m = mx4(r3, m)
                C_LOAD();
                if (__builtin_expect(any_sent(m), 0)) RETRY(C_LOAD(), any_sent(m));
                float z0 = tau ? u.z : u.x, z1 = tau ? u.w : u.y;
                z0 += ror8(z0); z1 += ror8(z1);
                { auto sw = __builtin_amdgcn_permlane16_swap(__float_as_uint(z0), __float_as_uint(z1), false, false); z0 = __uint_as_float(sw[0]); z1 = __uint_as_float(sw[1]); }
                const unsigned w2 = nosent(pk2(z0, z1));
                st4(w2, rs, o_pmx, sb); st4(w2, rs, o_pmx + 65536, sb);
                const unsigned plan = mt() + D1;
                wait_c(plan);
            }
            A_STAGE_A(s + 1);
        }
    } else {
        const int hw = wave - 4;
        LAS unsigned char* const l_qk = lds + LC_PA + tau * 12288 + (rh * 96 + 16 * hw + n16) * 16;
        const unsigned o_pqk = amap(qkvb + j * 1024 + (16 * hw + n16) * 16 + rh * 8 + tau * 4);
        LAS unsigned char* const l_v = lds + LC_PA + tau * 12288 + (rh * 96 + 64 + 16 * (hw & 1) + n16) * 16;
        const unsigned o_pv = amap(qkvb + 4096 + j * 512 + (16 * (hw & 1) + n16) * 16 + rh * 8 + tau * 4);
        LAS unsigned char* const l_kv = lds + LC_KV + hw * 4096 + lane * 16;
        WAITV2(0, p0, p1); WAITV2(0, p2, p3);
        BAR();
        *(LAS v4u*)l_kv = p0; *(LAS v4u*)(l_kv + 1024) = p1; *(LAS v4u*)(l_kv + 2048) = p2; *(LAS v4u*)(l_kv + 3072) = p3;
        __builtin_amdgcn_s_setprio(2);
        for (int s = -1; s < NSEG - 1; ++s) {
            if (s >= 0) {
                ZERO_D();
                BAR();
                __builtin_amdgcn_s_sleep(3);
                { const unsigned so = fb((s + 1) * 32 + h, fpar); ALD16(p0, o_kv, so, 0); ALD16(p1, o_kv, so, 1024); ALD16(p2, o_kv, so, 2048); ALD16(p3, o_kv, so, 3072); }
                {
                    const unsigned rp = P_PART + ((s + 2) & 3), rx = ((sidemask >> rp) & 1u) << 13;
                    st8((v2u){SENT, SENT}, rs, o_pp ^ rx, rp * PAGE); st8((v2u){SENT, SENT}, rs, (o_pp - 8 * 16384) ^ rx, rp * PAGE);
                    if (hw >= 2 && s + 2 < NSEG) {
                        const unsigned pg = P_HAND + ((s + 2) >> 2);
                        st16((v4u){SENT, SENT, SENT, SENT}, rs, (hw == 2 ? o_i1 : o_i2) ^ (((sidemask >> pg) & 1u) << 13), pg * PAGE + ((s + 2) & 3) * 524288u);
                    }
                }
                BAR();
                WO_STAGE();
                if (j == 0 && tid == 256) __hip_atomic_store(ctl + C_PROG + 32 * h, (unsigned)(s + 1), RLX_AGENT);
                if (((s + 2) & 3) == 0 && s + 2 < NSEG) wait_ge(ctl + C_KV + 8 * (((s + 2) >> 2) * 32 + h), 2u, ctl, dead);
                WAITV2(0, p0, p1); WAITV2(0, p2, p3);
                LAS unsigned char* kw = l_kv + ((s + 1) & 1) * 16384;
                *(LAS v4u*)kw = p0; *(LAS v4u*)(kw + 1024) = p1; *(LAS v4u*)(kw + 2048) = p2; *(LAS v4u*)(kw + 3072) = p3;
            }
            HAND_ADDR(s + 1);
            ZERO_A();
            unsigned o_pqkx = o_pqk ^ sx, o_gmx = o_gm ^ sx; PIN2(o_pqkx, o_gmx);
            BAR();
            GATHER_MEM(, 0);
            QK_MFMA();
            V_MFMA();
            BAR();
            REDUCE4(l_qk, o_pqkx);
            SB();
            if (s == NSEG - 2 && j == 0 && tid == 256) __hip_atomic_store(ctl + C_PROG + 32 * h, (unsigned)NSEG, RLX_AGENT);
            V_PUT();
            if (hw < 2) {
                while (__builtin_amdgcn_readfirstlane(*(volatile LAS unsigned*)(lds + LC_MISC + 4)) < 8u * (unsigned)(s + 2)) { }
                asm volatile("" ::: "memory");
                REDUCE4(l_v, o_pv ^ sx);
            }
        }
    }
}

DEVI int claim_slot(GAS unsigned* ctl) {
    const unsigned x = xcc_id() & 7u;
    const unsigned r = __hip_atomic_fetch_add(ctl + C_XR + 32 * x, 1u, RLX_AGENT);
    if (r < 32 && __hip_atomic_exchange(ctl + C_SLOT + x * 32 + r, 1u, RLX_AGENT) == 0u) return (int)(x * 32 + r);
    for (int k = 255;; k = (k + 255) & 255) if (__hip_atomic_exchange(ctl + C_SLOT + k, 1u, RLX_AGENT) == 0u) return k;
}

DEVI void glds(unsigned voff, const void* sbase, unsigned ldsaddr) {
    unsigned keep;
    asm volatile("s_mov_b32 %0, m0\n\ts_mov_b32 m0, %3\n\ts_nop 2\n\tglobal_load_lds_dwordx4 %1, %2\n\ts_mov_b32 m0, %0" : "=&s"(keep) : "v"(voff), "s"(sbase), "s"(ldsaddr) : "memory");
}
template <int BM, int BN, int WM, bool TR>
struct Gemm {
    static constexpr int WN = 8 / WM, MT = BM / WM / 16, NT = BN / WN / 16, NA = BM / 64, NB = BN / 64, STAGE = (BM + BN) * 128, NS = 147456 / STAGE, LPR = BN / 8;
    f32x4 acc[MT][NT];
    DEVI void run(LAS unsigned char* lds, const u16* A, int lda, const u16* B, int ldb, int nk) {
        const int tid = otid(), lane = tid & 63, wave = __builtin_amdgcn_readfirstlane(tid >> 6);
        const int wm = wave / WN, wn = wave % WN;
        unsigned va[NA], vb[NB];
#pragma unroll
        for (int q = 0; q < NA; ++q) va[q] = ((wave * NA * 8 + q * 8 + (lane >> 3)) * lda + (((lane & 7) ^ ((lane >> 3) & 7)) * 8)) * 2;
#pragma unroll
        for (int q = 0; q < NB; ++q) {
            const int k = (wave * NB + q) * (64 / LPR) + lane / LPR, sl = lane % LPR;
            const int f = (k & 3) | (((k >> 3) & 1) << 2);
            vb[q] = (k * ldb + (((sl >> 1) ^ f) * 16 + (sl & 1) * 8)) * 2;
        }
        const unsigned lbase = (unsigned)(uintptr_t)lds;
        auto piece = [&](int kt, int buf, int q) {
            if (q < NA) glds(va[q], A + kt * 64, lbase + buf * STAGE + (wave * NA + q) * 1024);
            else glds(vb[q - NA], B + (size_t)kt * 64 * ldb, lbase + buf * STAGE + BM * 128 + (wave * NB + q - NA) * 1024);
        };
        const int fr = lane & 15, fg = lane >> 4;
        const unsigned ra = (wm * (BM / WM) + fr) * 128;
        const unsigned ra0 = ra + ((fg ^ (fr & 7)) * 16), ra1 = ra + (((4 + fg) ^ (fr & 7)) * 16);
        const int q4 = fr >> 2, p4 = fr & 3, fsw = q4 | ((fg & 1) << 2);
        const unsigned rb = BM * 128 + (8 * fg + q4) * (BN * 2) + p4 * 8 + (((wn * NT) ^ fsw) * 32);
        v4u a0[MT], b0[NT], a1[MT], b1[NT];
        auto frag = [&](LAS unsigned char* sp, int ks, v4u* af, v4u* bf, int r) {
            if (r < NT) { LAS unsigned char* q = sp + (rb ^ (r * 32)) + ks * (BN * 64); const v2u x = trrd(q), y = trrd(q + BN * 8); bf[r] = (v4u){x.x, x.y, y.x, y.y}; }
            else af[r - NT] = *(LAS v4u*)(sp + (ks ? ra1 : ra0) + (r - NT) * 2048);
        };
        constexpr int NM = MT * NT, NF = MT + NT, NP = NA + NB;
        auto half = [&](const v4u* af, const v4u* bf, LAS unsigned char* np, int nks, v4u* naf, v4u* nbf, bool rd, bool stg, int skt, int sbuf) {
#pragma unroll
            for (int i = 0; i < NM; ++i) {
                const int m = i / NT, n = i % NT;
                acc[m][n] = TR ? mfma32(bf[n], af[m], acc[m][n]) : mfma32(af[m], bf[n], acc[m][n]);
#pragma unroll
                for (int r = i * NF / NM; r < (i + 1) * NF / NM; ++r) if (rd) frag(np, nks, naf, nbf, r);
#pragma unroll
                for (int q = i * NP / NM; q < (i + 1) * NP / NM; ++q) if (stg) piece(skt, sbuf, q);
                SB();
            }
        };
#pragma unroll
        for (int m = 0; m < MT; ++m)
#pragma unroll
            for (int n = 0; n < NT; ++n) acc[m][n] = (f32x4){0, 0, 0, 0};
#pragma unroll
        for (int q = 0; q < NS * NP; ++q) piece(q / NP, q / NP, q % NP);
        asm volatile("s_waitcnt vmcnt(%0)\n\ts_barrier" :: "n"((NS - 1) * NP) : "memory");
#pragma unroll
        for (int r = 0; r < NF; ++r) frag(lds, 0, a0, b0, r);
        int buf = 0;
        for (int kt = 0; kt < nk - NS; ++kt) {
            const int nb = buf == NS - 1 ? 0 : buf + 1;
            half(a0, b0, lds + buf * STAGE, 1, a1, b1, true, false, 0, 0);
            asm volatile("s_waitcnt vmcnt(%0) lgkmcnt(0)\n\ts_barrier" :: "n"((NS - 2) * NP) : "memory");
            half(a1, b1, lds + nb * STAGE, 0, a0, b0, true, true, kt + NS, buf);
            buf = nb;
        }
#pragma unroll
        for (int t = NS; t >= 1; --t) {
            const int nb = buf == NS - 1 ? 0 : buf + 1;
            half(a0, b0, lds + buf * STAGE, 1, a1, b1, true, false, 0, 0);
            asm volatile("s_waitcnt vmcnt(%0) lgkmcnt(0)\n\ts_barrier" :: "n"(t >= 2 ? (t - 2) * NP : 0) : "memory");
            half(a1, b1, lds + nb * STAGE, 0, a0, b0, t > 1, false, 0, 0);
            buf = nb;
        }
    }
};
DEVI v4u pk8(f32x4 a, f32x4 b) { return (v4u){pk2(a.x, a.y), pk2(a.z, a.w), pk2(b.x, b.y), pk2(b.z, b.w)}; }
DEVI void announce(GAS unsigned* w) {
    asm volatile("s_waitcnt vmcnt(0) lgkmcnt(0)\n\ts_barrier" ::: "memory");
    if (otid() == 0) __hip_atomic_fetch_add(w, 1u, RLX_AGENT);
}

template <int BM, int BN>
DEVI void kv_unit(const Params& p, LAS unsigned char* lds, int r0, int hh, int mat, unsigned fpar) {
    constexpr int WM = BM >= 128 ? 4 : 2, WN = 8 / WM, MT = BM / WM / 16, NT = BN / WN / 16;
    const int lane = otid() & 63, wave = __builtin_amdgcn_readfirstlane(otid() >> 6), wm = wave / WN, wn = wave % WN;
    const __amdgpu_buffer_rsrc_t rs = __builtin_amdgcn_make_buffer_rsrc(p.ws, 0, 0xFFFFFF00u, 0x00020000);
    const int row0 = r0 + wm * (BM / WM), hw = hh + wn * NT / 8, n0 = (wn * NT) & 7;
    if (mat == 0) {
        Gemm<BM, BN, WM, true> g; g.run(lds, p.x + (size_t)r0 * D_, D_, p.wk + hh * 128, 4096, 16);
#pragma unroll
        for (int m = 0; m < MT; ++m)
#pragma unroll
            for (int c = 0; c < NT / 2; ++c) {
                const int row = row0 + m * 16;
                st16(pk8(g.acc[m][2 * c], g.acc[m][2 * c + 1]), rs, lane * 16, O_KX + fb((row >> 5) * 32 + hw, fpar) + (((row >> 4) & 1) * 4 + n0 / 2 + c) * 1024);
            }
    } else {
        Gemm<BM, BN, WM, false> g; g.run(lds, p.x + (size_t)r0 * D_, D_, p.wv + hh * 128, 4096, 16);
#pragma unroll
        for (int m = 0; m < MT; m += 2)
#pragma unroll
            for (int n = 0; n < NT; ++n) {
                const int row = row0 + m * 16;
                st16(pk8(g.acc[m][n], g.acc[m + 1][n]), rs, lane * 16, O_VX + fb((row >> 5) * 32 + hw, fpar) + (n0 + n) * 1024);
            }
    }
    asm volatile("s_waitcnt vmcnt(0) lgkmcnt(0)\n\ts_barrier" ::: "memory");
    const int t = otid();
    if (t < (BM == 256 ? 2 : 1) * (BN / 128))
        __hip_atomic_fetch_add((GAS unsigned*)(p.ws + O_CTL) + C_KV + 8 * (((r0 >> 7) + t / (BN / 128)) * 32 + hh + t % (BN / 128)), 1u, RLX_AGENT);
}

DEVI void qa_unit(const Params& p, LAS unsigned char* lds, int panel, int hh, unsigned sidemask, bool& dead) {
    const int lane = otid() & 63, wave = __builtin_amdgcn_readfirstlane(otid() >> 6);
    const int g4 = lane >> 4, n16 = lane & 15;
    const __amdgpu_buffer_rsrc_t rs = __builtin_amdgcn_make_buffer_rsrc(p.ws, 0, 0xFFFFFF00u, 0x00020000);
    GAS unsigned* ctl = (GAS unsigned*)(p.ws + O_CTL);
    v4u qf[2][4];
    {
        Gemm<256, 128, 8, true> g; g.run(lds, p.x + (size_t)panel * 256 * D_, D_, p.wq + hh * 128, 4096, 16);
#pragma unroll
        for (int m = 0; m < 2; ++m)
#pragma unroll
            for (int c = 0; c < 4; ++c) qf[m][c] = pk8(g.acc[m][2 * c], g.acc[m][2 * c + 1]);
    }
    const int seg = panel * 8 + wave;
    const unsigned fpar = far_par(sidemask), kvo = fb(seg * 32 + hh, fpar);
    wait_ge(ctl + C_KV + 8 * ((seg >> 2) * 32 + hh), seg >> 2 ? 2u : 4u, ctl, dead);
    v4u kx[8], vx[8];
#pragma unroll
    for (int q = 0; q < 8; ++q) kx[q] = ld16(rs, lane * 16 + q * 1024, O_KX + kvo);
#pragma unroll
    for (int q = 0; q < 8; ++q) vx[q] = ld16(rs, lane * 16 + q * 1024, O_VX + kvo);
    f32x4 z = {0, 0, 0, 0};
    f32x4 s00 = z, s10 = z, s11 = z, sm0 = z, sm1 = z;
#pragma unroll
    for (int c = 0; c < 4; ++c) { s00 = mfma32(kx[c], qf[0][c], s00); s10 = mfma32(kx[c], qf[1][c], s10); s11 = mfma32(kx[4 + c], qf[1][c], s11); }
    LAS unsigned char* img = lds + wave * 6144;
    wait_ge(ctl + C_PROG + 32 * hh, (unsigned)(seg + 1), ctl, dead);
    {
        const unsigned pg = P_HAND + (seg >> 2), sb = pg * PAGE + (seg & 3) * 524288u, sx = ((sidemask >> pg) & 1u) << 13;
        v4u t[6];
        for (unsigned spins = 0;;) {
            unsigned m = 0;
#pragma unroll
            for (int q = 0; q < 6; ++q) t[q] = ld16(rs, amap(L_QKV + hh * 6144 + q * 1024 + lane * 16) ^ sx, sb);
#pragma unroll
            for (int q = 0; q < 6; ++q) m = mx4(t[q], m);
            if (!any_sent(m) || dead) break;
            if (poll_fail(spins, ctl, 128)) dead = true;
            asm volatile("" ::: "memory");
        }
#pragma unroll
        for (int q = 0; q < 6; ++q) *(LAS v4u*)(img + q * 1024 + lane * 16) = t[q];
        asm volatile("s_waitcnt lgkmcnt(0)" ::: "memory");
    }
    LAS unsigned char* kb = img + 2048 + (4 * g4 + (n16 >> 2)) * 16 + (lane & 1) * 8;
#pragma unroll
    for (int c = 0; c < 4; ++c) {
        const v2u ka = trrd(kb + c * 512), kc = trrd(kb + c * 512 + 256);
        const v4u kf = {ka.x, ka.y, kc.x, kc.y};
        sm0 = mfma32(kf, qf[0][c], sm0); sm1 = mfma32(kf, qf[1][c], sm1);
    }
    v4u b1[2]; v2u b2[2];
#pragma unroll
    for (int m = 0; m < 2; ++m) {
        float v[12];
#pragma unroll
        for (int e = 0; e < 4; ++e) {
            const bool diag = 4 * g4 + e <= n16;
            v[e] = rnd(rnd(m ? s10[e] : s00[e]) * SCALE); if (m == 0 && !diag) v[e] = -INFINITY;
            v[4 + e] = (m && diag) ? rnd(rnd(s11[e]) * SCALE) : -INFINITY;
            v[8 + e] = g4 < 2 ? rnd(rnd(m ? sm1[e] : sm0[e]) * SCALE) : -INFINITY;
        }
        float mx = v[0];
#pragma unroll
        for (int e = 1; e < 12; ++e) mx = fmaxf(mx, v[e]);
        mx = sw16max(mx); mx = sw32max(mx);
        const float ml = -mx * LOG2E; float l = 0.f;
#pragma unroll
        for (int e = 0; e < 12; ++e) { v[e] = __builtin_amdgcn_exp2f(fmaf(v[e], LOG2E, ml)); l += v[e]; }
        l = sw16sum(l, l); l = sw32sum(l, l);
        const float ri = 1.f / l;
        b1[m] = (v4u){pk2(v[0] * ri, v[1] * ri), pk2(v[2] * ri, v[3] * ri), pk2(v[4] * ri, v[5] * ri), pk2(v[6] * ri, v[7] * ri)};
        b2[m] = (v2u){pk2(v[8] * ri, v[9] * ri), pk2(v[10] * ri, v[11] * ri)};
    }
#pragma unroll
    for (int c = 0; c < 4; ++c) {
        f32x4 o[2][2];
#pragma unroll
        for (int t = 0; t < 2; ++t) {
            const v2u va = *(LAS v2u*)(img + 4096 + ((2 * c + t) * 16 + n16) * 16 + (g4 & 1) * 8);
#pragma unroll
            for (int m = 0; m < 2; ++m) { o[m][t] = mfma32(vx[2 * c + t], b1[m], z); o[m][t] = mfma16(va, b2[m], o[m][t]); }
        }
#pragma unroll
        for (int m = 0; m < 2; ++m) st16(pk8(o[m][0], o[m][1]), rs, n16 * 16384 + 16 * g4, O_OX + fb(seg * 32 + m * 16, fpar) + (hh * 128 + c * 32) * 2);
    }
    asm volatile("s_waitcnt vmcnt(0)" ::: "memory");
    if (lane == 0) __hip_atomic_fetch_add(ctl + C_OF + 16 * (seg * 8 + (hh >> 2)), 1u, RLX_AGENT);
}

DEVI void top2(const float* R, int& i0, int& i1) {
    const int lane = otid() & 63;
    float b0 = -INFINITY, b1 = -INFINITY; int j0 = 0x7fffffff, j1 = 0x7fffffff;
    f32x4 rv[8];
#pragma unroll
    for (int q = 0; q < 8; ++q) rv[q] = *(const f32x4*)(R + lane * 32 + q * 4);
#pragma unroll
    for (int q = 0; q < 32; ++q) {
        const float v = rv[q >> 2][q & 3]; const int k = lane * 32 + q;
        const bool f0 = v > b0 || j0 == 0x7fffffff, f1 = v > b1 || j1 == 0x7fffffff;
        b1 = f0 ? b0 : (f1 ? v : b1); j1 = f0 ? j0 : (f1 ? k : j1); b0 = f0 ? v : b0; j0 = f0 ? k : j0;
    }
    for (int m = 1; m < 64; m <<= 1) {
        const float c0 = __shfl_xor(b0, m), c1 = __shfl_xor(b1, m); const int k0 = __shfl_xor(j0, m), k1 = __shfl_xor(j1, m);
        auto gt = [](float a, int ja, float b, int jb) { return a > b || (!(b > a) && ja < jb); };
        if (gt(c0, k0, b0, j0)) { if (gt(b0, j0, c1, k1)) { b1 = b0; j1 = j0; } else { b1 = c1; j1 = k1; } b0 = c0; j0 = k0; }
        else if (gt(c0, k0, b1, j1)) { b1 = c0; j1 = k0; }
    }
    i0 = __builtin_amdgcn_readfirstlane(j0); i1 = __builtin_amdgcn_readfirstlane(j1);
}
DEVI void ffn1_unit(const Params& p, LAS unsigned char* lds, int u, int i0, int i1) {
    const int tid = otid(), co = tid & 7, ksl = tid >> 3, col = u * 64 + co * 8;
    LAS float* red = (LAS float*)lds;
    v4u xa[2], xb[2];
#pragma unroll
    for (int q = 0; q < 2; ++q) { xa[q] = *(const v4u*)(p.x + i0 * D_ + ksl * 16 + q * 8); xb[q] = *(const v4u*)(p.x + i1 * D_ + ksl * 16 + q * 8); }
    float a[4][8] = {};
    v4u wg[16], wu[16];
#pragma unroll
    for (int q = 0; q < 16; ++q) {
        wg[q] = __builtin_nontemporal_load((const v4u*)(p.wg + (size_t)(ksl * 16 + q) * F_ + col));
        wu[q] = __builtin_nontemporal_load((const v4u*)(p.wu + (size_t)(ksl * 16 + q) * F_ + col));
    }
#pragma unroll
    for (int q = 0; q < 16; ++q) {
        const unsigned ua = xa[q >> 3][(q >> 1) & 3], ub = xb[q >> 3][(q >> 1) & 3];
        const float x0 = (q & 1) ? hi16(ua) : lo16(ua), x1 = (q & 1) ? hi16(ub) : lo16(ub);
#pragma unroll
        for (int e = 0; e < 4; ++e) {
            a[0][2 * e] += x0 * lo16(wg[q][e]); a[0][2 * e + 1] += x0 * hi16(wg[q][e]); a[1][2 * e] += x1 * lo16(wg[q][e]); a[1][2 * e + 1] += x1 * hi16(wg[q][e]);
            a[2][2 * e] += x0 * lo16(wu[q][e]); a[2][2 * e + 1] += x0 * hi16(wu[q][e]); a[3][2 * e] += x1 * lo16(wu[q][e]); a[3][2 * e + 1] += x1 * hi16(wu[q][e]);
        }
    }
#pragma unroll
    for (int q = 0; q < 4; ++q)
#pragma unroll
        for (int e = 0; e < 8; ++e) red[(ksl * 4 + q) * 64 + co * 8 + e] = a[q][e];
    BAR();
    if (tid < 128) {
        const int t = tid >> 6, c = tid & 63; float g = 0.f, uu = 0.f;
        for (int k = 0; k < 64; ++k) { g += red[(k * 4 + t) * 64 + c]; uu += red[(k * 4 + 2 + t) * 64 + c]; }
        g = rnd(g); uu = rnd(uu); g = rnd(g / (1.f + __expf(-g)));
        __hip_atomic_store((GAS float*)(p.ws + O_FH) + t * F_ + u * 64 + c, rnd(g * uu), RLX_AGENT);
    }
    announce((GAS unsigned*)(p.ws + O_CTL) + C_F1);
}
DEVI void ffn2_unit(const Params& p, LAS unsigned char* lds, int u, bool& dead) {
    const int tid = otid(), co = tid & 3, ksl = tid >> 2, col = u * 32 + co * 8;
    GAS unsigned* ctl = (GAS unsigned*)(p.ws + O_CTL);
    LAS float* red = (LAS float*)lds;
    v4u w[28];
#pragma unroll
    for (int q = 0; q < 28; ++q) w[q] = __builtin_nontemporal_load((const v4u*)(p.wd + (size_t)(ksl * 28 + q) * D_ + col));
    wait_ge(ctl + C_F1, 56u, ctl, dead);
    const __amdgpu_buffer_rsrc_t rs = __builtin_amdgcn_make_buffer_rsrc(p.ws, 0, 0xFFFFFF00u, 0x00020000);
    v4u h0[7], h1[7];
#pragma unroll
    for (int q = 0; q < 7; ++q) { h0[q] = ld16(rs, ksl * 112 + q * 16, O_FH); h1[q] = ld16(rs, ksl * 112 + q * 16, O_FH + F_ * 4); }
    float a[2][8] = {};
#pragma unroll
    for (int q = 0; q < 28; ++q) {
        const float x0 = __uint_as_float(h0[q >> 2][q & 3]), x1 = __uint_as_float(h1[q >> 2][q & 3]);
#pragma unroll
        for (int e = 0; e < 4; ++e) { a[0][2 * e] += x0 * lo16(w[q][e]); a[0][2 * e + 1] += x0 * hi16(w[q][e]); a[1][2 * e] += x1 * lo16(w[q][e]); a[1][2 * e + 1] += x1 * hi16(w[q][e]); }
    }
#pragma unroll
    for (int q = 0; q < 2; ++q)
#pragma unroll
        for (int e = 0; e < 8; ++e) red[(ksl * 2 + q) * 32 + co * 8 + e] = a[q][e];
    BAR();
    if (tid < 64) {
        const int t = tid >> 5, c = tid & 31; float y = 0.f;
        for (int k = 0; k < 128; ++k) y += red[(k * 2 + t) * 32 + c];
        __hip_atomic_store((GAS float*)(p.ws + O_FY) + t * D_ + u * 32 + c, rnd(y), RLX_AGENT);
    }
    announce(ctl + C_F2);
}

DEVI void wo_fast_unit(const Params& p, LAS unsigned char* lds, int ct, int k8, int i0, int i1, unsigned fpar, bool& dead) {
    const int tid = otid(), lane = tid & 63, wave = __builtin_amdgcn_readfirstlane(tid >> 6), fr = lane & 15, fg = lane >> 4;
    GAS unsigned* ctl = (GAS unsigned*)(p.ws + O_CTL);
    const __amdgpu_buffer_rsrc_t rs = __builtin_amdgcn_make_buffer_rsrc(p.ws, 0, 0xFFFFFF00u, 0x00020000);
    {
        const unsigned lbase = (unsigned)(uintptr_t)lds + wave * 2048;
        const u16* gb = p.wo + (size_t)k8 * 512 * D_ + ct * 128;
#pragma unroll
        for (int q = 0; q < 2; ++q) {
            const int k = wave * 8 + (lane >> 4) + 4 * q;
            const int pk = (k & 32) + 16 * ((k >> 2) & 1) + 4 * ((k >> 3) & 3) + (k & 3), f = (k & 3) | (((k >> 3) & 1) << 2);
            const unsigned vb = (pk * D_ + ((((lane & 15) >> 1) ^ f) * 16 + (lane & 1) * 8)) * 2;
#pragma unroll
            for (int kt = 0; kt < 8; ++kt) glds(vb, gb + (size_t)kt * 64 * D_, lbase + kt * 16384 + q * 1024);
        }
    }
    wait_ge(ctl + C_INIT, 128u, ctl, dead);
    asm volatile("s_waitcnt vmcnt(0)\n\ts_barrier" ::: "memory");
    const int mt = wave & 1, np = wave >> 1;
    v4u bf[16][2];
    {
        const int q4 = fr >> 2, p4 = fr & 3, fsw = q4 | ((fg & 1) << 2);
        const unsigned bpo = (8 * fg + q4) * 256 + p4 * 8 + ((fsw ^ (2 * np)) * 32);
#pragma unroll
        for (int q = 0; q < 16; ++q)
#pragma unroll
            for (int n = 0; n < 2; ++n) { LAS unsigned char* bp = lds + (bpo ^ (n * 32)) + q * 8192; const v2u x = trrd(bp), y = trrd(bp + 1024); bf[q][n] = (v4u){x.x, x.y, y.x, y.y}; }
    }
    BAR();
    const int col = ct * 128 + k8 * 16 + fg * 4;
    v4u t[8]; float gv; v2u xv;
    auto request = [&](int sg) {
        const int row = sg * 32 + mt * 16 + fr;
#pragma unroll
        for (int q = 0; q < 8; ++q) t[q] = ld16(rs, lane * 16 + q * 1024, O_S2 + fb((((sg & 7) * 8 + ct) * 2 + mt) * 8 + k8, fpar));
        gv = ((const float*)(p.ws + O_G))[row];
        xv = *(const v2u*)(p.x + row * D_ + col);
    };
    auto finish = [&](int sg) {
        const int row = sg * 32 + mt * 16 + fr;
        for (unsigned spins = 0;;) {
            v4u an = t[0], orr = t[0];
#pragma unroll
            for (int q = 1; q < 8; ++q) { an &= t[q]; orr |= t[q]; }
            const unsigned bad = (sg & 8) ? ~(an.x & an.y & an.z & an.w) : (orr.x | orr.y | orr.z | orr.w);
            if (__builtin_amdgcn_ballot_w64((bad & 1u) != 0u) == 0 || dead) break;
            if (poll_fail(spins, ctl, 64)) dead = true;
            asm volatile("" ::: "memory");
            request(sg);
        }
        float bb[4] = {lo16(xv.x), hi16(xv.x), lo16(xv.y), hi16(xv.y)};
        if ((i0 >> 5) == sg || (i1 >> 5) == sg) wait_ge(ctl + C_F2, 32u, ctl, dead);
        if (row == i0 || row == i1) {
            const float rs_ = rnd(1.f / (1.f + __expf(-((const float*)(p.ws + O_R))[row])));
#pragma unroll
            for (int e = 0; e < 4; ++e) bb[e] = rnd(bb[e] + rnd(__hip_atomic_load((GAS float*)(p.ws + O_FY) + (row == i0 ? 0 : D_) + col + e, RLX_AGENT) * rs_));
        }
        f32x4 a = __builtin_bit_cast(f32x4, t[0]);
#pragma unroll
        for (int q = 1; q < 8; ++q) a += __builtin_bit_cast(f32x4, t[q]);
        const float og = rnd(1.f - gv);
        float o[4];
#pragma unroll
        for (int e = 0; e < 4; ++e) o[e] = rnd(gv * rnd(a[e])) + rnd(og * bb[e]);
        *(v2u*)(p.out + row * D_ + col) = (v2u){pk2(o[0], o[1]), pk2(o[2], o[3])};
    };
    int ready = 0;
    const v4u rd = {(unsigned)(uintptr_t)p.ws, (unsigned)((uintptr_t)p.ws >> 32) & 0xffffu, 0xFFFFFF00u, 0x00020000u};
    v4u ar0, ar1, ar2, ar3;
    const unsigned va0 = lane * 16, va1 = va0 + 16384, va2 = va0 + 32768, va3 = va0 + 49152;
    auto aload = [&](int sg) {
        const unsigned so = O_OX + fb(sg * 32 + wave * 4, fpar) + k8 * 1024u;
        ALD16(ar0, va0, so, 0); ALD16(ar1, va1, so, 0); ALD16(ar2, va2, so, 0); ALD16(ar3, va3, so, 0);
    };
    LAS unsigned char* const aw = lds + wave * 4096;
    const unsigned sw0 = (lane ^ ((wave * 4) & 15)) * 16;
    for (int seg = 0; seg < NSEG; ++seg) {
        if (seg >= ready) {
            if (wave == ((2 * seg + 4) & 7)) {
                unsigned long long ok;
                for (unsigned spins = 0;;) {
                    const unsigned c = seg + lane < NSEG ? __hip_atomic_load(ctl + C_OF + 16 * ((seg + lane) * 8 + k8), RLX_AGENT) : 0u;
                    ok = __builtin_amdgcn_ballot_w64(c >= 4u);
                    if ((ok & 1ull) || dead) break;
                    if (poll_fail(spins, ctl, 64)) dead = true;
                }
                if (lane == 0) *(LAS int*)(lds + 65536) = seg + max(1, (int)__builtin_ctzll(~ok));
            }
            BAR();
            ready = __builtin_amdgcn_readfirstlane(*(LAS int*)(lds + 65536));
            aload(seg);
            asm volatile("s_waitcnt vmcnt(0)" ::: "memory");
        }
        WAITV2(2, ar0, ar1); WAITV2(2, ar2, ar3);
        LAS unsigned char* img = lds + (seg & 1) * 32768;
        { LAS unsigned char* w_ = aw + (seg & 1) * 32768;
          *(LAS v4u*)(w_ + sw0) = ar0; *(LAS v4u*)(w_ + 1024 + (sw0 ^ 16)) = ar1; *(LAS v4u*)(w_ + 2048 + (sw0 ^ 32)) = ar2; *(LAS v4u*)(w_ + 3072 + (sw0 ^ 48)) = ar3; }
        asm volatile("s_waitcnt lgkmcnt(0)" ::: "memory");
        SB();
        if (seg >= 3 && np == ((seg - 3) & 3)) finish(seg - 3);
        if (seg >= 2 && np == ((seg - 2) & 3)) request(seg - 2);
        if (seg + 1 < ready) aload(seg + 1);
        BAR();
        f32x4 c0 = {0, 0, 0, 0}, c1 = c0;
        {
            v4u af[2][4];
            LAS unsigned char* ap = img + (mt * 16 + fr) * 1024;
#pragma unroll
            for (int q = 0; q < 4; ++q) af[0][q] = *(LAS v4u*)(ap + (((q * 4 + fg) ^ fr) * 16));
#pragma unroll
            for (int g = 0; g < 4; ++g) {
                if (g < 3) {
#pragma unroll
                    for (int q = 0; q < 4; ++q) af[(g + 1) & 1][q] = *(LAS v4u*)(ap + (g + 1) * 256 + (((q * 4 + fg) ^ fr) * 16));
                }
                SB();
#pragma unroll
                for (int q = 0; q < 4; ++q) { c0 = mfma32(bf[g * 4 + q][0], af[g & 1][q], c0); c1 = mfma32(bf[g * 4 + q][1], af[g & 1][q], c1); }
                SB();
            }
        }
        const unsigned sb2 = O_S2 + fb((((seg & 7) * 8 + ct) * 2 + mt) * 8 + 2 * np, fpar) + k8 * 1024u;
        const unsigned tg = (seg >> 3) & 1u;
        st16((__builtin_bit_cast(v4u, c0) & ~1u) | tg, rs, lane * 16, sb2);
        st16((__builtin_bit_cast(v4u, c1) & ~1u) | tg, rs, lane * 16, sb2 + 16384u);
    }
    if (np == 1) finish(NSEG - 3);
    if (np >= 2) { if (np == 3) __builtin_amdgcn_s_sleep(5); request(NSEG - 4 + np); finish(NSEG - 4 + np); }
}

constexpr int Q_F1 = 0, Q_F2 = Q_F1 + 14, Q_QA = Q_F2 + 8, Q_K2 = Q_QA + 16, Q_WF = Q_K2 + 16, Q_Q2 = Q_WF + 16, Q_END = Q_Q2 + 48;
DEVI void worker_body(const Params& p, LAS unsigned char* lds, int w) {
    const int tid = otid(), lane = tid & 63;
    GAS unsigned* ctl = (GAS unsigned*)(p.ws + O_CTL);
    const unsigned sidemask = side_mask(ctl, lane);
    bool dead = false;
    const unsigned fpar = far_par(sidemask);
    const int xq = w >> 5, hm = xq * 16 + ((w & 31) >> 1);
    kv_unit<64, 128>(p, lds, (w & 1) * 64, hm >> 1, hm & 1, fpar);
    kv_unit<128, 128>(p, lds, 128 + (w & 1) * 128, hm >> 1, hm & 1, fpar);
    kv_unit<256, 128>(p, lds, 384 + (w & 1) * 256, hm >> 1, hm & 1, fpar);
    { const int hp = xq * 8 + ((w & 31) >> 2); kv_unit<256, 256>(p, lds, 896 + (w & 3) * 256, (hp >> 1) * 2, hp & 1, fpar); }
    {
        const __amdgpu_buffer_rsrc_t rs = __builtin_amdgcn_make_buffer_rsrc(p.ws, 0, 0xFFFFFF00u, 0x00020000);
        const v4u zz = {SENT, SENT, SENT, SENT};
#pragma unroll
        for (int q = 0; q < 8; ++q) st16(zz, rs, otid() * 16, O_S2 + fb(w * 8 + q, fpar));
        announce(ctl + C_INIT);
    }
    int i0, i1; top2((const float*)(p.ws + O_R), i0, i1);
    for (;;) {
        BAR();
        if (tid == 0) *(LAS unsigned*)(lds + 147456) = __hip_atomic_fetch_add(ctl + C_QUEUE + 16 * xq, 1u, RLX_AGENT);
        BAR();
        int t = __builtin_amdgcn_readfirstlane(*(LAS unsigned*)(lds + 147456));
        if (t >= Q_END) break;
        if (t < Q_F2) ffn1_unit(p, lds, xq * 14 + t - Q_F1, i0, i1);
        else if (t < Q_QA) ffn2_unit(p, lds, xq * 8 + t - Q_F2, dead);
        else if (t >= Q_K2 && t < Q_WF) kv_unit<128, 128>(p, lds, 1920, xq * 8 + ((t - Q_K2) >> 1), t & 1, fpar);
        else if (t >= Q_WF && t < Q_Q2) wo_fast_unit(p, lds, (t - Q_WF) >> 1, 2 * xq + ((t - Q_WF) & 1), i0, i1, fpar, dead);
        else { const int qa = t < Q_K2 ? t - Q_QA : t - Q_Q2 + 16; qa_unit(p, lds, qa >> 3, xq * 8 + (qa & 7), sidemask, dead); }
    }
}

__global__ void __launch_bounds__(512, 2) k_main(Params p) {
    extern __shared__ __attribute__((aligned(16))) unsigned char smem[];
    LAS unsigned char* lds = (LAS unsigned char*)smem;
    if (threadIdx.x == 0) *(LAS int*)(lds + 147456) = claim_slot((GAS unsigned*)(p.ws + O_CTL));
    __syncthreads();
    const int slot = __builtin_amdgcn_readfirstlane(*(LAS int*)(lds + 147456));
    if (slot < 128) chain_body(p, lds, slot >> 5, slot & 31);
    else worker_body(p, lds, slot - 128);
}

extern "C" void kernel_launch(void* const* d_in, const int* in_sizes, int n_in, void* d_out, int out_size, void* d_ws, size_t ws_size, hipStream_t stream) {
    Params p;
    p.x = (const u16*)d_in[0]; p.mem0 = (const u16*)d_in[1]; p.wq = (const u16*)d_in[2]; p.wk = (const u16*)d_in[3]; p.wv = (const u16*)d_in[4]; p.wo = (const u16*)d_in[5];
    p.wr = (const u16*)d_in[8]; p.wg = (const u16*)d_in[9]; p.wu = (const u16*)d_in[10]; p.wd = (const u16*)d_in[11]; p.gw = (const u16*)d_in[12];
    p.out = (u16*)d_out;
    p.ws = (unsigned char*)(((uintptr_t)d_ws + PAGE - 1) & ~(uintptr_t)(PAGE - 1));
    constexpr int LDS_M = 147456 + 64;
    (void)hipFuncSetAttribute((const void*)k_main, hipFuncAttributeMaxDynamicSharedMemorySize, LDS_M);
    hipLaunchKernelGGL(k_prep, dim3(256), dim3(320), 0, stream, p);
    hipLaunchKernelGGL(k_main, dim3(256), dim3(512), LDS_M, stream, p);
}
```

```cpp
#include <hip/hip_runtime.h>
#include <hip/hip_bf16.h>
#include <stdint.h>

typedef unsigned short u16;
typedef unsigned v4u __attribute__((ext_vector_type(4)));
typedef unsigned v2u __attribute__((ext_vector_type(2)));
typedef unsigned v8u __attribute__((ext_vector_type(8)));
typedef float f32x4 __attribute__((ext_vector_type(4)));
typedef __bf16 bf16x2 __attribute__((ext_vector_type(2)));
typedef __bf16 bf16x8 __attribute__((ext_vector_type(8)));
typedef __bf16 bf16x16 __attribute__((ext_vector_type(16)));
typedef short s16x4 __attribute__((ext_vector_type(4)));
#define GAS __attribute__((address_space(1)))
#define LAS __attribute__((address_space(3)))
#define RLX_AGENT __ATOMIC_RELAXED, __HIP_MEMORY_SCOPE_AGENT
#define DEVI __device__ __forceinline__
#define BAR() asm volatile("s_waitcnt lgkmcnt(0)\n\ts_barrier" ::: "memory")

constexpr int D_ = 1024, NSEG = 64, F_ = 3584;
constexpr float SCALE = 0.08838834764831845f, LOG2E = 1.4426950408889634f;
constexpr unsigned SENT = 0xFFFFFFFFu;
constexpr unsigned PAGE = 2u << 20;
constexpr unsigned O_CTL = 0, O_R = 65536, O_G = O_R + 8192, O_FH = 131072, O_FY = O_FH + 32768;
constexpr unsigned P_HAND = 1, P_PART = 17;
constexpr unsigned O_KX = 22 * PAGE, O_VX = 38 * PAGE, O_OX = 54 * PAGE, O_S2 = 70 * PAGE  ;
constexpr unsigned L_QKV = 65536;
constexpr int C_ABORT = 0, C_ARRIVE = 64, C_T0 = 128, C_QUEUE = 192, C_XR = 256  , C_F1 = 768, C_F2 = 800, C_INIT = 832, C_SLOT = 1024  ,
              C_KV = 2048  , C_PROG = 6144  , C_OF = 7168  , C_ZEND = 15360,
              C_SIDE = 15360  ;

struct Params { const u16 *x, *mem0, *wq, *wk, *wv, *wo, *wr, *wg, *wu, *wd, *gw; u16* out; unsigned char* ws; };
constexpr int DQ = 700, D3 = 850, D1 = 750, T0_LEAD = 120;

DEVI float bf2f(u16 b) { return __uint_as_float((unsigned)b << 16); }
DEVI unsigned pk2(float lo, float hi) { bf16x2 v = {(__bf16)lo, (__bf16)hi}; return __builtin_bit_cast(unsigned, v); }
DEVI float rnd(float f) { return __uint_as_float(pk2(f, 0.f) << 16); }
DEVI float lo16(unsigned u) { return __uint_as_float(u << 16); }
DEVI float hi16(unsigned u) { return __uint_as_float(u & 0xffff0000u); }
DEVI int otid() { int t = threadIdx.x; asm volatile("" : "+v"(t)); return t; }
DEVI unsigned xcc_id() { return (unsigned)__builtin_amdgcn_s_getreg(20 | (3 << 11)) & 0xfu; }
DEVI unsigned rt() { return (unsigned)__builtin_amdgcn_s_memrealtime(); }
DEVI unsigned amap(unsigned lo) { return ((lo >> 13) << 14) + (lo & 8191); }
DEVI unsigned fb(unsigned blk, unsigned par) { return blk * 16384u + (((par ^ (blk >> 7)) & 1u) << 13); }
DEVI unsigned far_par(unsigned sidemask) { return (sidemask & 1u) ^ 1u; }
DEVI unsigned side_mask(GAS unsigned* ctl, int lane) {
    const v4u v = *(GAS v4u*)(ctl + C_SIDE + lane * 4);
    int n = 0;
#pragma unroll
    for (int e = 0; e < 4; ++e) n += __builtin_popcountll(__builtin_amdgcn_ballot_w64((v[e] & 1u) != 0u));
    return n > 128 ? 0x55555555u : 0xAAAAAAAAu;
}
DEVI v4u ld16(__amdgpu_buffer_rsrc_t r, unsigned vo, unsigned so) { return __builtin_amdgcn_raw_buffer_load_b128(r, vo, so, 16); }
DEVI void st16(v4u v, __amdgpu_buffer_rsrc_t r, unsigned vo, unsigned so) {
    __builtin_amdgcn_raw_buffer_store_b128(v, r, vo, so, 16);
    asm volatile("s_nop 1" :: "v"(v));
}
DEVI void st8(v2u v, __amdgpu_buffer_rsrc_t r, unsigned vo, unsigned so) { __builtin_amdgcn_raw_buffer_store_b64(v, r, vo, so, 16); }
DEVI void st4(unsigned v, __amdgpu_buffer_rsrc_t r, unsigned vo, unsigned so) { __builtin_amdgcn_raw_buffer_store_b32(v, r, vo, so, 16); }
DEVI unsigned umax3(unsigned a, unsigned b, unsigned c) { unsigned r; asm("v_max3_u32 %0, %1, %2, %3" : "=v"(r) : "v"(a), "v"(b), "v"(c)); return r; }
DEVI unsigned mx4(v4u v, unsigned m) { return umax3(umax3(v.x, v.y, m), v.z, v.w); }
DEVI bool any_sent(unsigned m) { return __builtin_amdgcn_ballot_w64(m == SENT) != 0; }
DEVI unsigned nosent(unsigned u) { return u < 0xFFFFFFFEu ? u : 0xFFFFFFFEu; }
DEVI f32x4 mfma32(v4u a, v4u b, f32x4 c) { return __builtin_amdgcn_mfma_f32_16x16x32_bf16(__builtin_bit_cast(bf16x8, a), __builtin_bit_cast(bf16x8, b), c, 0, 0, 0); }
DEVI f32x4 mfma16(v2u a, v2u b, f32x4 c) { return __builtin_amdgcn_mfma_f32_16x16x16bf16_1k(__builtin_bit_cast(s16x4, a), __builtin_bit_cast(s16x4, b), c, 0, 0, 0); }
DEVI f32x4 smf(v4u a, v8u b, f32x4 c, int idx) { return __builtin_amdgcn_smfmac_f32_16x16x64_bf16(__builtin_bit_cast(bf16x8, a), __builtin_bit_cast(bf16x16, b), c, idx, 0, 0); }
DEVI v2u trrd(LAS unsigned char* p) { return __builtin_bit_cast(v2u, __builtin_amdgcn_ds_read_tr16_b64_v4i16((LAS s16x4*)p)); }
DEVI float ror8(float v) { return __int_as_float(__builtin_amdgcn_update_dpp(0, __float_as_int(v), 0x128, 0xf, 0xf, false)); }
DEVI float sw32sum(float a, float b) { auto r = __builtin_amdgcn_permlane32_swap(__float_as_uint(a), __float_as_uint(b), false, false); return __uint_as_float(r[0]) + __uint_as_float(r[1]); }
DEVI float sw16sum(float a, float b) { auto r = __builtin_amdgcn_permlane16_swap(__float_as_uint(a), __float_as_uint(b), false, false); return __uint_as_float(r[0]) + __uint_as_float(r[1]); }
DEVI float sw32max(float a) { auto r = __builtin_amdgcn_permlane32_swap(__float_as_uint(a), __float_as_uint(a), false, false); return fmaxf(__uint_as_float(r[0]), __uint_as_float(r[1])); }
DEVI float sw16max(float a) { auto r = __builtin_amdgcn_permlane16_swap(__float_as_uint(a), __float_as_uint(a), false, false); return fmaxf(__uint_as_float(r[0]), __uint_as_float(r[1])); }
DEVI f32x4 fold(f32x4 a, f32x4 b) { f32x4 r; r.x = sw32sum(a.x, b.x); r.y = sw32sum(a.y, b.y); r.z = sw32sum(a.z, b.z); r.w = sw32sum(a.w, b.w); return r; }
DEVI float max5x8(float a, float b, float c, float d, float e) {
    float m, t;
    asm("v_max3_f32 %0, %2, %3, %4\n\tv_max3_f32 %0, %0, %5, %6\n\ts_nop 1\n\t"
        "v_max_f32_dpp %0, %0, %0 row_ror:8 row_mask:0xf bank_mask:0xf\n\tv_mov_b32 %1, %0\n\ts_nop 1\n\t"
        "v_permlane16_swap_b32 %0, %1\n\tv_max_f32 %0, %0, %1\n\tv_mov_b32 %1, %0\n\ts_nop 1\n\t"
        "v_permlane32_swap_b32 %0, %1\n\tv_max_f32 %0, %0, %1"
        : "=&v"(m), "=&v"(t) : "v"(a), "v"(b), "v"(c), "v"(d), "v"(e));
    return m;
}
#define ALD16(D, VO, SO, IMM) asm volatile("buffer_load_dwordx4 %0, %1, %2, %3 offen offset:" #IMM " sc1" : "=v"(D) : "v"(VO), "s"(rd), "s"(SO) : "memory")
#define ALD4(D, VO, SO, IMM) asm volatile("buffer_load_dword %0, %1, %2, %3 offen offset:" #IMM " sc1" : "=v"(D) : "v"(VO), "s"(rd), "s"(SO) : "memory")
#define WAITV1(N, A) asm volatile("s_waitcnt vmcnt(" #N ")" : "+v"(A) :: "memory")
#define WAITV2(N, A, B) asm volatile("s_waitcnt vmcnt(" #N ")" : "+v"(A), "+v"(B) :: "memory")
#define SB() __builtin_amdgcn_sched_barrier(0)
#define PIN2(a, b) asm volatile("" : "+v"(a), "+v"(b))
DEVI unsigned mt() { return (unsigned)__builtin_amdgcn_s_memtime(); }
DEVI void wait_l(unsigned t) {
    unsigned d;
    asm volatile("1:\n\ts_memrealtime vcc\n\ts_waitcnt lgkmcnt(0)\n\ts_sub_i32 %0, vcc_lo, %1\n\ts_cmp_lt_i32 %0, 0\n\ts_cbranch_scc1 1b"
                 : "=&s"(d) : "s"(__builtin_amdgcn_readfirstlane(t)) : "memory", "scc", "vcc");
}
DEVI void wait_c(unsigned t) {
    unsigned d;
    asm volatile("1:\n\ts_memtime vcc\n\ts_waitcnt lgkmcnt(0)\n\ts_sub_i32 %0, vcc_lo, %1\n\ts_cmp_lt_i32 %0, 0\n\ts_cbranch_scc1 1b"
                 : "=&s"(d) : "s"(__builtin_amdgcn_readfirstlane(t)) : "memory", "scc", "vcc");
}
constexpr unsigned SPINCAP = 40000;
DEVI bool poll_fail(unsigned& spins, GAS unsigned* ctl, int back) {
    if (back >= 128) __builtin_amdgcn_s_sleep(2); else if (back) __builtin_amdgcn_s_sleep(1);
    ++spins;
    if ((spins & 63u) == 0u) {
        if (spins > SPINCAP) { __hip_atomic_store(ctl + C_ABORT, 1u, RLX_AGENT); return true; }
        if (__builtin_amdgcn_readfirstlane(__hip_atomic_load(ctl + C_ABORT, RLX_AGENT))) return true;
    }
    return false;
}

DEVI void wait_ge(GAS unsigned* w, unsigned v, GAS unsigned* ctl, bool& dead) {
    unsigned spins = 0;
    while (!dead && (unsigned)__builtin_amdgcn_readfirstlane(__hip_atomic_load(w, RLX_AGENT)) < v) { if (poll_fail(spins, ctl, 64)) dead = true; }
    asm volatile("" ::: "memory");
}

__global__ void __launch_bounds__(320) k_prep(Params p) {
    const int tid = threadIdx.x, lane = tid & 63, wave = tid >> 6, b = blockIdx.x;
    unsigned* ctl = (unsigned*)(p.ws + O_CTL);
    if (b >= 196 && tid < 256) ctl[(b - 196) * 256 + tid] = 0u;
    if (tid == 256) {
        const unsigned page = 1 + (b >> 3) % 20;
        __amdgpu_buffer_rsrc_t rs = __builtin_amdgcn_make_buffer_rsrc(p.ws, 0, 0xFFFFFF00u, 0x00020000);
        unsigned t[2] = {0xffffffffu, 0xffffffffu};
        for (int rep = 0; rep < 2; ++rep)
            for (int side = 0; side < 2; ++side) {
                const unsigned long long c0 = __builtin_amdgcn_s_memtime();
                asm volatile("s_waitcnt lgkmcnt(0)" ::: "memory");
                st4(SENT, rs, page * PAGE + 480 * 1024 + side * 8192 + (b & 7) * 512 + (b >= 160 ? 256 : 0), 0);
                asm volatile("s_waitcnt vmcnt(0)" ::: "memory");
                const unsigned long long c1 = __builtin_amdgcn_s_memtime();
                t[side] = min(t[side], (unsigned)(c1 - c0));
            }
        ctl[C_SIDE + b] = (t[1] < t[0] ? 1u : 0u) ^ (xcc_id() >= 4 ? 1u : 0u) ^ (page & 1u);
    }
    if (wave == 4) return;
    float* R = (float*)(p.ws + O_R); float* G = (float*)(p.ws + O_G);
    v4u xv[2][2], wr[2], gw[2];
#pragma unroll
    for (int q = 0; q < 2; ++q) {
        const int k = q * 512 + lane * 8;
        xv[0][q] = *(const v4u*)(p.x + (b * 8 + wave * 2) * D_ + k); xv[1][q] = *(const v4u*)(p.x + (b * 8 + wave * 2 + 1) * D_ + k);
        wr[q] = *(const v4u*)(p.wr + k); gw[q] = *(const v4u*)(p.gw + k);
    }
    float a[2] = {0.f, 0.f}, g[2] = {0.f, 0.f};
#pragma unroll
    for (int rr = 0; rr < 2; ++rr)
#pragma unroll
        for (int q = 0; q < 2; ++q)
#pragma unroll
            for (int e = 0; e < 4; ++e) {
                const unsigned u = xv[rr][q][e];
                a[rr] = fmaf(lo16(u), lo16(wr[q][e]), a[rr]); a[rr] = fmaf(hi16(u), hi16(wr[q][e]), a[rr]);
                const unsigned pr = pk2(lo16(u) * lo16(gw[q][e]), hi16(u) * hi16(gw[q][e]));
                g[rr] += lo16(pr); g[rr] += hi16(pr);
            }
#pragma unroll
    for (int m = 32; m >= 1; m >>= 1) { a[0] += __shfl_xor(a[0], m); g[0] += __shfl_xor(g[0], m); a[1] += __shfl_xor(a[1], m); g[1] += __shfl_xor(g[1], m); }
    if (lane < 2) { const int row = b * 8 + wave * 2 + lane; R[row] = rnd(lane ? a[1] : a[0]); G[row] = rnd(1.f / (1.f + __expf(-rnd(lane ? g[1] : g[0])))); }
}

constexpr unsigned LC_PA = 0, LC_QK = 24576, LC_O = LC_QK + 6144, LC_MISC = 98304 + 16, LC_KV = 33792  ;

DEVI void chain_body(const Params& p, LAS unsigned char* lds, const int g, const int i) {
    const int tid = threadIdx.x, lane = tid & 63, wave = __builtin_amdgcn_readfirstlane(tid >> 6);
    const int h = g * 8 + (i >> 2), j = i & 3, cid = h * 4 + j;
    const int kw = (wave + i) & 7;
    GAS unsigned* ctl = (GAS unsigned*)(p.ws + O_CTL);
    const __amdgpu_buffer_rsrc_t rs = __builtin_amdgcn_make_buffer_rsrc(p.ws, 0, 0xFFFFFF00u, 0x00020000);
    const unsigned sidemask = side_mask(ctl, lane);
    v8u WA[6][2], WO[2][2];
    v4u sa[2][12], so[8];
    {
        const u16* wqk = (j < 2 ? p.wq : p.wk) + h * 128 + 64 * (j & 1) + (lane & 7) * 8;
        const u16* wvv = p.wv + h * 128 + 32 * j + (lane & 3) * 8;
#pragma unroll
        for (int ks = 0; ks < 2; ++ks) {
#pragma unroll
            for (int q = 0; q < 8; ++q) sa[ks][q] = *(const v4u*)(wqk + (size_t)(128 * kw + 64 * ks + 8 * q + (lane >> 3)) * 4096);
#pragma unroll
            for (int q = 0; q < 4; ++q) sa[ks][8 + q] = *(const v4u*)(wvv + (size_t)(128 * kw + 64 * ks + 16 * q + (lane >> 2)) * 4096);
        }
#pragma unroll
        for (int q = 0; q < 8; ++q) so[q] = *(const v4u*)(p.wo + (size_t)(h * 128 + 16 * wave + 2 * q + (lane >> 5)) * 1024 + 256 * j + (lane & 31) * 8);
    }
    SB();
    const int tau = lane >> 5, rh = (lane >> 4) & 1, n16 = lane & 15, c8 = (lane >> 3) & 1, g4 = lane >> 4;
    const unsigned o_gm = amap(g * 16384 + kw * 2048 + lane * 16);
    const unsigned qkvb = L_QKV + h * 6144;
    const int pcol = 256 * j + 32 * wave + 16 * tau + n16;
    const unsigned o_pp = amap((pcol >> 3) * 4096 + h * 128 + (rh * 8 + (pcol & 7)) * 8);
    const int sidx = c8 ? 0xEEEE : 0x4444;
    LAS unsigned char* const pw = lds + LC_PA + ((wave * 2 + rh) * 96 + 16 * tau + n16) * 16;
    LAS unsigned char* const ob = lds + LC_O + (lane & 7) * 320 + c8 * 16 + g4 * 32;
    const v4u rd = {(unsigned)(uintptr_t)p.ws, (unsigned)((uintptr_t)p.ws >> 32) & 0xffffu, 0xFFFFFF00u, 0x00020000u};
    const unsigned o_i1 = amap(qkvb + j * 1024 + lane * 16);
    const unsigned o_i2 = lane < 32 ? amap(qkvb + 4096 + j * 512 + lane * 16) : amap(((lane >> 3) & 3) * 16384 + cid * 128 + (lane & 7) * 16);
    {
        const v4u zz = {SENT, SENT, SENT, SENT};
        if (wave < 2) {
            const unsigned sb = P_HAND * PAGE + wave * 524288u, sx = ((sidemask >> P_HAND) & 1u) << 13;
            st16(zz, rs, o_i1 ^ sx, sb);
            v4u iv = zz;
            if (wave == 0 && lane >= 32) {
                iv = *(const v4u*)(p.mem0 + (lane & 7) * 1024 + cid * 8);
                iv = (v4u){nosent(iv.x), nosent(iv.y), nosent(iv.z), nosent(iv.w)};
            }
            st16(iv, rs, o_i2 ^ sx, sb);
        }
#pragma unroll
        for (int sl = 0; sl < 2; ++sl) st8((v2u){SENT, SENT}, rs, o_pp ^ (((sidemask >> (P_PART + sl)) & 1u) << 13), (P_PART + sl) * PAGE);
    }
    bool dead = false;
    const unsigned o_kv = (wave & 2 ? O_VX : O_KX) + (wave & 1) * 4096 + lane * 16, fpar = far_par(sidemask);
    v4u p0, p1, p2, p3;
    {
        LAS unsigned char* img = lds + wave * 12288;
        const int wr = lane >> 2;
        LAS unsigned char* iw = img + ((wr & 9) | ((wr >> 1) & 2) | ((wr << 1) & 4)) * 64 + (((lane & 3) ^ ((wr >> 2) & 2)) * 16);
        const int gB = lane >> 4, q4 = (lane & 15) >> 2, p4 = lane & 3;
        LAS unsigned char* ir = img + (16 * (gB >> 1) + 2 * (gB & 1) + (q4 & 1) + 8 * (q4 >> 1)) * 64 + p4 * 8;
        const unsigned hs = (q4 >> 1) * 32;
        LAS unsigned char* jw = img + (lane >> 3) * 128 + (lane & 1) * 16;
        const int jc = ((lane >> 1) & 3) ^ (lane >> 5);
        LAS unsigned char* jr = img + (16 * (gB >> 1) + 4 * (gB & 1) + (q4 & 1) + 8 * (q4 >> 1)) * 128 + p4 * 8;
        const int js = (gB & 1) | ((q4 >> 1) << 1);
        auto shuffleA = [&](int ks) {
#pragma unroll
            for (int q = 0; q < 8; ++q) *(LAS v4u*)(jw + q * 1024 + ((jc ^ ((q & 1) << 1)) * 32)) = sa[ks][q];
#pragma unroll
            for (int q = 0; q < 4; ++q) *(LAS v4u*)(iw + 8192 + q * 1024) = sa[ks][8 + q];
            asm volatile("s_waitcnt lgkmcnt(0)" ::: "memory");
#pragma unroll
            for (int t = 0; t < 6; ++t)
#pragma unroll
                for (int m = 0; m < 4; ++m) {
                    const v2u r = t < 4 ? trrd(jr + ((t ^ js) * 32) + (32 * (m >> 1) + 2 * (m & 1)) * 128) : trrd(ir + 8192 + (((t & 1) * 32) ^ hs) + (32 * (m >> 1) + 4 * (m & 1)) * 64);
                    WA[t][ks][2 * m] = r.x; WA[t][ks][2 * m + 1] = r.y;
                }
            asm volatile("s_waitcnt lgkmcnt(0)" ::: "memory");
        };
        shuffleA(0);
        SB();
        asm volatile("s_waitcnt vmcnt(0)" ::: "memory");
        BAR();
        if (tid == 0) {
            if (__hip_atomic_fetch_add(ctl + C_ARRIVE, 1u, RLX_AGENT) == 127u) __hip_atomic_store(ctl + C_T0, ((rt() + T0_LEAD) & ~3u) | 0x80000000u, RLX_AGENT);
        }
        SB();
        shuffleA(1);
        SB();
        if (wave >= 4) {
            wait_ge(ctl + C_KV + 8 * h, 4u, ctl, dead);
            const unsigned so = fb(h, fpar); ALD16(p0, o_kv, so, 0); ALD16(p1, o_kv, so, 1024); ALD16(p2, o_kv, so, 2048); ALD16(p3, o_kv, so, 3072);
        }
        BAR();
#pragma unroll
        for (int q = 0; q < 8; ++q) {
            const int f = (lane >> 5) | (((q >> 1) & 3) << 1);
            *(LAS v4u*)(lds + wave * 8192 + q * 1024 + (lane >> 5) * 512 + ((((lane >> 1) & 15) ^ f) * 32) + (lane & 1) * 16) = so[q];
        }
        BAR();
        const int fo = (q4 & 1) | (js << 1);
        LAS unsigned char* orp = lds + (16 * (gB >> 1) + 4 * (gB & 1) + (q4 & 1) + 8 * (q4 >> 1)) * 512 + p4 * 8;
#pragma unroll
        for (int t = 0; t < 2; ++t)
#pragma unroll
            for (int ks = 0; ks < 2; ++ks)
#pragma unroll
                for (int m = 0; m < 4; ++m) {
                    const v2u r = trrd(orp + (((2 * wave + t) ^ fo) * 32) + (64 * ks + 32 * (m >> 1) + 2 * (m & 1)) * 512);
                    WO[t][ks][2 * m] = r.x; WO[t][ks][2 * m + 1] = r.y;
                }
    }
#define HAND_ADDR(s) do { const unsigned pg = P_HAND + ((s) >> 2); sb = pg * PAGE + ((s) & 3) * 524288u; sx = ((sidemask >> pg) & 1u) << 13; } while (0)
#define RETRY(RELOAD, BAD)    do { unsigned spins = 0;                                                                    \
                while (!dead) { if (poll_fail(spins, ctl, 0)) { dead = true; break; } RELOAD; if (!(BAD)) break; } } while (0)
#define ZERO_A() f32x4 c0 = {0, 0, 0, 0}, c1 = c0, c2 = c0, c3 = c0, c4 = c0, c5 = c0; PIN2(c0, c1); PIN2(c2, c3); PIN2(c4, c5)
#define GATHER_MEM(EXTRA, NX) v4u a0, a1;                                                                                 \
            ALD16(a0, o_gmx, sb, 0); ALD16(a1, o_gmx, sb, 1024); EXTRA; WAITV2(NX, a0, a1);                               \
            if (__builtin_expect(any_sent(mx4(a1, mx4(a0, 0u))), 0))                                                      \
                RETRY(ALD16(a0, o_gmx, sb, 0); ALD16(a1, o_gmx, sb, 1024); WAITV2(0, a0, a1), any_sent(mx4(a1, mx4(a0, 0u))))
#define QK_MFMA() do {                                                                                                    \
            c0 = smf(a0, WA[0][0], c0, sidx); c1 = smf(a0, WA[1][0], c1, sidx); c0 = smf(a1, WA[0][1], c0, sidx); c1 = smf(a1, WA[1][1], c1, sidx); \
            SB();                                                                                                         \
            c2 = smf(a0, WA[2][0], c2, sidx); c3 = smf(a0, WA[3][0], c3, sidx); c2 = smf(a1, WA[2][1], c2, sidx); c3 = smf(a1, WA[3][1], c3, sidx); \
            SB(); *(LAS f32x4*)pw = fold(c0, c1); SB(); *(LAS f32x4*)(pw + 512) = fold(c2, c3); SB(); } while (0)
#define V_PART() do { c4 = smf(a0, WA[4][0], c4, sidx); c5 = smf(a0, WA[5][0], c5, sidx); c4 = smf(a1, WA[4][1], c4, sidx); c5 = smf(a1, WA[5][1], c5, sidx); \
            *(LAS f32x4*)(pw + 1024) = fold(c4, c5);                                                                      \
            if (lane == 0) __hip_atomic_fetch_add((LAS unsigned*)(lds + LC_MISC + 4), 1u, __ATOMIC_RELAXED, __HIP_MEMORY_SCOPE_WORKGROUP); } while (0)
#define ZERO_D() f32x4 d0 = {0, 0, 0, 0}, d1 = d0; const unsigned pp = P_PART + (s & 3), px = ((sidemask >> pp) & 1u) << 13; unsigned o_ppx = o_pp ^ px; PIN2(d0, d1); asm volatile("" : "+v"(o_ppx))
#define WO_STAGE() do {                                                                                                   \
            const v4u o0 = *(LAS v4u*)ob, o1 = *(LAS v4u*)(ob + 128); SB();                                               \
            d0 = smf(o0, WO[0][0], d0, sidx); d1 = smf(o0, WO[1][0], d1, sidx); d0 = smf(o1, WO[0][1], d0, sidx); d1 = smf(o1, WO[1][1], d1, sidx); \
            const f32x4 f = fold(d0, d1);                                                                                 \
            st8((v2u){nosent(pk2(f.x, f.y)), nosent(pk2(f.z, f.w))}, rs, o_ppx, pp * PAGE); } while (0)
#define REDUCE4(LP, OFF) do {                                                                                             \
            const f32x4 r0 = *(LAS f32x4*)(LP), r1 = *(LAS f32x4*)((LP) + 3072), r2 = *(LAS f32x4*)((LP) + 6144), r3 = *(LAS f32x4*)((LP) + 9216); \
            const f32x4 t = (r0 + r1) + (r2 + r3);                                                                        \
            st4(nosent(pk2(sw32sum(t.x, t.z), sw32sum(t.y, t.w))), rs, (OFF), sb); } while (0)
    unsigned sb, sx;
    if (wave < 4) {
        const unsigned o_gq = amap(qkvb + wave * 1024 + lane * 16);
        const unsigned o_gp = amap(cid * 4096 + lane * 16);
        const int crow = 4 * ((lane >> 2) & 1) + g4;
        v4u sel = {0u, 0u, 0u, 0u};
        sel[(lane & 7) >> 1] = (lane & 1) ? 0x3f800000u : 0x00003f80u;
        const unsigned o_pm = amap(c8 * 16384 + cid * 128 + crow * 16 + (lane & 3) * 4);
        const unsigned o_gv = amap(qkvb + 4096 + wave * 512 + n16 * 16 + (4 * (g4 & 1) + 2 * (g4 >> 1)) * 2);
        LAS unsigned char* const qb = lds + LC_QK + (4 * g4 + ((lane & 15) >> 2)) * 16 + (lane & 1) * 8;
        LAS unsigned char* const qw = lds + LC_QK + wave * 1024 + lane * 16;
        LAS unsigned char* const ow = lds + LC_O + (lane & 7) * 320 + (32 * wave + 16 * c8 + 4 * g4) * 2;
        if (tid == 0) *(LAS unsigned*)(lds + LC_MISC + 4) = 0u;
        BAR();
        if (wave == 0) {
            unsigned plan, spins = 0;
            for (;;) { plan = __builtin_amdgcn_readfirstlane(__hip_atomic_load(ctl + C_T0, RLX_AGENT)); if (plan) break; if (poll_fail(spins, ctl, 64)) { dead = true; break; } }
            { const unsigned now = rt(); plan = now + ((int)((plan - now) << 1) >> 1); }
            if (!dead) wait_l(plan);
        }
        v4u kx[8], vx[2];
        unsigned tq;
#define A_STAGE_A(S1)                                                                                                     \
            BAR();                                                                                  \
            GATHER_MEM({ LAS unsigned char* kvb = lds + LC_KV + ((S1) & 1) * 16384 + lane * 16;                           \
                _Pragma("unroll") for (int q = 0; q < 8; ++q) kx[q] = *(LAS v4u*)(kvb + q * 1024);                        \
                vx[0] = *(LAS v4u*)(kvb + 8192 + wave * 2048); vx[1] = *(LAS v4u*)(kvb + 8192 + wave * 2048 + 1024); SB(); }, 0); \
            QK_MFMA();                                                                                                    \
            BAR();                                                                                                 \
            tq = mt() + DQ;                                                                                          \
            V_PART()
        {
            HAND_ADDR(0);
            ZERO_A();
            const unsigned o_gmx = o_gm ^ sx;
            A_STAGE_A(0);
        }
        for (int s = 0; s < NSEG - 1; ++s) {
            __builtin_amdgcn_s_setprio(3);
            v4u gq;
            ZERO_D();
            unsigned o_gqx = o_gq ^ sx, o_gvx = o_gv ^ sx; PIN2(o_gqx, o_gvx);
            wait_c(tq);
            ALD16(gq, o_gqx, sb, 0); WAITV1(0, gq);
            *(LAS v4u*)qw = gq;
            SB();
            if (__builtin_expect(any_sent(mx4(gq, 0u)), 0)) { RETRY(ALD16(gq, o_gqx, sb, 0); WAITV1(0, gq), any_sent(mx4(gq, 0u))); *(LAS v4u*)qw = gq; }
            BAR();
            {
                unsigned va, vc;
                ALD4(va, o_gvx, sb, 0); ALD4(vc, o_gvx, sb, 256);
                v4u qf[4], kf[4];
#pragma unroll
                for (int c = 0; c < 4; ++c) { const v2u qa = trrd(qb + c * 512), qc = trrd(qb + c * 512 + 256); qf[c] = (v4u){qa.x, qa.y, qc.x, qc.y}; }
#pragma unroll
                for (int c = 0; c < 4; ++c) { const v2u ka = trrd(qb + c * 512 + 2048), kc = trrd(qb + c * 512 + 2304); kf[c] = (v4u){ka.x, ka.y, kc.x, kc.y}; }
                SB();
                f32x4 s0 = {0, 0, 0, 0}, s1 = s0, sm = s0;
#pragma unroll
                for (int c = 0; c < 4; ++c) { s0 = mfma32(kx[c], qf[c], s0); s1 = mfma32(kx[4 + c], qf[c], s1); sm = mfma32(kf[c], qf[c], sm); }
                SB();
                float x0 = c8 ? s1.x : s0.x, x1 = c8 ? s1.y : s0.y, x2 = c8 ? s1.z : s0.z, x3 = c8 ? s1.w : s0.w;
                float xm = tau ? (c8 ? sm.w : sm.z) : (c8 ? sm.y : sm.x);
                constexpr float CS = SCALE * LOG2E;
                const float ml = -CS * max5x8(x0, x1, x2, x3, xm);
                x0 = __builtin_amdgcn_exp2f(fmaf(x0, CS, ml)); x1 = __builtin_amdgcn_exp2f(fmaf(x1, CS, ml));
                x2 = __builtin_amdgcn_exp2f(fmaf(x2, CS, ml)); x3 = __builtin_amdgcn_exp2f(fmaf(x3, CS, ml));
                xm = __builtin_amdgcn_exp2f(fmaf(xm, CS, ml));
                const unsigned e01 = pk2(x0, x1), e23 = pk2(x2, x3), em = pk2(xm, xm);
                const unsigned f01 = __float_as_uint(ror8(__uint_as_float(e01))), f23 = __float_as_uint(ror8(__uint_as_float(e23)));
                const v4u b1 = {c8 ? f01 : e01, c8 ? f23 : e23, c8 ? e01 : f01, c8 ? e23 : f23};
                unsigned b2 = em & (c8 ? 0xffff0000u : 0x0000ffffu);
                b2 |= __float_as_uint(ror8(__uint_as_float(b2)));
                asm volatile("" : "+v"(b2));
                f32x4 o0 = {0, 0, 0, 0}, o1 = o0;
                o0 = mfma32(vx[0], b1, o0); o1 = mfma32(vx[1], b1, o1);
                SB();
                WAITV2(0, va, vc);
                if (__builtin_expect(any_sent(max(va, vc)), 0)) RETRY(ALD4(va, o_gvx, sb, 0); ALD4(vc, o_gvx, sb, 256); WAITV2(0, va, vc), any_sent(max(va, vc)));
                o0 = mfma16((v2u){va, 0u}, (v2u){b2, 0u}, o0); o1 = mfma16((v2u){vc, 0u}, (v2u){b2, 0u}, o1);
                SB();
                float l = ((x0 + x1) + (x2 + x3)) + xm;
                l += ror8(l); l = sw16sum(l, l); l = sw32sum(l, l);
                const float rinv = __builtin_amdgcn_rcpf(l);
                SB();
                float y[4];
#pragma unroll
                for (int e = 0; e < 4; ++e) y[e] = (c8 ? o1[e] : o0[e]) * rinv;
                *(LAS v2u*)ow = (v2u){pk2(y[0], y[1]), pk2(y[2], y[3])};
            }
            BAR();
            WO_STAGE();
            const unsigned t3 = mt() + D3;
            __builtin_amdgcn_s_setprio(0);
            HAND_ADDR(s + 1);
            ZERO_A();
            unsigned o_gmx = o_gm ^ sx; asm volatile("" : "+v"(o_gmx));
            if (__builtin_expect(wave == 0, 1)) {
                unsigned o_pmx = o_pm ^ sx, o_gpx = o_gp ^ px; PIN2(o_pmx, o_gpx);
                wait_c(t3);
                v4u r0, r1, r2, r3; f32x4 u; unsigned m;
#define C_LOAD() ALD16(r0, o_gpx, pp * PAGE, 0); ALD16(r1, o_gpx, pp * PAGE, 1024); ALD16(r2, o_gpx, pp * PAGE, 2048); ALD16(r3, o_gpx, pp * PAGE, 3072);  \
                WAITV1(3, r0); u = mfma32(sel, r0, (f32x4){0, 0, 0, 0}); m = mx4(r0, 0u); SB(); WAITV1(2, r1); u = mfma32(sel, r1, u); m = mx4(r1, m); SB(); \
                WAITV1(1, r2); u = mfma32(sel, r2, u); m = mx4(r2, m); SB(); WAITV1(0, r3); u = mfma32(sel, r3, u); m = mx4(r3, m)
                C_LOAD();
                if (__builtin_expect(any_sent(m), 0)) RETRY(C_LOAD(), any_sent(m));
                float z0 = tau ? u.z : u.x, z1 = tau ? u.w : u.y;
                z0 += ror8(z0); z1 += ror8(z1);
                { auto sw = __builtin_amdgcn_permlane16_swap(__float_as_uint(z0), __float_as_uint(z1), false, false); z0 = __uint_as_float(sw[0]); z1 = __uint_as_float(sw[1]); }
                const unsigned w2 = nosent(pk2(z0, z1));
                st4(w2, rs, o_pmx, sb); st4(w2, rs, o_pmx + 65536, sb);
                const unsigned plan = mt() + D1;
                wait_c(plan);
            }
            A_STAGE_A(s + 1);
        }
    } else {
        const int hw = wave - 4;
        LAS unsigned char* const l_qk = lds + LC_PA + tau * 12288 + (rh * 96 + 16 * hw + n16) * 16;
        const unsigned o_pqk = amap(qkvb + j * 1024 + (16 * hw + n16) * 16 + rh * 8 + tau * 4);
        LAS unsigned char* const l_v = lds + LC_PA + tau * 12288 + (rh * 96 + 64 + 16 * (hw & 1) + n16) * 16;
        const unsigned o_pv = amap(qkvb + 4096 + j * 512 + (16 * (hw & 1) + n16) * 16 + rh * 8 + tau * 4);
        LAS unsigned char* const l_kv = lds + LC_KV + hw * 4096 + lane * 16;
        WAITV2(0, p0, p1); WAITV2(0, p2, p3);
        BAR();
        *(LAS v4u*)l_kv = p0; *(LAS v4u*)(l_kv + 1024) = p1; *(LAS v4u*)(l_kv + 2048) = p2; *(LAS v4u*)(l_kv + 3072) = p3;
        __builtin_amdgcn_s_setprio(2);
        for (int s = -1; s < NSEG - 1; ++s) {
            if (s >= 0) {
                ZERO_D();
                BAR();
                __builtin_amdgcn_s_sleep(3);
                { const unsigned so = fb((s + 1) * 32 + h, fpar); ALD16(p0, o_kv, so, 0); ALD16(p1, o_kv, so, 1024); ALD16(p2, o_kv, so, 2048); ALD16(p3, o_kv, so, 3072); }
                {
                    const unsigned rp = P_PART + ((s + 2) & 3), rx = ((sidemask >> rp) & 1u) << 13;
                    st8((v2u){SENT, SENT}, rs, o_pp ^ rx, rp * PAGE); st8((v2u){SENT, SENT}, rs, (o_pp - 8 * 16384) ^ rx, rp * PAGE);
                    if (hw >= 2 && s + 2 < NSEG) {
                        const unsigned pg = P_HAND + ((s + 2) >> 2);
                        st16((v4u){SENT, SENT, SENT, SENT}, rs, (hw == 2 ? o_i1 : o_i2) ^ (((sidemask >> pg) & 1u) << 13), pg * PAGE + ((s + 2) & 3) * 524288u);
                    }
                }
                BAR();
                WO_STAGE();
                if (j == 0 && tid == 256) __hip_atomic_store(ctl + C_PROG + 32 * h, (unsigned)(s + 1), RLX_AGENT);
                if (((s + 2) & 3) == 0 && s + 2 < NSEG) wait_ge(ctl + C_KV + 8 * (((s + 2) >> 2) * 32 + h), 2u, ctl, dead);
                WAITV2(0, p0, p1); WAITV2(0, p2, p3);
                LAS unsigned char* kw = l_kv + ((s + 1) & 1) * 16384;
                *(LAS v4u*)kw = p0; *(LAS v4u*)(kw + 1024) = p1; *(LAS v4u*)(kw + 2048) = p2; *(LAS v4u*)(kw + 3072) = p3;
            }
            HAND_ADDR(s + 1);
            ZERO_A();
            unsigned o_pqkx = o_pqk ^ sx, o_gmx = o_gm ^ sx; PIN2(o_pqkx, o_gmx);
            BAR();
            GATHER_MEM(, 0);
            QK_MFMA();
            BAR();
            REDUCE4(l_qk, o_pqkx);
            SB();
            if (s == NSEG - 2 && j == 0 && tid == 256) __hip_atomic_store(ctl + C_PROG + 32 * h, (unsigned)NSEG, RLX_AGENT);
            V_PART();
            if (hw < 2) {
                while (__builtin_amdgcn_readfirstlane(*(volatile LAS unsigned*)(lds + LC_MISC + 4)) < 8u * (unsigned)(s + 2)) { }
                asm volatile("" ::: "memory");
                REDUCE4(l_v, o_pv ^ sx);
            }
        }
    }
}

DEVI int claim_slot(GAS unsigned* ctl) {
    const unsigned x = xcc_id() & 7u;
    const unsigned r = __hip_atomic_fetch_add(ctl + C_XR + 32 * x, 1u, RLX_AGENT);
    if (r < 32 && __hip_atomic_exchange(ctl + C_SLOT + x * 32 + r, 1u, RLX_AGENT) == 0u) return (int)(x * 32 + r);
    for (int k = 255;; k = (k + 255) & 255) if (__hip_atomic_exchange(ctl + C_SLOT + k, 1u, RLX_AGENT) == 0u) return k;
}

DEVI void glds(unsigned voff, const void* sbase, unsigned ldsaddr) {
    unsigned keep;
    asm volatile("s_mov_b32 %0, m0\n\ts_mov_b32 m0, %3\n\ts_nop 2\n\tglobal_load_lds_dwordx4 %1, %2\n\ts_mov_b32 m0, %0" : "=&s"(keep) : "v"(voff), "s"(sbase), "s"(ldsaddr) : "memory");
}
template <int BM, int BN, int WM, bool TR>
struct Gemm {
    static constexpr int WN = 8 / WM, MT = BM / WM / 16, NT = BN / WN / 16, NA = BM / 64, NB = BN / 64, STAGE = (BM + BN) * 128, NS = 147456 / STAGE, LPR = BN / 8;
    f32x4 acc[MT][NT];
    DEVI void run(LAS unsigned char* lds, const u16* A, int lda, const u16* B, int ldb, int nk) {
        const int tid = otid(), lane = tid & 63, wave = __builtin_amdgcn_readfirstlane(tid >> 6);
        const int wm = wave / WN, wn = wave % WN;
        unsigned va[NA], vb[NB];
#pragma unroll
        for (int q = 0; q < NA; ++q) va[q] = ((wave * NA * 8 + q * 8 + (lane >> 3)) * lda + (((lane & 7) ^ ((lane >> 3) & 7)) * 8)) * 2;
#pragma unroll
        for (int q = 0; q < NB; ++q) {
            const int k = (wave * NB + q) * (64 / LPR) + lane / LPR, sl = lane % LPR;
            const int f = (k & 3) | (((k >> 3) & 1) << 2);
            vb[q] = (k * ldb + (((sl >> 1) ^ f) * 16 + (sl & 1) * 8)) * 2;
        }
        const unsigned lbase = (unsigned)(uintptr_t)lds;
        auto piece = [&](int kt, int buf, int q) {
            if (q < NA) glds(va[q], A + kt * 64, lbase + buf * STAGE + (wave * NA + q) * 1024);
            else glds(vb[q - NA], B + (size_t)kt * 64 * ldb, lbase + buf * STAGE + BM * 128 + (wave * NB + q - NA) * 1024);
        };
        const int fr = lane & 15, fg = lane >> 4;
        const unsigned ra = (wm * (BM / WM) + fr) * 128;
        const unsigned ra0 = ra + ((fg ^ (fr & 7)) * 16), ra1 = ra + (((4 + fg) ^ (fr & 7)) * 16);
        const int q4 = fr >> 2, p4 = fr & 3, fsw = q4 | ((fg & 1) << 2);
        const unsigned rb = BM * 128 + (8 * fg + q4) * (BN * 2) + p4 * 8 + (((wn * NT) ^ fsw) * 32);
        v4u a0[MT], b0[NT], a1[MT], b1[NT];
        auto frag = [&](LAS unsigned char* sp, int ks, v4u* af, v4u* bf, int r) {
            if (r < NT) { LAS unsigned char* q = sp + (rb ^ (r * 32)) + ks * (BN * 64); const v2u x = trrd(q), y = trrd(q + BN * 8); bf[r] = (v4u){x.x, x.y, y.x, y.y}; }
            else af[r - NT] = *(LAS v4u*)(sp + (ks ? ra1 : ra0) + (r - NT) * 2048);
        };
        constexpr int NM = MT * NT, NF = MT + NT, NP = NA + NB;
        auto half = [&](const v4u* af, const v4u* bf, LAS unsigned char* np, int nks, v4u* naf, v4u* nbf, bool rd, bool stg, int skt, int sbuf) {
#pragma unroll
            for (int i = 0; i < NM; ++i) {
                const int m = i / NT, n = i % NT;
                acc[m][n] = TR ? mfma32(bf[n], af[m], acc[m][n]) : mfma32(af[m], bf[n], acc[m][n]);
#pragma unroll
                for (int r = i * NF / NM; r < (i + 1) * NF / NM; ++r) if (rd) frag(np, nks, naf, nbf, r);
#pragma unroll
                for (int q = i * NP / NM; q < (i + 1) * NP / NM; ++q) if (stg) piece(skt, sbuf, q);
                SB();
            }
        };
#pragma unroll
        for (int m = 0; m < MT; ++m)
#pragma unroll
            for (int n = 0; n < NT; ++n) acc[m][n] = (f32x4){0, 0, 0, 0};
#pragma unroll
        for (int q = 0; q < NS * NP; ++q) piece(q / NP, q / NP, q % NP);
        asm volatile("s_waitcnt vmcnt(%0)\n\ts_barrier" :: "n"((NS - 1) * NP) : "memory");
#pragma unroll
        for (int r = 0; r < NF; ++r) frag(lds, 0, a0, b0, r);
        int buf = 0;
        for (int kt = 0; kt < nk - NS; ++kt) {
            const int nb = buf == NS - 1 ? 0 : buf + 1;
            half(a0, b0, lds + buf * STAGE, 1, a1, b1, true, false, 0, 0);
            asm volatile("s_waitcnt vmcnt(%0) lgkmcnt(0)\n\ts_barrier" :: "n"((NS - 2) * NP) : "memory");
            half(a1, b1, lds + nb * STAGE, 0, a0, b0, true, true, kt + NS, buf);
            buf = nb;
        }
#pragma unroll
        for (int t = NS; t >= 1; --t) {
            const int nb = buf == NS - 1 ? 0 : buf + 1;
            half(a0, b0, lds + buf * STAGE, 1, a1, b1, true, false, 0, 0);
            asm volatile("s_waitcnt vmcnt(%0) lgkmcnt(0)\n\ts_barrier" :: "n"(t >= 2 ? (t - 2) * NP : 0) : "memory");
            half(a1, b1, lds + nb * STAGE, 0, a0, b0, t > 1, false, 0, 0);
            buf = nb;
        }
    }
};
DEVI v4u pk8(f32x4 a, f32x4 b) { return (v4u){pk2(a.x, a.y), pk2(a.z, a.w), pk2(b.x, b.y), pk2(b.z, b.w)}; }
DEVI void announce(GAS unsigned* w) {
    asm volatile("s_waitcnt vmcnt(0) lgkmcnt(0)\n\ts_barrier" ::: "memory");
    if (otid() == 0) __hip_atomic_fetch_add(w, 1u, RLX_AGENT);
}

template <int BM, int BN>
DEVI void kv_unit(const Params& p, LAS unsigned char* lds, int r0, int hh, int mat, unsigned fpar) {
    constexpr int WM = BM >= 128 ? 4 : 2, WN = 8 / WM, MT = BM / WM / 16, NT = BN / WN / 16;
    const int lane = otid() & 63, wave = __builtin_amdgcn_readfirstlane(otid() >> 6), wm = wave / WN, wn = wave % WN;
    const __amdgpu_buffer_rsrc_t rs = __builtin_amdgcn_make_buffer_rsrc(p.ws, 0, 0xFFFFFF00u, 0x00020000);
    const int row0 = r0 + wm * (BM / WM), hw = hh + wn * NT / 8, n0 = (wn * NT) & 7;
    if (mat == 0) {
        Gemm<BM, BN, WM, true> g; g.run(lds, p.x + (size_t)r0 * D_, D_, p.wk + hh * 128, 4096, 16);
#pragma unroll
        for (int m = 0; m < MT; ++m)
#pragma unroll
            for (int c = 0; c < NT / 2; ++c) {
                const int row = row0 + m * 16;
                st16(pk8(g.acc[m][2 * c], g.acc[m][2 * c + 1]), rs, lane * 16, O_KX + fb((row >> 5) * 32 + hw, fpar) + (((row >> 4) & 1) * 4 + n0 / 2 + c) * 1024);
            }
    } else {
        Gemm<BM, BN, WM, false> g; g.run(lds, p.x + (size_t)r0 * D_, D_, p.wv + hh * 128, 4096, 16);
#pragma unroll
        for (int m = 0; m < MT; m += 2)
#pragma unroll
            for (int n = 0; n < NT; ++n) {
                const int row = row0 + m * 16;
                st16(pk8(g.acc[m][n], g.acc[m + 1][n]), rs, lane * 16, O_VX + fb((row >> 5) * 32 + hw, fpar) + (n0 + n) * 1024);
            }
    }
    asm volatile("s_waitcnt vmcnt(0) lgkmcnt(0)\n\ts_barrier" ::: "memory");
    const int t = otid();
    if (t < (BM == 256 ? 2 : 1) * (BN / 128))
        __hip_atomic_fetch_add((GAS unsigned*)(p.ws + O_CTL) + C_KV + 8 * (((r0 >> 7) + t / (BN / 128)) * 32 + hh + t % (BN / 128)), 1u, RLX_AGENT);
}

DEVI void qa_unit(const Params& p, LAS unsigned char* lds, int panel, int hh, unsigned sidemask, bool& dead) {
    const int lane = otid() & 63, wave = __builtin_amdgcn_readfirstlane(otid() >> 6);
    const int g4 = lane >> 4, n16 = lane & 15;
    const __amdgpu_buffer_rsrc_t rs = __builtin_amdgcn_make_buffer_rsrc(p.ws, 0, 0xFFFFFF00u, 0x00020000);
    GAS unsigned* ctl = (GAS unsigned*)(p.ws + O_CTL);
    v4u qf[2][4];
    {
        Gemm<256, 128, 8, true> g; g.run(lds, p.x + (size_t)panel * 256 * D_, D_, p.wq + hh * 128, 4096, 16);
#pragma unroll
        for (int m = 0; m < 2; ++m)
#pragma unroll
            for (int c = 0; c < 4; ++c) qf[m][c] = pk8(g.acc[m][2 * c], g.acc[m][2 * c + 1]);
    }
    const int seg = panel * 8 + wave;
    const unsigned fpar = far_par(sidemask), kvo = fb(seg * 32 + hh, fpar);
    wait_ge(ctl + C_KV + 8 * ((seg >> 2) * 32 + hh), seg >> 2 ? 2u : 4u, ctl, dead);
    v4u kx[8], vx[8];
#pragma unroll
    for (int q = 0; q < 8; ++q) kx[q] = ld16(rs, lane * 16 + q * 1024, O_KX + kvo);
#pragma unroll
    for (int q = 0; q < 8; ++q) vx[q] = ld16(rs, lane * 16 + q * 1024, O_VX + kvo);
    f32x4 z = {0, 0, 0, 0};
    f32x4 s00 = z, s10 = z, s11 = z, sm0 = z, sm1 = z;
#pragma unroll
    for (int c = 0; c < 4; ++c) { s00 = mfma32(kx[c], qf[0][c], s00); s10 = mfma32(kx[c], qf[1][c], s10); s11 = mfma32(kx[4 + c], qf[1][c], s11); }
    LAS unsigned char* img = lds + wave * 6144;
    wait_ge(ctl + C_PROG + 32 * hh, (unsigned)(seg + 1), ctl, dead);
    {
        const unsigned pg = P_HAND + (seg >> 2), sb = pg * PAGE + (seg & 3) * 524288u, sx = ((sidemask >> pg) & 1u) << 13;
        v4u t[6];
        for (unsigned spins = 0;;) {
            unsigned m = 0;
#pragma unroll
            for (int q = 0; q < 6; ++q) t[q] = ld16(rs, amap(L_QKV + hh * 6144 + q * 1024 + lane * 16) ^ sx, sb);
#pragma unroll
            for (int q = 0; q < 6; ++q) m = mx4(t[q], m);
            if (!any_sent(m) || dead) break;
            if (poll_fail(spins, ctl, 128)) dead = true;
            asm volatile("" ::: "memory");
        }
#pragma unroll
        for (int q = 0; q < 6; ++q) *(LAS v4u*)(img + q * 1024 + lane * 16) = t[q];
        asm volatile("s_waitcnt lgkmcnt(0)" ::: "memory");
    }
    LAS unsigned char* kb = img + 2048 + (4 * g4 + (n16 >> 2)) * 16 + (lane & 1) * 8;
#pragma unroll
    for (int c = 0; c < 4; ++c) {
        const v2u ka = trrd(kb + c * 512), kc = trrd(kb + c * 512 + 256);
        const v4u kf = {ka.x, ka.y, kc.x, kc.y};
        sm0 = mfma32(kf, qf[0][c], sm0); sm1 = mfma32(kf, qf[1][c], sm1);
    }
    v4u b1[2]; v2u b2[2];
#pragma unroll
    for (int m = 0; m < 2; ++m) {
        float v[12];
#pragma unroll
        for (int e = 0; e < 4; ++e) {
            const bool diag = 4 * g4 + e <= n16;
            v[e] = rnd(rnd(m ? s10[e] : s00[e]) * SCALE); if (m == 0 && !diag) v[e] = -INFINITY;
            v[4 + e] = (m && diag) ? rnd(rnd(s11[e]) * SCALE) : -INFINITY;
            v[8 + e] = g4 < 2 ? rnd(rnd(m ? sm1[e] : sm0[e]) * SCALE) : -INFINITY;
        }
        float mx = v[0];
#pragma unroll
        for (int e = 1; e < 12; ++e) mx = fmaxf(mx, v[e]);
        mx = sw16max(mx); mx = sw32max(mx);
        const float ml = -mx * LOG2E; float l = 0.f;
#pragma unroll
        for (int e = 0; e < 12; ++e) { v[e] = __builtin_amdgcn_exp2f(fmaf(v[e], LOG2E, ml)); l += v[e]; }
        l = sw16sum(l, l); l = sw32sum(l, l);
        const float ri = 1.f / l;
        b1[m] = (v4u){pk2(v[0] * ri, v[1] * ri), pk2(v[2] * ri, v[3] * ri), pk2(v[4] * ri, v[5] * ri), pk2(v[6] * ri, v[7] * ri)};
        b2[m] = (v2u){pk2(v[8] * ri, v[9] * ri), pk2(v[10] * ri, v[11] * ri)};
    }
#pragma unroll
    for (int c = 0; c < 4; ++c) {
        f32x4 o[2][2];
#pragma unroll
        for (int t = 0; t < 2; ++t) {
            const v2u va = *(LAS v2u*)(img + 4096 + ((2 * c + t) * 16 + n16) * 16 + (g4 & 1) * 8);
#pragma unroll
            for (int m = 0; m < 2; ++m) { o[m][t] = mfma32(vx[2 * c + t], b1[m], z); o[m][t] = mfma16(va, b2[m], o[m][t]); }
        }
#pragma unroll
        for (int m = 0; m < 2; ++m) st16(pk8(o[m][0], o[m][1]), rs, n16 * 16384 + 16 * g4, O_OX + fb(seg * 32 + m * 16, fpar) + (hh * 128 + c * 32) * 2);
    }
    asm volatile("s_waitcnt vmcnt(0)" ::: "memory");
    if (lane == 0) __hip_atomic_fetch_add(ctl + C_OF + 16 * (seg * 8 + (hh >> 2)), 1u, RLX_AGENT);
}

DEVI void top2(const float* R, int& i0, int& i1) {
    const int lane = otid() & 63;
    float b0 = -INFINITY, b1 = -INFINITY; int j0 = 0x7fffffff, j1 = 0x7fffffff;
    f32x4 rv[8];
#pragma unroll
    for (int q = 0; q < 8; ++q) rv[q] = *(const f32x4*)(R + lane * 32 + q * 4);
#pragma unroll
    for (int q = 0; q < 32; ++q) {
        const float v = rv[q >> 2][q & 3]; const int k = lane * 32 + q;
        const bool f0 = v > b0 || j0 == 0x7fffffff, f1 = v > b1 || j1 == 0x7fffffff;
        b1 = f0 ? b0 : (f1 ? v : b1); j1 = f0 ? j0 : (f1 ? k : j1); b0 = f0 ? v : b0; j0 = f0 ? k : j0;
    }
    for (int m = 1; m < 64; m <<= 1) {
        const float c0 = __shfl_xor(b0, m), c1 = __shfl_xor(b1, m); const int k0 = __shfl_xor(j0, m), k1 = __shfl_xor(j1, m);
        auto gt = [](float a, int ja, float b, int jb) { return a > b || (!(b > a) && ja < jb); };
        if (gt(c0, k0, b0, j0)) { if (gt(b0, j0, c1, k1)) { b1 = b0; j1 = j0; } else { b1 = c1; j1 = k1; } b0 = c0; j0 = k0; }
        else if (gt(c0, k0, b1, j1)) { b1 = c0; j1 = k0; }
    }
    i0 = __builtin_amdgcn_readfirstlane(j0); i1 = __builtin_amdgcn_readfirstlane(j1);
}
DEVI void ffn1_unit(const Params& p, LAS unsigned char* lds, int u, int i0, int i1) {
    const int tid = otid(), co = tid & 7, ksl = tid >> 3, col = u * 64 + co * 8;
    LAS float* red = (LAS float*)lds;
    v4u xa[2], xb[2];
#pragma unroll
    for (int q = 0; q < 2; ++q) { xa[q] = *(const v4u*)(p.x + i0 * D_ + ksl * 16 + q * 8); xb[q] = *(const v4u*)(p.x + i1 * D_ + ksl * 16 + q * 8); }
    float a[4][8] = {};
    v4u wg[16], wu[16];
#pragma unroll
    for (int q = 0; q < 16; ++q) {
        wg[q] = __builtin_nontemporal_load((const v4u*)(p.wg + (size_t)(ksl * 16 + q) * F_ + col));
        wu[q] = __builtin_nontemporal_load((const v4u*)(p.wu + (size_t)(ksl * 16 + q) * F_ + col));
    }
#pragma unroll
    for (int q = 0; q < 16; ++q) {
        const unsigned ua = xa[q >> 3][(q >> 1) & 3], ub = xb[q >> 3][(q >> 1) & 3];
        const float x0 = (q & 1) ? hi16(ua) : lo16(ua), x1 = (q & 1) ? hi16(ub) : lo16(ub);
#pragma unroll
        for (int e = 0; e < 4; ++e) {
            a[0][2 * e] += x0 * lo16(wg[q][e]); a[0][2 * e + 1] += x0 * hi16(wg[q][e]); a[1][2 * e] += x1 * lo16(wg[q][e]); a[1][2 * e + 1] += x1 * hi16(wg[q][e]);
            a[2][2 * e] += x0 * lo16(wu[q][e]); a[2][2 * e + 1] += x0 * hi16(wu[q][e]); a[3][2 * e] += x1 * lo16(wu[q][e]); a[3][2 * e + 1] += x1 * hi16(wu[q][e]);
        }
    }
#pragma unroll
    for (int q = 0; q < 4; ++q)
#pragma unroll
        for (int e = 0; e < 8; ++e) red[(ksl * 4 + q) * 64 + co * 8 + e] = a[q][e];
    BAR();
    if (tid < 128) {
        const int t = tid >> 6, c = tid & 63; float g = 0.f, uu = 0.f;
        for (int k = 0; k < 64; ++k) { g += red[(k * 4 + t) * 64 + c]; uu += red[(k * 4 + 2 + t) * 64 + c]; }
        g = rnd(g); uu = rnd(uu); g = rnd(g / (1.f + __expf(-g)));
        __hip_atomic_store((GAS float*)(p.ws + O_FH) + t * F_ + u * 64 + c, rnd(g * uu), RLX_AGENT);
    }
    announce((GAS unsigned*)(p.ws + O_CTL) + C_F1);
}
DEVI void ffn2_unit(const Params& p, LAS unsigned char* lds, int u, bool& dead) {
    const int tid = otid(), co = tid & 3, ksl = tid >> 2, col = u * 32 + co * 8;
    GAS unsigned* ctl = (GAS unsigned*)(p.ws + O_CTL);
    LAS float* red = (LAS float*)lds;
    v4u w[28];
#pragma unroll
    for (int q = 0; q < 28; ++q) w[q] = __builtin_nontemporal_load((const v4u*)(p.wd + (size_t)(ksl * 28 + q) * D_ + col));
    wait_ge(ctl + C_F1, 56u, ctl, dead);
    const __amdgpu_buffer_rsrc_t rs = __builtin_amdgcn_make_buffer_rsrc(p.ws, 0, 0xFFFFFF00u, 0x00020000);
    v4u h0[7], h1[7];
#pragma unroll
    for (int q = 0; q < 7; ++q) { h0[q] = ld16(rs, ksl * 112 + q * 16, O_FH); h1[q] = ld16(rs, ksl * 112 + q * 16, O_FH + F_ * 4); }
    float a[2][8] = {};
#pragma unroll
    for (int q = 0; q < 28; ++q) {
        const float x0 = __uint_as_float(h0[q >> 2][q & 3]), x1 = __uint_as_float(h1[q >> 2][q & 3]);
#pragma unroll
        for (int e = 0; e < 4; ++e) { a[0][2 * e] += x0 * lo16(w[q][e]); a[0][2 * e + 1] += x0 * hi16(w[q][e]); a[1][2 * e] += x1 * lo16(w[q][e]); a[1][2 * e + 1] += x1 * hi16(w[q][e]); }
    }
#pragma unroll
    for (int q = 0; q < 2; ++q)
#pragma unroll
        for (int e = 0; e < 8; ++e) red[(ksl * 2 + q) * 32 + co * 8 + e] = a[q][e];
    BAR();
    if (tid < 64) {
        const int t = tid >> 5, c = tid & 31; float y = 0.f;
        for (int k = 0; k < 128; ++k) y += red[(k * 2 + t) * 32 + c];
        __hip_atomic_store((GAS float*)(p.ws + O_FY) + t * D_ + u * 32 + c, rnd(y), RLX_AGENT);
    }
    announce(ctl + C_F2);
}

DEVI void wo_fast_unit(const Params& p, LAS unsigned char* lds, int ct, int k8, int i0, int i1, unsigned fpar, bool& dead) {
    const int tid = otid(), lane = tid & 63, wave = __builtin_amdgcn_readfirstlane(tid >> 6), fr = lane & 15, fg = lane >> 4;
    GAS unsigned* ctl = (GAS unsigned*)(p.ws + O_CTL);
    const __amdgpu_buffer_rsrc_t rs = __builtin_amdgcn_make_buffer_rsrc(p.ws, 0, 0xFFFFFF00u, 0x00020000);
    {
        const unsigned lbase = (unsigned)(uintptr_t)lds + wave * 2048;
        const u16* gb = p.wo + (size_t)k8 * 512 * D_ + ct * 128;
#pragma unroll
        for (int q = 0; q < 2; ++q) {
            const int k = wave * 8 + (lane >> 4) + 4 * q;
            const int pk = (k & 32) + 16 * ((k >> 2) & 1) + 4 * ((k >> 3) & 3) + (k & 3), f = (k & 3) | (((k >> 3) & 1) << 2);
            const unsigned vb = (pk * D_ + ((((lane & 15) >> 1) ^ f) * 16 + (lane & 1) * 8)) * 2;
#pragma unroll
            for (int kt = 0; kt < 8; ++kt) glds(vb, gb + (size_t)kt * 64 * D_, lbase + kt * 16384 + q * 1024);
        }
    }
    wait_ge(ctl + C_INIT, 128u, ctl, dead);
    asm volatile("s_waitcnt vmcnt(0)\n\ts_barrier" ::: "memory");
    const int mt = wave & 1, np = wave >> 1;
    v4u bf[16][2];
    {
        const int q4 = fr >> 2, p4 = fr & 3, fsw = q4 | ((fg & 1) << 2);
        const unsigned bpo = (8 * fg + q4) * 256 + p4 * 8 + ((fsw ^ (2 * np)) * 32);
#pragma unroll
        for (int q = 0; q < 16; ++q)
#pragma unroll
            for (int n = 0; n < 2; ++n) { LAS unsigned char* bp = lds + (bpo ^ (n * 32)) + q * 8192; const v2u x = trrd(bp), y = trrd(bp + 1024); bf[q][n] = (v4u){x.x, x.y, y.x, y.y}; }
    }
    BAR();
    const int col = ct * 128 + k8 * 16 + fg * 4;
    v4u t[8]; float gv; v2u xv;
    auto request = [&](int sg) {
        const int row = sg * 32 + mt * 16 + fr;
#pragma unroll
        for (int q = 0; q < 8; ++q) t[q] = ld16(rs, lane * 16 + q * 1024, O_S2 + fb((((sg & 7) * 8 + ct) * 2 + mt) * 8 + k8, fpar));
        gv = ((const float*)(p.ws + O_G))[row];
        xv = *(const v2u*)(p.x + row * D_ + col);
    };
    auto finish = [&](int sg) {
        const int row = sg * 32 + mt * 16 + fr;
        for (unsigned spins = 0;;) {
            v4u an = t[0], orr = t[0];
#pragma unroll
            for (int q = 1; q < 8; ++q) { an &= t[q]; orr |= t[q]; }
            const unsigned bad = (sg & 8) ? ~(an.x & an.y & an.z & an.w) : (orr.x | orr.y | orr.z | orr.w);
            if (__builtin_amdgcn_ballot_w64((bad & 1u) != 0u) == 0 || dead) break;
            if (poll_fail(spins, ctl, 64)) dead = true;
            asm volatile("" ::: "memory");
            request(sg);
        }
        float bb[4] = {lo16(xv.x), hi16(xv.x), lo16(xv.y), hi16(xv.y)};
        if ((i0 >> 5) == sg || (i1 >> 5) == sg) wait_ge(ctl + C_F2, 32u, ctl, dead);
        if (row == i0 || row == i1) {
            const float rs_ = rnd(1.f / (1.f + __expf(-((const float*)(p.ws + O_R))[row])));
#pragma unroll
            for (int e = 0; e < 4; ++e) bb[e] = rnd(bb[e] + rnd(__hip_atomic_load((GAS float*)(p.ws + O_FY) + (row == i0 ? 0 : D_) + col + e, RLX_AGENT) * rs_));
        }
        f32x4 a = __builtin_bit_cast(f32x4, t[0]);
#pragma unroll
        for (int q = 1; q < 8; ++q) a += __builtin_bit_cast(f32x4, t[q]);
        const float og = rnd(1.f - gv);
        float o[4];
#pragma unroll
        for (int e = 0; e < 4; ++e) o[e] = rnd(gv * rnd(a[e])) + rnd(og * bb[e]);
        *(v2u*)(p.out + row * D_ + col) = (v2u){pk2(o[0], o[1]), pk2(o[2], o[3])};
    };
    int ready = 0;
    const v4u rd = {(unsigned)(uintptr_t)p.ws, (unsigned)((uintptr_t)p.ws >> 32) & 0xffffu, 0xFFFFFF00u, 0x00020000u};
    v4u ar0, ar1, ar2, ar3;
    const unsigned va0 = lane * 16, va1 = va0 + 16384, va2 = va0 + 32768, va3 = va0 + 49152;
    auto aload = [&](int sg) {
        const unsigned so = O_OX + fb(sg * 32 + wave * 4, fpar) + k8 * 1024u;
        ALD16(ar0, va0, so, 0); ALD16(ar1, va1, so, 0); ALD16(ar2, va2, so, 0); ALD16(ar3, va3, so, 0);
    };
    LAS unsigned char* const aw = lds + wave * 4096;
    const unsigned sw0 = (lane ^ ((wave * 4) & 15)) * 16;
    for (int seg = 0; seg < NSEG; ++seg) {
        if (seg >= ready) {
            if (wave == ((2 * seg + 4) & 7)) {
                unsigned long long ok;
                for (unsigned spins = 0;;) {
                    const unsigned c = seg + lane < NSEG ? __hip_atomic_load(ctl + C_OF + 16 * ((seg + lane) * 8 + k8), RLX_AGENT) : 0u;
                    ok = __builtin_amdgcn_ballot_w64(c >= 4u);
                    if ((ok & 1ull) || dead) break;
                    if (poll_fail(spins, ctl, 64)) dead = true;
                }
                if (lane == 0) *(LAS int*)(lds + 65536) = seg + max(1, (int)__builtin_ctzll(~ok));
            }
            BAR();
            ready = __builtin_amdgcn_readfirstlane(*(LAS int*)(lds + 65536));
            aload(seg);
            asm volatile("s_waitcnt vmcnt(0)" ::: "memory");
        }
        WAITV2(2, ar0, ar1); WAITV2(2, ar2, ar3);
        LAS unsigned char* img = lds + (seg & 1) * 32768;
        { LAS unsigned char* w_ = aw + (seg & 1) * 32768;
          *(LAS v4u*)(w_ + sw0) = ar0; *(LAS v4u*)(w_ + 1024 + (sw0 ^ 16)) = ar1; *(LAS v4u*)(w_ + 2048 + (sw0 ^ 32)) = ar2; *(LAS v4u*)(w_ + 3072 + (sw0 ^ 48)) = ar3; }
        asm volatile("s_waitcnt lgkmcnt(0)" ::: "memory");
        SB();
        if (seg >= 3 && np == ((seg - 3) & 3)) finish(seg - 3);
        if (seg >= 2 && np == ((seg - 2) & 3)) request(seg - 2);
        if (seg + 1 < ready) aload(seg + 1);
        BAR();
        f32x4 c0 = {0, 0, 0, 0}, c1 = c0;
        {
            v4u af[2][4];
            LAS unsigned char* ap = img + (mt * 16 + fr) * 1024;
#pragma unroll
            for (int q = 0; q < 4; ++q) af[0][q] = *(LAS v4u*)(ap + (((q * 4 + fg) ^ fr) * 16));
#pragma unroll
            for (int g = 0; g < 4; ++g) {
                if (g < 3) {
#pragma unroll
                    for (int q = 0; q < 4; ++q) af[(g + 1) & 1][q] = *(LAS v4u*)(ap + (g + 1) * 256 + (((q * 4 + fg) ^ fr) * 16));
                }
                SB();
#pragma unroll
                for (int q = 0; q < 4; ++q) { c0 = mfma32(bf[g * 4 + q][0], af[g & 1][q], c0); c1 = mfma32(bf[g * 4 + q][1], af[g & 1][q], c1); }
                SB();
            }
        }
        const unsigned sb2 = O_S2 + fb((((seg & 7) * 8 + ct) * 2 + mt) * 8 + 2 * np, fpar) + k8 * 1024u;
        const unsigned tg = (seg >> 3) & 1u;
        st16((__builtin_bit_cast(v4u, c0) & ~1u) | tg, rs, lane * 16, sb2);
        st16((__builtin_bit_cast(v4u, c1) & ~1u) | tg, rs, lane * 16, sb2 + 16384u);
    }
    if (np == 1) finish(NSEG - 3);
    if (np >= 2) { if (np == 3) __builtin_amdgcn_s_sleep(5); request(NSEG - 4 + np); finish(NSEG - 4 + np); }
}

constexpr int Q_F1 = 0, Q_F2 = Q_F1 + 14, Q_QA = Q_F2 + 8, Q_K2 = Q_QA + 16, Q_WF = Q_K2 + 16, Q_Q2 = Q_WF + 16, Q_END = Q_Q2 + 48;
DEVI void worker_body(const Params& p, LAS unsigned char* lds, int w) {
    const int tid = otid(), lane = tid & 63;
    GAS unsigned* ctl = (GAS unsigned*)(p.ws + O_CTL);
    const unsigned sidemask = side_mask(ctl, lane);
    bool dead = false;
    const unsigned fpar = far_par(sidemask);
    const int xq = w >> 5, hm = xq * 16 + ((w & 31) >> 1);
    kv_unit<64, 128>(p, lds, (w & 1) * 64, hm >> 1, hm & 1, fpar);
    kv_unit<128, 128>(p, lds, 128 + (w & 1) * 128, hm >> 1, hm & 1, fpar);
    kv_unit<256, 128>(p, lds, 384 + (w & 1) * 256, hm >> 1, hm & 1, fpar);
    { const int hp = xq * 8 + ((w & 31) >> 2); kv_unit<256, 256>(p, lds, 896 + (w & 3) * 256, (hp >> 1) * 2, hp & 1, fpar); }
    {
        const __amdgpu_buffer_rsrc_t rs = __builtin_amdgcn_make_buffer_rsrc(p.ws, 0, 0xFFFFFF00u, 0x00020000);
        const v4u zz = {SENT, SENT, SENT, SENT};
#pragma unroll
        for (int q = 0; q < 8; ++q) st16(zz, rs, otid() * 16, O_S2 + fb(w * 8 + q, fpar));
        announce(ctl + C_INIT);
    }
    int i0, i1; top2((const float*)(p.ws + O_R), i0, i1);
    for (;;) {
        BAR();
        if (tid == 0) *(LAS unsigned*)(lds + 147456) = __hip_atomic_fetch_add(ctl + C_QUEUE + 16 * xq, 1u, RLX_AGENT);
        BAR();
        int t = __builtin_amdgcn_readfirstlane(*(LAS unsigned*)(lds + 147456));
        if (t >= Q_END) break;
        if (t < Q_F2) ffn1_unit(p, lds, xq * 14 + t - Q_F1, i0, i1);
        else if (t < Q_QA) ffn2_unit(p, lds, xq * 8 + t - Q_F2, dead);
        else if (t >= Q_K2 && t < Q_WF) kv_unit<128, 128>(p, lds, 1920, xq * 8 + ((t - Q_K2) >> 1), t & 1, fpar);
        else if (t >= Q_WF && t < Q_Q2) wo_fast_unit(p, lds, (t - Q_WF) >> 1, 2 * xq + ((t - Q_WF) & 1), i0, i1, fpar, dead);
        else { const int qa = t < Q_K2 ? t - Q_QA : t - Q_Q2 + 16; qa_unit(p, lds, qa >> 3, xq * 8 + (qa & 7), sidemask, dead); }
    }
}

__global__ void __launch_bounds__(512, 2) k_main(Params p) {
    extern __shared__ __attribute__((aligned(16))) unsigned char smem[];
    LAS unsigned char* lds = (LAS unsigned char*)smem;
    if (threadIdx.x == 0) *(LAS int*)(lds + 147456) = claim_slot((GAS unsigned*)(p.ws + O_CTL));
    __syncthreads();
    const int slot = __builtin_amdgcn_readfirstlane(*(LAS int*)(lds + 147456));
    if (slot < 128) chain_body(p, lds, slot >> 5, slot & 31);
    else worker_body(p, lds, slot - 128);
}

extern "C" void kernel_launch(void* const* d_in, const int* in_sizes, int n_in, void* d_out, int out_size, void* d_ws, size_t ws_size, hipStream_t stream) {
    Params p;
    p.x = (const u16*)d_in[0]; p.mem0 = (const u16*)d_in[1]; p.wq = (const u16*)d_in[2]; p.wk = (const u16*)d_in[3]; p.wv = (const u16*)d_in[4]; p.wo = (const u16*)d_in[5];
    p.wr = (const u16*)d_in[8]; p.wg = (const u16*)d_in[9]; p.wu = (const u16*)d_in[10]; p.wd = (const u16*)d_in[11]; p.gw = (const u16*)d_in[12];
    p.out = (u16*)d_out;
    p.ws = (unsigned char*)(((uintptr_t)d_ws + PAGE - 1) & ~(uintptr_t)(PAGE - 1));
    constexpr int LDS_M = 147456 + 64;
    (void)hipFuncSetAttribute((const void*)k_main, hipFuncAttributeMaxDynamicSharedMemorySize, LDS_M);
    hipLaunchKernelGGL(k_prep, dim3(256), dim3(320), 0, stream, p);
    hipLaunchKernelGGL(k_main, dim3(256), dim3(512), LDS_M, stream, p);
}
```
